```python
import torch

def _softmax(x, axis=-1):
    return torch.softmax(x, dim=axis)
def _gelu(x):
    return torch.nn.functional.gelu(x, approximate="tanh")
def _silu(x):
    return torch.nn.functional.silu(x)
def _sigmoid(x):
    return torch.sigmoid(x)
def _layernorm(x, axis=-1, eps=1e-5):
    m = x.mean(dim=axis, keepdim=True)
    v = x.var(dim=axis, keepdim=True, unbiased=False)
    return (x - m) / torch.sqrt(v + eps)
def _scatter_add(tgt, idx, src, axis=0):
    return tgt.clone().index_add_(axis, idx, src)
def _segment_sum(src, seg, n):
    out = torch.zeros((n,) + src.shape[1:], dtype=src.dtype)
    return out.index_add_(0, seg, src)
def _pad(x, pw):
    flat = tuple(v for pair in reversed(pw) for v in pair)
    return torch.nn.functional.pad(x, flat)

kernel_name = 'sequential_preLN__linear_attn_block__moe_arch_mod__prompt'

def setup_inputs():

    _w = (lambda a: (lambda t: t.to(torch.bfloat16) if t.is_floating_point() else t)(torch.as_tensor(a)))
    def _A_setup():
        B, S, D = 1, 2048, 1024
        return {
            "x": _w((lambda *s: torch.randn(*s, dtype=torch.bfloat16) * 0.05)(B, S, D)),
            "wqkv": _w((lambda *s: torch.randn(*s, dtype=torch.bfloat16) * 0.05)(D, 3 * D)),
            "wo": _w((lambda *s: torch.randn(*s, dtype=torch.bfloat16) * 0.05)(D, D)),
        }
    _A_in = _A_setup()

    def _B_setup():
        B, S, D, F, C = 1, 2048, 1024, 3584, 2
        return {
            "x": _w((lambda *s: torch.randn(*s, dtype=torch.bfloat16) * 0.05)(B, S, D)),
            "w_r": _w((lambda *s: torch.randn(*s, dtype=torch.bfloat16) * 0.05)(D)),
            "w_gate": _w((lambda *s: torch.randn(*s, dtype=torch.bfloat16) * 0.05)(D, F)),
            "w_up": _w((lambda *s: torch.randn(*s, dtype=torch.bfloat16) * 0.05)(D, F)),
            "w_down": _w((lambda *s: torch.randn(*s, dtype=torch.bfloat16) * 0.05)(F, D)),
        }
    _B_in = _B_setup()

    return {
        "A_x": _A_in["x"],
        "A_wqkv": _A_in["wqkv"],
        "A_wo": _A_in["wo"],
        "B_x": _B_in["x"],
        "B_w_r": _B_in["w_r"],
        "B_w_gate": _B_in["w_gate"],
        "B_w_up": _B_in["w_up"],
        "B_w_down": _B_in["w_down"],
    }

def reference(A_x, A_wqkv, A_wo, B_x, B_w_r, B_w_gate, B_w_up, B_w_down):
    def _A_ref(x, wqkv, wo):
        B, S, D = x.shape
        H = 32; Dh = D // H
        qkv = (lambda x, *d: x.permute(*d))((x @ wqkv).reshape(B, S, 3, H, Dh), 2, 0, 3, 1, 4)
        q, k, v = qkv[0], qkv[1], qkv[2]
        phi = lambda t: torch.where(t > 0, t + 1, torch.exp(t)) + 1e-6
        pq, pk = phi(q), phi(k)
        kv = torch.einsum("bhsd,bhse->bhde", pk, v)
        z = torch.einsum("bhsd->bhd", pk)
        num = torch.einsum("bhqd,bhde->bhqe", pq, kv)
        den = torch.einsum("bhqd,bhd->bhq", pq, z)[..., None] + 1e-6
        o = num / den
        return (lambda x, *d: x.permute(*d))(o, 0, 2, 1, 3).reshape(B, S, D) @ wo

    def _B_ref(x, w_r, w_gate, w_up, w_down):
        B, S, D = x.shape
        C = 2
        r = x @ w_r
        idx = torch.argsort(-r, axis=-1)[:, :C]
        idx_e = (lambda x, n, axis: torch.repeat_interleave(x, n, dim=axis))(idx.reshape(B, C, 1), D, axis=2)
        x_sel = (lambda x, i, axis: torch.take_along_dim(x, i, dim=axis))(x, idx_e, axis=1)
        g = _silu(x_sel @ w_gate)
        u = x_sel @ w_up
        y = (g * u) @ w_down
        r_sel = _sigmoid((lambda x, i, axis: torch.take_along_dim(x, i, dim=axis))(r, idx, axis=1))
        y = y * r_sel.reshape(B, C, 1)
        b_off = (torch.arange(B) * S).reshape(B, 1)
        flat_idx = (idx + b_off).reshape(B * C)
        zero = (x * 0.0).reshape(B * S, D)
        upd = _scatter_add(zero, flat_idx, y.reshape(B * C, D), axis=0)
        return x + upd.reshape(B, S, D)

    x = A_x
    h = x + _A_ref(_layernorm(x, axis=-1), A_wqkv, A_wo)
    h2 = h + _B_ref(_layernorm(h, axis=-1), B_w_r, B_w_gate, B_w_up, B_w_down)
    return h2
```

```cpp
#define PROBE 999
#include <hip/hip_runtime.h>
#include <stdint.h>

#define LAS __attribute__((address_space(3)))
#define GAS __attribute__((address_space(1)))
#define DI __device__ __forceinline__

constexpr int KROT = 2;
constexpr int XSR = 4;
constexpr int XSL1 = 6;
constexpr int YSL0 = 64, YSL1 = 4;
typedef unsigned short u16;
typedef unsigned long long u64;
typedef __attribute__((ext_vector_type(2))) __bf16 bf16x2;
typedef __attribute__((ext_vector_type(8))) __bf16 bf16x8;
typedef __attribute__((ext_vector_type(4))) short s16x4;
typedef __attribute__((ext_vector_type(2))) unsigned short u16x2;
typedef __attribute__((ext_vector_type(16))) float f32x16;
typedef __attribute__((ext_vector_type(4))) float f32x4;
typedef __attribute__((ext_vector_type(2))) float f32x2;
typedef __attribute__((ext_vector_type(4))) unsigned u32x4;
typedef __attribute__((ext_vector_type(2))) unsigned u32x2;
typedef GAS unsigned gu32;
typedef GAS unsigned long long gu64;

constexpr int S_ = 2048, D_ = 1024, F_ = 3584;

constexpr unsigned SY_CH = 0, SY_GO = 1024, SY_RE = 2048, SY_FSR = 4096, SY_FSC = 8192, SY_FA = 12288, SY_FB = 16384,
                   SY_FC = 20480, SY_FD = 28672, SY_FE = 32768;
constexpr unsigned WS_RS  = 65536;
constexpr unsigned WS_CS  = WS_RS + 16 * 128 * 128;
constexpr unsigned WS_KVP = WS_CS + 16 * 192 * 64;
constexpr unsigned WS_O   = WS_KVP + 256 * 8448;
constexpr unsigned WS_ST2 = WS_O + S_ * D_ * 2;
constexpr unsigned WS_YP  = WS_ST2 + 32 * 8 * 64 * 16;
constexpr unsigned WS_GU  = WS_YP + 224 * 8192;
constexpr unsigned WS_XS  = WS_GU + 256 * 1024;
constexpr unsigned WS_END = WS_XS + 4 * 32 * 16384;
constexpr size_t WS_DIAG  = 64u << 20;
#define STAMPT(kid, i, T) do { if ((kid) * 16 + (i) == PROBE) pt_ = __builtin_amdgcn_s_memrealtime(); } while (0)

DI float bflo(unsigned u) { return __uint_as_float(u << 16); }
DI float bfhi(unsigned u) { return __uint_as_float(u & 0xffff0000u); }
DI float rbf(float x) { return (float)(__bf16)x; }
DI unsigned pack2(float a, float b) { bf16x2 c; c[0] = (__bf16)a; c[1] = (__bf16)b; return __builtin_bit_cast(unsigned, c); }
DI f32x16 mfma32(u32x4 a, u32x4 b, f32x16 c) {
  return __builtin_amdgcn_mfma_f32_32x32x16_bf16(__builtin_bit_cast(bf16x8, a), __builtin_bit_cast(bf16x8, b), c, 0, 0, 0);
}
DI u32x2 trread(const char* p) {
  s16x4 v = __builtin_amdgcn_ds_read_tr16_b64_v4i16((LAS s16x4*)p);
  return __builtin_bit_cast(u32x2, v);
}
DI const char* uni(const void* p) {
  const u64 v = (u64)p;
  return (const char*)(((u64)(unsigned)__builtin_amdgcn_readfirstlane((int)(v >> 32)) << 32) | (unsigned)__builtin_amdgcn_readfirstlane((int)v));
}
#define RSV "a119", "a120", "a121", "a122", "a123", "a124", "a125", "a126", "a127"
DI void glds16(unsigned voff, const void* sbase, unsigned lds) {
  asm volatile("s_mov_b32 m0, %2\n\ts_nop 0\n\tglobal_load_lds_dwordx4 %0, %1" ::"v"(voff), "s"(uni(sbase)), "s"(__builtin_amdgcn_readfirstlane(lds)) : "memory");
}
DI void glds16r(unsigned voff, const void* sbase, unsigned lds) {
  asm volatile("s_mov_b32 m0, %2\n\ts_nop 0\n\tglobal_load_lds_dwordx4 %0, %1" ::"v"(voff), "s"(uni(sbase)), "s"(__builtin_amdgcn_readfirstlane(lds)) : "memory", RSV);
}
DI void glds16_sc1(unsigned voff, const void* sbase, unsigned lds) {
  asm volatile("s_mov_b32 m0, %2\n\ts_nop 0\n\tglobal_load_lds_dwordx4 %0, %1 sc1" ::"v"(voff), "s"(uni(sbase)), "s"(__builtin_amdgcn_readfirstlane(lds)) : "memory");
}
DI float xh_sum(float v) {
  auto r = __builtin_amdgcn_permlane32_swap(__float_as_uint(v), __float_as_uint(v), false, false);
  return __uint_as_float(r[0]) + __uint_as_float(r[1]);
}
template <int CTRL> DI unsigned dppu(unsigned v) { return (unsigned)__builtin_amdgcn_update_dpp(0, (int)v, CTRL, 0xf, 0xf, true); }
template <int CTRL> DI float dppf(float v) { return __uint_as_float(dppu<CTRL>(__float_as_uint(v))); }
constexpr int DPP_X1 = 0xB1, DPP_X2 = 0x4E, DPP_HMIR = 0x141, DPP_MIR = 0x140;
DI float quad_sum(float v) { v += dppf<DPP_X1>(v); v += dppf<DPP_X2>(v); return v; }
DI float x16_sum(float v) {
  auto r = __builtin_amdgcn_permlane16_swap(__float_as_uint(v), __float_as_uint(v), false, false);
  return __uint_as_float(r[0]) + __uint_as_float(r[1]);
}
DI float wave_sum(float v) {
  v = quad_sum(v); v += dppf<DPP_HMIR>(v); v += dppf<DPP_MIR>(v);
  return xh_sum(x16_sum(v));
}
DI unsigned phi_pair(f32x2 t, u16x2& tmax) {
  const unsigned tb = pack2(t[0], t[1]);
  tmax = __builtin_elementwise_max(tmax, __builtin_bit_cast(u16x2, tb));
  const f32x2 tr = {bflo(tb), bfhi(tb)};
  const f32x2 u = tr + 1.0f;
  const f32x2 l = tr * 1.44269504088896340736f;
  const float e0 = __builtin_amdgcn_exp2f(l[0]), e1 = __builtin_amdgcn_exp2f(l[1]);
  return pack2(tr[0] > 0.f ? u[0] : e0, tr[1] > 0.f ? u[1] : e1);
}
#define RLX_AGENT __ATOMIC_RELAXED, __HIP_MEMORY_SCOPE_AGENT
DI void st32(char* ws, unsigned off, unsigned v) { __hip_atomic_store((gu32*)(ws + off), v, RLX_AGENT); }
DI void st64(char* ws, unsigned off, u64 v) { __hip_atomic_store((gu64*)(ws + off), v, RLX_AGENT); }
DI unsigned ld32(char* ws, unsigned off) { return __hip_atomic_load((gu32*)(ws + off), RLX_AGENT); }
DI u64 ld64(char* ws, unsigned off) { return __hip_atomic_load((gu64*)(ws + off), RLX_AGENT); }
#define WAIT_V(n) asm volatile("s_waitcnt vmcnt(" #n ")" ::: "memory")
#define WAIT_L0() asm volatile("s_waitcnt lgkmcnt(0)" ::: "memory")
#define RAW_BAR() do { WAIT_L0(); __builtin_amdgcn_s_barrier(); asm volatile("" ::: "memory"); } while (0)
#define SB0() __builtin_amdgcn_sched_barrier(0)
#define WAIT_VR(n) asm volatile("s_waitcnt vmcnt(" #n ")" ::: "memory", RSV)
#define RAW_BARR() do { asm volatile("s_waitcnt lgkmcnt(0)" ::: "memory", RSV); __builtin_amdgcn_s_barrier(); asm volatile("" ::: "memory", RSV); } while (0)
#define SBR() do { asm volatile("" ::: RSV); __builtin_amdgcn_sched_barrier(0); } while (0)
constexpr unsigned SPIN_CAP = 1u << 22;
DI void poll_flags(char* ws, unsigned off, bool act) {
  for (unsigned s = 0; s < SPIN_CAP; ++s) {
    const unsigned v = act ? ld32(ws, off) : 1u;
    if (__all(v != 0u)) break;
  }
}
DI unsigned mk_key(float v, unsigned idx) {
  const unsigned u = __float_as_uint(v) >> 16;
  const unsigned o = (u & 0x8000u) ? (~u & 0xffffu) : (u | 0x8000u);
  return (o << 16) | (0xFFFFu - idx);
}
DI unsigned key_idx(unsigned k) { return 0xFFFFu - (k & 0xFFFFu); }
DI void top2_mix(unsigned& b0, unsigned& b1, unsigned c0, unsigned c1) {
  const unsigned lo = min(b0, c0), mx = max(b1, c1);
  b0 = max(b0, c0); b1 = max(lo, mx);
}
DI void top2_wave(unsigned& b0, unsigned& b1) {
  top2_mix(b0, b1, dppu<DPP_X1>(b0), dppu<DPP_X1>(b1));
  top2_mix(b0, b1, dppu<DPP_X2>(b0), dppu<DPP_X2>(b1));
  top2_mix(b0, b1, dppu<DPP_HMIR>(b0), dppu<DPP_HMIR>(b1));
  top2_mix(b0, b1, dppu<DPP_MIR>(b0), dppu<DPP_MIR>(b1));
  { auto p = __builtin_amdgcn_permlane16_swap(b0, b0, false, false); auto q = __builtin_amdgcn_permlane16_swap(b1, b1, false, false);
    b0 = p[0]; b1 = q[0]; top2_mix(b0, b1, p[1], q[1]); }
  { auto p = __builtin_amdgcn_permlane32_swap(b0, b0, false, false); auto q = __builtin_amdgcn_permlane32_swap(b1, b1, false, false);
    b0 = p[0]; b1 = q[0]; top2_mix(b0, b1, p[1], q[1]); }
}

constexpr int K1_STAGE = 40960;
constexpr int LDS_TOTAL = 4 * K1_STAGE;
constexpr int K2_STAGE = 24576;
constexpr int L_KVL = 147456, L_ZL = L_KVL + 4096, L_MISC = L_ZL + 256;

__global__ __launch_bounds__(512, 2) void fused(const u16* __restrict__ x, const u16* __restrict__ wqkv,
                                                const u16* __restrict__ wo, const u16* __restrict__ w_r,
                                                const u16* __restrict__ wg, const u16* __restrict__ wu,
                                                const u16* __restrict__ wd, u16* __restrict__ out, char* __restrict__ ws) {
  extern __shared__ __attribute__((aligned(16))) char smem[];
  u64 pt_ = 0;
  STAMPT(0, 0, 0);
  { float dz = 0.f; asm volatile("" : "+a"(dz)); }
  int tid = threadIdx.x, lane = tid & 63;
  const int wave = __builtin_amdgcn_readfirstlane(tid >> 6);
  const int g = wave >> 2, wr = (wave >> 1) & 1, wc = wave & 1;
  const int bid = blockIdx.x, xcd = bid & 7, idx = bid >> 3;
  const int rb = (xcd & 1) * 8 + (idx & 7);
  const int hp = (xcd >> 1) * 4 + (idx >> 3);
  const int rp = xcd * 4 + (idx >> 3), cb = idx & 7;
  int r = lane & 31, h = lane >> 5;
  const unsigned lds0 = (unsigned)(size_t)smem;
  const __amdgpu_buffer_rsrc_t wsr = __builtin_amdgcn_make_buffer_rsrc(ws, 0, WS_END, 0x00020000);
#define LDW(off) __builtin_amdgcn_raw_buffer_load_b128(wsr, (off), 0, 16)
#define STW(v, off) __builtin_amdgcn_raw_buffer_store_b128((v), wsr, (off), 0, 16)

  unsigned hs_cur = 0, hs_g0 = 0; int hs_state = 0;
  u32x4 ms_exp = {0, 0, 0, 0}; bool ms_done = false;
  const unsigned hs_off = (lane == 1 ? SY_GO : SY_CH) + bid * 4;
  const unsigned ms_off = lane * 16;
  const char* wsu = uni(ws);
#define HS_PROBE() asm volatile("global_load_dword a119, %0, %1 sc1" :: "v"(hs_off), "s"(wsu) : "memory", RSV)
#define MS_PROBE() asm volatile("global_load_dwordx4 a[124:127], %0, %1 offset:2048 sc1" :: "v"(ms_off), "s"(wsu) : "memory", RSV)
  const bool is_master = (bid == 0) && (wave == 1);
  auto hs_begin = [&]() {
    unsigned zo = SY_FA + hp * 256 + rb * 4;
    if (lane == 1) zo = SY_FB + rb * 256 + hp * 4;
    if (lane == 2) zo = SY_FC + rp * 256 + cb * 4;
    if (lane == 3) zo = SY_FE + bid * 4;
    if (lane >= 4) zo = SY_FD + cb * 512 + rp * 8 + (lane & 1) * 4;
    if (lane < 6) st32(ws, zo, 0u);
    STW(((u32x4){0u, 0u, 0u, 0u}), WS_ST2 + (rp * 8 + cb) * 1024 + lane * 16);
  };
  if (is_master) {
    MS_PROBE();
    asm volatile("global_load_dwordx4 a[120:123], %0, %1 offset:1024 sc1" :: "v"(ms_off), "s"(wsu) : "memory", RSV);
  }
  auto hs_step = [&](bool first, bool mfirst) {
    if (wave == 0 && !mfirst) {
      unsigned hv_;
      asm volatile("v_accvgpr_read_b32 %0, a119" : "=v"(hv_) :: RSV);
      const unsigned c = __builtin_amdgcn_readlane(hv_, 0), go = __builtin_amdgcn_readlane(hv_, 1);
      if (first) {
        hs_cur = c;
        if (lane == 0) st32(ws, SY_RE + bid * 4, c);
      } else if (hs_state == 0) {
        if (go == hs_cur && go != hs_g0) hs_state = 2;
        else if (c != hs_cur) { hs_cur = c; if (lane == 0) st32(ws, SY_RE + bid * 4, c); }
      }
      if (hs_state < 2) HS_PROBE();
    }
    if (is_master && !ms_done) {
      u32x4 re;
      asm volatile("v_accvgpr_read_b32 %0, a124\n\tv_accvgpr_read_b32 %1, a125\n\tv_accvgpr_read_b32 %2, a126\n\tv_accvgpr_read_b32 %3, a127"
                   : "=v"(re[0]), "=v"(re[1]), "=v"(re[2]), "=v"(re[3]) :: RSV);
      if (mfirst) {
        u32x4 g0;
        asm volatile("v_accvgpr_read_b32 %0, a120\n\tv_accvgpr_read_b32 %1, a121\n\tv_accvgpr_read_b32 %2, a122\n\tv_accvgpr_read_b32 %3, a123"
                     : "=v"(g0[0]), "=v"(g0[1]), "=v"(g0[2]), "=v"(g0[3]) :: RSV);
#pragma unroll
        for (int j = 0; j < 4; ++j) { const unsigned c = re[j] + 1u; ms_exp[j] = (c == g0[j]) ? c + 1u : c; }
        STW(ms_exp, SY_CH + ms_off);
      } else if (__all(re[0] == ms_exp[0] && re[1] == ms_exp[1] && re[2] == ms_exp[2] && re[3] == ms_exp[3])) {
        STW(ms_exp, SY_GO + ms_off);
        ms_done = true;
      }
      if (!ms_done) MS_PROBE();
    }
  };

  auto hs_pre = [&]() {
    if (wave == 0) {
      unsigned hv_;
      asm volatile("v_accvgpr_read_b32 %0, a119" : "=v"(hv_) :: RSV);
      hs_g0 = __builtin_amdgcn_readlane(hv_, 1);
      HS_PROBE();
    }
  };
  auto stat_dma = [&]() {
    int t2 = threadIdx.x; asm volatile("" : "+v"(t2));
    const unsigned vo = (t2 & 63) * 16;
#pragma unroll
    for (int i = 0; i < 2; ++i)
      asm volatile("s_mov_b32 m0, %2\n\ts_nop 0\n\tglobal_load_lds_dwordx4 %0, %1 sc1" ::"v"(vo), "s"(uni(ws + WS_RS + rb * 16384 + (wave * 2 + i) * 1024)),
                   "s"(__builtin_amdgcn_readfirstlane(lds0 + 98304 + (wave * 2 + i) * 1024)) : "memory", RSV);
#pragma unroll
    for (int i = 0; i < 2; ++i)
      if (wave * 2 + i < 12)
        asm volatile("s_mov_b32 m0, %2\n\ts_nop 0\n\tglobal_load_lds_dwordx4 %0, %1 sc1" ::"v"(vo), "s"(uni(ws + WS_CS + hp * 12288 + (wave * 2 + i) * 1024)),
                     "s"(__builtin_amdgcn_readfirstlane(lds0 + 114688 + (wave * 2 + i) * 1024)) : "memory", RSV);
  };
  const int sigma = (rb + hp) & 15;
  const int krot = KROT * xcd, sg = (sigma - krot) & 15;
  auto korder = [&](int t) { return ((t < 2 ? (t < sg ? t : t + 1) : t == 2 ? sg : (t - 1 < sg ? t - 1 : t)) + krot) & 15; };
#define RS_SLOT(T) (WS_RS + ((rb * 16 + sigma) * 128 + ((T) >> 2)) * 8)
#define CS_SLOT(T) (WS_CS + ((hp * 16 + sigma) * 192 + wave * 32 + ((T) & 31)) * 4)

  unsigned voA0, voA1, voB;
  {
    const int r0 = 16 * wave + (lane >> 3), r1 = r0 + 8;
    const int c0 = (lane & 7) ^ ((r0 >> 1) & 7), c1 = (lane & 7) ^ ((r1 >> 1) & 7);
    voA0 = (unsigned)(r0 * D_ + c0 * 8) * 2u;
    voA1 = (unsigned)(r1 * D_ + c1 * 8) * 2u;
    const int bi = lane >> 4, q = (lane >> 2) & 3, ch = lane & 3;
    voB = (unsigned)((8 * wave + 4 * (bi >> 1) + q) * (3 * D_) + (bi & 1) * 32 + ch * 8) * 2u;
  }
  const char* baseA = uni(x + (size_t)rb * 128 * D_);
  const char* baseB = uni(wqkv + hp * 64);
  const int ft = wc ^ g;
  int trl = ((lane & 15) >> 2) * 64 + ((lane >> 4) & 1) * 32 + (lane & 3) * 8;
  f32x16 acc[2][3];
  {
    auto stage = [&](int t, int sb) {
      const int kt = korder(t);
      const unsigned l = lds0 + sb;
      const char* pa = baseA + kt * 128;
      const char* pb = baseB + (size_t)kt * (64 * 3 * D_ * 2);
      glds16r(voA0, pa, l + (2 * wave) * 1024);
      glds16r(voA1, pa, l + (2 * wave + 1) * 1024);
      glds16r(voB, pb, l + 16384 + (3 * wave) * 1024);
      glds16r(voB + 2048, pb, l + 16384 + (3 * wave + 1) * 1024);
      glds16r(voB + 4096, pb, l + 16384 + (3 * wave + 2) * 1024);
    };
    const int ka = 4 * g + h, kb = 4 * g + 2 + h;
    const int aF0 = (64 * wr + 32 * ft + r) * 128 + ((ka ^ ((r >> 1) & 7)) * 16);
    const int aF1 = (64 * wr + 32 * ft + r) * 128 + ((kb ^ ((r >> 1) & 7)) * 16);
    const int aS0 = aF0 ^ 4096, aS1 = aF1 ^ 4096;
    const int bo0 = 16384 + wc * 256 + trl + ka * 3072, bo1 = 16384 + wc * 256 + trl + kb * 3072;
#pragma unroll
    for (int i = 0; i < 2; ++i)
#pragma unroll
      for (int j = 0; j < 3; ++j)
#pragma unroll
        for (int k = 0; k < 16; ++k) acc[i][j][k] = 0.f;
    struct Frag { u32x4 a[2][2]; u32x2 bl_[2][3], bh_[2][3]; };
    Frag FA, FB;
#define K1_B(F, kl, t) ((u32x4){F.bl_[kl][t][0], F.bl_[kl][t][1], F.bh_[kl][t][0], F.bh_[kl][t][1]})
    auto body = [&](Frag& C, Frag& N, int sn, int t4, int sb, bool ST) {
      const char* nb = smem + sn;
      const int kt = korder(t4 & 15);
      const unsigned l = lds0 + sb;
      const char* pa = baseA + kt * 128;
      const char* pb = baseB + (size_t)kt * (64 * 3 * D_ * 2);
      acc[0][0] = mfma32(K1_B(C, 0, 0), C.a[0][0], acc[0][0]);
      if (ST) glds16r(voA0, pa, l + (2 * wave) * 1024);
      N.a[0][0] = *(const u32x4*)(nb + aF0); N.bl_[0][0] = trread(nb + bo0);
      SBR();
      acc[0][1] = mfma32(C.a[0][0], K1_B(C, 0, 1), acc[0][1]);
      if (ST) glds16r(voA1, pa, l + (2 * wave + 1) * 1024);
      N.bh_[0][0] = trread(nb + bo0 + 512); N.bl_[0][1] = trread(nb + bo0 + 1024);
      SBR();
      acc[0][2] = mfma32(C.a[0][0], K1_B(C, 0, 2), acc[0][2]);
      if (ST) glds16r(voB, pb, l + 16384 + (3 * wave) * 1024);
      N.bh_[0][1] = trread(nb + bo0 + 1536);
      SBR();
      acc[1][0] = mfma32(K1_B(C, 0, 0), C.a[0][1], acc[1][0]);
      if (ST) glds16r(voB + 2048, pb, l + 16384 + (3 * wave + 1) * 1024);
      N.bl_[0][2] = trread(nb + bo0 + 2048);
      SBR();
      acc[1][1] = mfma32(C.a[0][1], K1_B(C, 0, 1), acc[1][1]);
      if (ST) glds16r(voB + 4096, pb, l + 16384 + (3 * wave + 2) * 1024);
      N.bh_[0][2] = trread(nb + bo0 + 2560);
      SBR();
      acc[1][2] = mfma32(C.a[0][1], K1_B(C, 0, 2), acc[1][2]);
      N.a[0][1] = *(const u32x4*)(nb + aS0); N.a[1][0] = *(const u32x4*)(nb + aF1);
      SBR();
      acc[0][0] = mfma32(K1_B(C, 1, 0), C.a[1][0], acc[0][0]);
      N.bl_[1][0] = trread(nb + bo1); N.bh_[1][0] = trread(nb + bo1 + 512);
      SBR();
      acc[0][1] = mfma32(C.a[1][0], K1_B(C, 1, 1), acc[0][1]);
      N.bl_[1][1] = trread(nb + bo1 + 1024); N.bh_[1][1] = trread(nb + bo1 + 1536);
      SBR();
      acc[0][2] = mfma32(C.a[1][0], K1_B(C, 1, 2), acc[0][2]);
      N.bl_[1][2] = trread(nb + bo1 + 2048); N.bh_[1][2] = trread(nb + bo1 + 2560);
      SBR();
      acc[1][0] = mfma32(K1_B(C, 1, 0), C.a[1][1], acc[1][0]);
      N.a[1][1] = *(const u32x4*)(nb + aS1);
      SBR();
      acc[1][1] = mfma32(C.a[1][1], K1_B(C, 1, 1), acc[1][1]);
      acc[1][2] = mfma32(C.a[1][1], K1_B(C, 1, 2), acc[1][2]);
      SBR();
    };
    u64 st_row = 0; unsigned st_col = 0;
    auto stats = [&](int sn) {
      const char* nb = smem + sn;
      int t2 = threadIdx.x; asm volatile("" : "+v"(t2));
      {
        const int row = t2 >> 2, q = t2 & 3, key = (row >> 1) & 7;
        const u32x4 v0 = *(const u32x4*)(nb + row * 128 + (((2 * q) ^ key) * 16));
        const u32x4 v1 = *(const u32x4*)(nb + row * 128 + (((2 * q + 1) ^ key) * 16));
        float s = 0.f, sq = 0.f;
#pragma unroll
        for (int i = 0; i < 4; ++i) {
          const float a0 = bflo(v0[i]), a1 = bfhi(v0[i]), a2 = bflo(v1[i]), a3 = bfhi(v1[i]);
          s += (a0 + a1) + (a2 + a3);
          sq = __builtin_fmaf(a0, a0, sq); sq = __builtin_fmaf(a1, a1, sq); sq = __builtin_fmaf(a2, a2, sq); sq = __builtin_fmaf(a3, a3, sq);
        }
        s = quad_sum(s); sq = quad_sum(sq);
        st_row = ((u64)__float_as_uint(sq) << 32) | __float_as_uint(s);
      }
      if (wave < 6) {
        const int cw = wave >= 3 ? 1 : 0, ct = wave - 3 * cw;
        const u32x4 ONES4 = {0x3F803F80u, 0x3F803F80u, 0x3F803F80u, 0x3F803F80u};
        f32x16 ca;
#pragma unroll
        for (int k = 0; k < 16; ++k) ca[k] = 0.f;
#pragma unroll
        for (int ks = 0; ks < 4; ++ks) {
          const char* bp = nb + 16384 + (2 * ks + ((t2 >> 5) & 1)) * 3072 + ct * 1024 + cw * 256 + (((t2 & 15) >> 2) * 64 + ((t2 >> 4) & 1) * 32 + (t2 & 3) * 8);
          const u32x2 lo = trread(bp), hi = trread(bp + 512);
          ca = mfma32(ONES4, (u32x4){lo[0], lo[1], hi[0], hi[1]}, ca);
        }
        st_col = __float_as_uint(ca[0]);
      }
    };

    stage(0, 0); stage(1, K1_STAGE); stage(2, 2 * K1_STAGE); stage(3, 3 * K1_STAGE);
    if (wave == 0) HS_PROBE();
    if (wave == 7) {
      u64 pc; asm volatile("s_getpc_b64 %0" : "=s"(pc));
      const char* cp = (const char*)(pc - (pc & 127)) + (idx * 10 + (lane < 10 ? lane : 0)) * 128;
      asm volatile("global_load_dword a122, %0, off" :: "v"(cp) : "memory", RSV);
    }
    WAIT_VR(15); RAW_BARR();
    hs_step(false, true);
    STAMPT(0, 1, 0);
    FA.a[0][0] = *(const u32x4*)(smem + aF0); FA.a[0][1] = *(const u32x4*)(smem + aS0);
    FA.a[1][0] = *(const u32x4*)(smem + aF1); FA.a[1][1] = *(const u32x4*)(smem + aS1);
#pragma unroll
    for (int t = 0; t < 3; ++t) {
      FA.bl_[0][t] = trread(smem + bo0 + t * 1024); FA.bh_[0][t] = trread(smem + bo0 + t * 1024 + 512);
      FA.bl_[1][t] = trread(smem + bo1 + t * 1024); FA.bh_[1][t] = trread(smem + bo1 + t * 1024 + 512);
    }
    for (int t = 0; t < 16; t += 4) {
      const bool st = t < 12;
      WAIT_VR(10);
      RAW_BARR(); if (t) hs_step(t == 4, false);
      body(FA, FB, 1 * K1_STAGE, t + 4, 0 * K1_STAGE, st);
      if (st) WAIT_VR(10); else WAIT_VR(5);
      RAW_BARR();
      body(FB, FA, 2 * K1_STAGE, t + 5, 1 * K1_STAGE, st);
      if (t == 0) {
        stats(2 * K1_STAGE);
        int t2 = threadIdx.x; asm volatile("" : "+v"(t2));
        if ((t2 & 3) == 0) st64(ws, RS_SLOT(t2), st_row);
        if (wave < 6 && (t2 & 63) < 32) st32(ws, CS_SLOT(t2), st_col);
        if (wave == 0) hs_begin();
      }
      if (st) WAIT_VR(10); else WAIT_VR(0);
      RAW_BARR(); if (t) hs_step(false, false); else hs_pre();
      body(FA, FB, 3 * K1_STAGE, t + 6, 2 * K1_STAGE, st);
      if (st) WAIT_VR(10); else WAIT_VR(0);
      RAW_BARR();
      if (!st) stat_dma();
      body(FB, FA, 0 * K1_STAGE, t + 7, 3 * K1_STAGE, st);
    }
  }
  STAMPT(0, 2, 0);
  tid = threadIdx.x; asm volatile("" : "+v"(tid));
  lane = tid & 63; r = lane & 31; h = lane >> 5;
  trl = ((lane & 15) >> 2) * 64 + ((lane >> 4) & 1) * 32 + (lane & 3) * 8;
  float* xch = (float*)smem;
  float* stm = (float*)(smem + 144384);
  float* kvr = (float*)(smem + 98304);
  float* zr = (float*)(smem + 143360);
  volatile int* misc = (volatile int*)(smem + L_MISC);
  const int R0 = 64 * wr + 32 * ft;
  auto put_stats = [&]() {
    if (wave < 2) {
      float s = 0.f, sq = 0.f;
#pragma unroll
      for (int i = 0; i < 16; ++i) { const f32x2 v = *(const f32x2*)(smem + 98304 + i * 1024 + tid * 8); s += v[0]; sq += v[1]; }
      const float mean = s * (1.0f / 1024.0f);
      const float var = fmaxf(sq * (1.0f / 1024.0f) - mean * mean, 0.f);
      stm[tid] = rbf(mean);
      stm[128 + tid] = __builtin_amdgcn_rcpf(rbf(__builtin_amdgcn_sqrtf(rbf(rbf(var) + 1e-5f))));
    } else if (wave < 5) {
      float c = 0.f;
#pragma unroll
      for (int i = 0; i < 16; ++i) c += *(const float*)(smem + 114688 + i * 768 + (tid - 128) * 4);
      stm[256 + tid - 128] = c;
    }
  };
#pragma unroll
  for (int t = 0; t < 3; ++t)
#pragma unroll
    for (int q4 = 0; q4 < 4; ++q4)
      *(f32x4*)(xch + wave * 3072 + (t * 4 + q4) * 256 + lane * 4) =
          (f32x4){acc[1][t][4 * q4], acc[1][t][4 * q4 + 1], acc[1][t][4 * q4 + 2], acc[1][t][4 * q4 + 3]};
  if (tid == 0) misc[0] = hs_state;
  if (is_master && !ms_done) {
    for (unsigned s = 0; s < SPIN_CAP; ++s) {
      bool ok = true;
#pragma unroll
      for (int j = 0; j < 4; ++j) ok &= (ld32(ws, SY_RE + lane * 16 + j * 4) == ms_exp[j]);
      if (__all(ok)) break;
    }
    STW(ms_exp, SY_GO + lane * 16);
  }
  if (wave == 0 && hs_state < 2) {
    if (hs_state == 0) {
      for (unsigned s = 0; s < SPIN_CAP; ++s) {
        const unsigned go = ld32(ws, SY_GO + bid * 4);
        if (go == hs_cur && go != hs_g0) break;
        const unsigned c = ld32(ws, SY_CH + bid * 4);
        if (c != hs_cur) { hs_cur = c; if (lane == 0) st32(ws, SY_RE + bid * 4, c); }
      }
    }
  }
  WAIT_V(0);
  STAMPT(1, 0, 0);
  RAW_BAR();
  if (misc[0] != 2) { stat_dma(); WAIT_V(0); RAW_BAR(); }
  put_stats();
  RAW_BAR();
  STAMPT(1, 1, 0);
  if (wave >= 4) {
    const u32x4 z4 = {0u, 0u, 0u, 0u};
    const int sl_ = (bid & 7) * 28 + (bid >> 3);
    if (bid < 224) { STW(z4, WS_YP + sl_ * 8192 + (tid - 256) * 16); STW(z4, WS_YP + sl_ * 8192 + 4096 + (tid - 256) * 16); }
    if (wave == 4) STW(z4, WS_GU + sl_ * 1024 + lane * 16);
    if (wave >= 6) {
#pragma unroll
      for (int rr_ = 0; rr_ < XSR; ++rr_) STW(z4, WS_XS + rr_ * 524288 + rp * 16384 + (wave - 6) * 8192 + cb * 1024 + lane * 16);
    }
  }
  const float m_b = stm[R0 + r], rinv = stm[128 + R0 + r], csk = stm[256 + wc * 96 + 32 + r], csv = stm[256 + wc * 96 + 64 + r];
  STAMPT(0, 3, 0);
  const int head = 2 * hp + wc;
  const int rowtile = rb * 4 + 2 * wr + ft;
  unsigned qp[8];
  f32x4 tq[4];
  {
    unsigned kp[8], vp[8];
    u16x2 tmin = {0, 0};
    const float* px = xch + (wave ^ 4) * 3072 + lane * 4;
#pragma unroll
    for (int q4 = 0; q4 < 4; ++q4) {
      const f32x4 xq = *(const f32x4*)(px + q4 * 256);
      const f32x4 xk = *(const f32x4*)(px + (4 + q4) * 256);
      const f32x4 xv = *(const f32x4*)(px + (8 + q4) * 256);
      tq[q4] = (f32x4){acc[0][0][4 * q4], acc[0][0][4 * q4 + 1], acc[0][0][4 * q4 + 2], acc[0][0][4 * q4 + 3]} + xq;
      const f32x4 m4 = *(const f32x4*)(stm + R0 + 8 * q4 + 4 * h);
      const f32x4 i4 = *(const f32x4*)(stm + 128 + R0 + 8 * q4 + 4 * h);
      const f32x2 ck2 = {csk, csk}, cv2 = {csv, csv};
      const f32x2 ma = {m4[0], m4[1]}, mb = {m4[2], m4[3]}, ia = {i4[0], i4[1]}, ib = {i4[2], i4[3]};
      f32x2 k0 = (((f32x2){acc[0][1][4 * q4], acc[0][1][4 * q4 + 1]} + (f32x2){xk[0], xk[1]}) - ma * ck2) * ia;
      f32x2 k1 = (((f32x2){acc[0][1][4 * q4 + 2], acc[0][1][4 * q4 + 3]} + (f32x2){xk[2], xk[3]}) - mb * ck2) * ib;
      kp[2 * q4] = phi_pair(k0, tmin); kp[2 * q4 + 1] = phi_pair(k1, tmin);
      f32x2 v0 = (((f32x2){acc[0][2][4 * q4], acc[0][2][4 * q4 + 1]} + (f32x2){xv[0], xv[1]}) - ma * cv2) * ia;
      f32x2 v1 = (((f32x2){acc[0][2][4 * q4 + 2], acc[0][2][4 * q4 + 3]} + (f32x2){xv[2], xv[3]}) - mb * cv2) * ib;
      vp[2 * q4] = pack2(v0[0], v0[1]); vp[2 * q4 + 1] = pack2(v1[0], v1[1]);
    }
    if (__any(tmin[0] > 0xC0F0 || tmin[1] > 0xC0F0)) {
#pragma unroll
      for (int i = 0; i < 8; ++i) kp[i] = pack2(bflo(kp[i]) + 1e-6f, bfhi(kp[i]) + 1e-6f);
    }
    f32x16 kv, zv;
#pragma unroll
    for (int k = 0; k < 16; ++k) { kv[k] = 0.f; zv[k] = 0.f; }
    const u32x4 ONES4 = {0x3F803F80u, 0x3F803F80u, 0x3F803F80u, 0x3F803F80u};
    kv = mfma32((u32x4){kp[0], kp[1], kp[2], kp[3]}, (u32x4){vp[0], vp[1], vp[2], vp[3]}, kv);
    zv = mfma32((u32x4){kp[0], kp[1], kp[2], kp[3]}, ONES4, zv);
    kv = mfma32((u32x4){kp[4], kp[5], kp[6], kp[7]}, (u32x4){vp[4], vp[5], vp[6], vp[7]}, kv);
    zv = mfma32((u32x4){kp[4], kp[5], kp[6], kp[7]}, ONES4, zv);
#pragma unroll
    for (int q4 = 0; q4 < 4; ++q4)
      *(f32x4*)(kvr + wave * 1024 + q4 * 256 + lane * 4) = (f32x4){kv[4 * q4], kv[4 * q4 + 1], kv[4 * q4 + 2], kv[4 * q4 + 3]};
    if (r == 0) {
#pragma unroll
      for (int q4 = 0; q4 < 4; ++q4)
        *(f32x4*)(zr + wave * 32 + 8 * q4 + 4 * h) = (f32x4){zv[4 * q4], zv[4 * q4 + 1], zv[4 * q4 + 2], zv[4 * q4 + 3]};
    }
  }
  STAMPT(1, 2, 0);
  RAW_BAR();
  STAMPT(1, 3, 0);
  const int hc = tid >> 8, f = tid & 255;
  {
    f32x4 s = *(const f32x4*)(kvr + hc * 1024 + f * 4);
#pragma unroll
    for (int p = 1; p < 4; ++p) s += *(const f32x4*)(kvr + (hc + 2 * p) * 1024 + f * 4);
    const unsigned o_ = WS_KVP + ((hp * 16 + rb) * 2 + hc) * 4224;
    STW(__builtin_bit_cast(u32x4, s), o_ + f * 16);
    if (f < 8) {
      f32x4 z = *(const f32x4*)(zr + hc * 32 + f * 4);
#pragma unroll
      for (int p = 1; p < 4; ++p) z += *(const f32x4*)(zr + (hc + 2 * p) * 32 + f * 4);
      STW(__builtin_bit_cast(u32x4, z), o_ + 4096 + f * 16);
    }
  }
  WAIT_V(0);
  STAMPT(1, 4, 0);
  RAW_BAR();
  if (tid == 0) st32(ws, SY_FA + hp * 256 + rb * 4, 1u);
  STAMPT(0, 4, 0);

  const int rt = wave & 1, kh = (wave >> 1) & 1;
  unsigned voW;
  {
    const int bi = lane >> 4, q = (lane >> 2) & 3, ch = lane & 3;
    voW = (unsigned)((4 * (bi >> 1) + q) * D_ + (bi & 1) * 32 + ch * 8) * 2u;
  }
  const unsigned voL = lane * 16;
  const char* baseW = uni(wo + (size_t)((16 * kh) * 32 + 16 * rt) * D_ + cb * 128);
  const char* baseO = uni(ws + WS_O + (size_t)(((rp * 2 + rt) * 32 + 16 * kh) * 2) * 1024);
  auto stageW = [&](int t, int sb) {
    const char* sw = baseW + (size_t)t * (32 * D_ * 2);
    const unsigned dw = lds0 + sb + kh * 8192 + (2 * rt) * 2048;
    glds16(voW, sw, dw);
    glds16(voW + 128, sw, dw + 1024);
    glds16(voW + 8 * D_ * 2, sw, dw + 2048);
    glds16(voW + 8 * D_ * 2 + 128, sw, dw + 3072);
  };
  auto stageO = [&](int t, int sb) {
    const char* sp = baseO + (size_t)t * 2048;
    glds16_sc1(voL, sp, lds0 + sb + 16384 + wave * 2048);
    glds16_sc1(voL + 1024, sp, lds0 + sb + 16384 + wave * 2048 + 1024);
  };
  if (wave < 4) {
#pragma unroll
    for (int t = 0; t < 6; ++t) stageW(t, t * K2_STAGE);
  }

  {
    u16x2 tmin = {0, 0};
    const f32x2 mb2 = {m_b, m_b}, ri2 = {rinv, rinv};
#pragma unroll
    for (int q4 = 0; q4 < 4; ++q4) {
      const f32x4 c4 = *(const f32x4*)(stm + 256 + wc * 96 + 8 * q4 + 4 * h);
      const f32x2 t0 = ((f32x2){tq[q4][0], tq[q4][1]} - mb2 * (f32x2){c4[0], c4[1]}) * ri2;
      const f32x2 t1 = ((f32x2){tq[q4][2], tq[q4][3]} - mb2 * (f32x2){c4[2], c4[3]}) * ri2;
      qp[2 * q4] = phi_pair(t0, tmin); qp[2 * q4 + 1] = phi_pair(t1, tmin);
    }
    if (__any(tmin[0] > 0xC0F0 || tmin[1] > 0xC0F0)) {
#pragma unroll
      for (int i = 0; i < 8; ++i) qp[i] = pack2(bflo(qp[i]) + 1e-6f, bfhi(qp[i]) + 1e-6f);
    }
#pragma unroll
    for (int i = 0; i < 8; ++i) asm volatile("" : "+v"(qp[i]));
  }
  if (wave == 4) poll_flags(ws, SY_FA + hp * 256 + (lane & 15) * 4, true);
  RAW_BAR();
  STAMPT(0, 5, 0);
  {
    u32x4 pv[16];
    const unsigned o_ = WS_KVP + (hp * 16 * 2 + hc) * 4224 + f * 16;
#pragma unroll
    for (int p = 0; p < 16; ++p) pv[p] = LDW(o_ + p * 8448);
    f32x4 zs = {0.f, 0.f, 0.f, 0.f};
    if (f < 8) {
#pragma unroll
      for (int p = 0; p < 16; ++p) zs += __builtin_bit_cast(f32x4, LDW(o_ + 4096 + p * 8448));
    }
    f32x4 s = __builtin_bit_cast(f32x4, pv[0]);
#pragma unroll
    for (int p = 1; p < 16; ++p) s += __builtin_bit_cast(f32x4, pv[p]);
    asm volatile("" : "+v"(s));
    STAMPT(1, 5, 0);
    const int q4 = f >> 6, ln = f & 63;
    *(u32x2*)(smem + L_KVL + ((hc * 2 + (q4 >> 1)) * 64 + ln) * 16 + (q4 & 1) * 8) = (u32x2){pack2(s[0], s[1]), pack2(s[2], s[3])};
    if (f < 8)
      *(u32x2*)(smem + L_ZL + hc * 64 + (f & 1) * 32 + (f >> 1) * 8) = (u32x2){pack2(zs[0], zs[1]), pack2(zs[2], zs[3])};
  }
  RAW_BAR();
  {
    const u32x4 k0 = *(const u32x4*)(smem + L_KVL + (wc * 2) * 1024 + lane * 16);
    const u32x4 k1 = *(const u32x4*)(smem + L_KVL + (wc * 2 + 1) * 1024 + lane * 16);
    const u32x4 z0 = *(const u32x4*)(smem + L_ZL + wc * 64 + h * 32), z1 = *(const u32x4*)(smem + L_ZL + wc * 64 + h * 32 + 16);
    f32x16 X, Dn;
#pragma unroll
    for (int k = 0; k < 16; ++k) { X[k] = 0.f; Dn[k] = 0.f; }
    const u32x4 p0 = {qp[0], qp[1], qp[2], qp[3]}, p1 = {qp[4], qp[5], qp[6], qp[7]};
    X = mfma32(k0, p0, X); Dn = mfma32(z0, p0, Dn);
    X = mfma32(k1, p1, X); Dn = mfma32(z1, p1, Dn);
    const float ri = __builtin_amdgcn_rcpf(rbf(rbf(Dn[0]) + 1e-6f));
    u32x4 o0, o1;
#pragma unroll
    for (int i = 0; i < 4; ++i) {
      o0[i] = pack2(rbf(X[2 * i]) * ri, rbf(X[2 * i + 1]) * ri);
      o1[i] = pack2(rbf(X[8 + 2 * i]) * ri, rbf(X[8 + 2 * i + 1]) * ri);
    }
    const unsigned oo = WS_O + ((rowtile * 32 + head) * 2) * 1024 + lane * 16;
    STAMPT(1, 6, 0);
    STW(o0, oo); STW(o1, oo + 1024);
  }
  WAIT_V(0);
  STAMPT(1, 7, 0);
  RAW_BAR();
  if (tid == 0) st32(ws, SY_FB + rb * 256 + hp * 4, 1u);
  STAMPT(0, 6, 0);

  const bool has_mlp = bid < 224;
  float* xs = (float*)smem;
  float* hs = (float*)(smem + 8192);
  float* red = (float*)(smem + 28672);
  float* act = (float*)(smem + 17408);
  float* sgm = (float*)(smem + 17536);
  float* yh = (float*)(smem + 18432);

  if (wave < 4) {
    poll_flags(ws, SY_FB + (rp >> 1) * 256 + (lane & 15) * 4, wave == 0);
    RAW_BAR();
    STAMPT(0, 7, 0);
    float hv[32];
    const int erow = tid >> 2, eseg = tid & 3;
    const size_t ego = (size_t)(rp * 64 + erow) * D_ + cb * 128 + eseg * 32;
#pragma unroll
    for (int t = 0; t < 6; ++t) stageO(t, t * K2_STAGE);
    f32x16 a2[4];
#pragma unroll
    for (int i = 0; i < 4; ++i)
#pragma unroll
      for (int k = 0; k < 16; ++k) a2[i][k] = 0.f;
    const int woff = kh * 8192 + h * 512 + trl;
    const int poff = 16384 + wave * 2048 + lane * 16;
    struct Frag2 { u32x4 o[2]; u32x2 lo[2][4], hi[2][4]; };
    Frag2 GA, GB;
    auto ld2 = [&](Frag2& N, int sn, int st, int nt) {
      const char* base = smem + sn + woff;
      N.lo[st][nt] = trread(base + ((2 * st) * 2 + (nt >> 1)) * 1024 + (nt & 1) * 256);
      N.hi[st][nt] = trread(base + ((2 * st + 1) * 2 + (nt >> 1)) * 1024 + (nt & 1) * 256);
    };
    auto body2 = [&](Frag2& C, Frag2& N, int t, bool LD) {
      const int sn = ((t + 1) % 6) * K2_STAGE, sb = (t % 6) * K2_STAGE;
      const bool ST = t + 6 < 16;
      const char* sw = baseW + (size_t)(t + 6) * (32 * D_ * 2);
      const char* sp = baseO + (size_t)(t + 6) * 2048;
      const unsigned dw = lds0 + sb + kh * 8192 + (2 * rt) * 2048, dp = lds0 + sb + 16384 + wave * 2048;
#pragma unroll
      for (int st = 0; st < 2; ++st)
#pragma unroll
        for (int nt = 0; nt < 4; ++nt) {
          const int i = st * 4 + nt;
          a2[nt] = mfma32((u32x4){C.lo[st][nt][0], C.lo[st][nt][1], C.hi[st][nt][0], C.hi[st][nt][1]}, C.o[st], a2[nt]);
          if (ST) {
            if (i == 0) glds16(voW, sw, dw);
            if (i == 1) glds16(voW + 128, sw, dw + 1024);
            if (i == 2) glds16(voW + 8 * D_ * 2, sw, dw + 2048);
            if (i == 3) glds16(voW + 8 * D_ * 2 + 128, sw, dw + 3072);
            if (i == 4) glds16_sc1(voL, sp, dp);
            if (i == 5) glds16_sc1(voL + 1024, sp, dp + 1024);
          }
          if (LD) {
            if (nt == 0) N.o[st] = *(const u32x4*)(smem + sn + poff + st * 1024);
            ld2(N, sn, st, nt);
          }
          SB0();
        }
    };
    WAIT_V(10); RAW_BAR();
    GA.o[0] = *(const u32x4*)(smem + poff); GA.o[1] = *(const u32x4*)(smem + poff + 1024);
#pragma unroll
    for (int st = 0; st < 2; ++st)
#pragma unroll
      for (int nt = 0; nt < 4; ++nt) ld2(GA, 0, st, nt);
    WAIT_V(8);  RAW_BAR(); body2(GA, GB, 0, true);
    WAIT_V(12); RAW_BAR(); body2(GB, GA, 1, true);
    WAIT_V(16); RAW_BAR(); body2(GA, GB, 2, true);
    WAIT_V(20); RAW_BAR(); body2(GB, GA, 3, true);
    WAIT_V(24); RAW_BAR(); body2(GA, GB, 4, true);
    WAIT_V(24); RAW_BAR(); body2(GB, GA, 5, true);
    WAIT_V(24); RAW_BAR(); body2(GA, GB, 6, true);
    WAIT_V(24); RAW_BAR(); body2(GB, GA, 7, true);
    WAIT_V(24); RAW_BAR(); body2(GA, GB, 8, true);
    WAIT_V(24); RAW_BAR(); body2(GB, GA, 9, true);
    WAIT_V(24); RAW_BAR(); body2(GA, GB, 10, true);
    WAIT_V(18); RAW_BAR(); body2(GB, GA, 11, true);
    WAIT_V(12); RAW_BAR(); body2(GA, GB, 12, true);
    WAIT_V(6);  RAW_BAR(); body2(GB, GA, 13, true);
    WAIT_V(0);  RAW_BAR(); body2(GA, GB, 14, true);
    u32x4 xv[4], wv[4];
#pragma unroll
    for (int c = 0; c < 4; ++c) { xv[c] = *(const u32x4*)(x + ego + c * 8); wv[c] = *(const u32x4*)(w_r + cb * 128 + eseg * 32 + c * 8); }
    RAW_BAR();             body2(GB, GA, 15, false);
    STAMPT(0, 8, 0);
    float* P = (float*)smem;
    {
      float* pr = P + (kh * 64 + 32 * rt + r) * 132 + 4 * h;
#pragma unroll
      for (int nt = 0; nt < 4; ++nt)
#pragma unroll
        for (int q4 = 0; q4 < 4; ++q4)
          *(f32x4*)(pr + 32 * nt + 8 * q4) = (f32x4){a2[nt][4 * q4], a2[nt][4 * q4 + 1], a2[nt][4 * q4 + 2], a2[nt][4 * q4 + 3]};
    }
    RAW_BAR();
    STAMPT(1, 8, 0);
    {
      const float* p0 = P + erow * 132 + eseg * 32;
      const float* p1 = p0 + 64 * 132;
      float s = 0.f, pw = 0.f, sw_ = 0.f;
#pragma unroll
      for (int c = 0; c < 4; ++c) {
        f32x4 a0 = *(const f32x4*)(p0 + c * 8), a1 = *(const f32x4*)(p0 + c * 8 + 4);
        a0 += *(const f32x4*)(p1 + c * 8); a1 += *(const f32x4*)(p1 + c * 8 + 4);
        u32x4 hb;
        hb[0] = pack2(bflo(xv[c][0]) + rbf(a0[0]), bfhi(xv[c][0]) + rbf(a0[1]));
        hb[1] = pack2(bflo(xv[c][1]) + rbf(a0[2]), bfhi(xv[c][1]) + rbf(a0[3]));
        hb[2] = pack2(bflo(xv[c][2]) + rbf(a1[0]), bfhi(xv[c][2]) + rbf(a1[1]));
        hb[3] = pack2(bflo(xv[c][3]) + rbf(a1[2]), bfhi(xv[c][3]) + rbf(a1[3]));
#pragma unroll
        for (int i = 0; i < 4; ++i) {
          const float h0 = bflo(hb[i]), h1 = bfhi(hb[i]), w0 = bflo(wv[c][i]), w1 = bfhi(wv[c][i]);
          hv[c * 8 + 2 * i] = h0; hv[c * 8 + 2 * i + 1] = h1;
          s += h0 + h1; pw = __builtin_fmaf(h0, w0, pw); pw = __builtin_fmaf(h1, w1, pw); sw_ += w0 + w1;
        }
      }
      const float cm = quad_sum(s) * (1.0f / 128.0f);
      float m2 = 0.f;
#pragma unroll
      for (int i = 0; i < 32; ++i) { const float d = hv[i] - cm; m2 = __builtin_fmaf(d, d, m2); }
      m2 = quad_sum(m2); pw = quad_sum(pw); sw_ = quad_sum(sw_);
      if (eseg == 0) STW(((u32x4){__float_as_uint(cm), __float_as_uint(m2) | 1u, __float_as_uint(pw), __float_as_uint(sw_) | 1u}),
                         WS_ST2 + ((rp * 8 + cb) * 64 + erow) * 16);
    }
    STAMPT(0, 9, 0);
    int lc0, lc1;
    {
      u32x4 pu_[8];
      for (unsigned s_ = 0; s_ < SPIN_CAP; ++s_) {
        unsigned ok = 1u;
#pragma unroll
        for (int j = 0; j < 8; ++j) { pu_[j] = LDW(WS_ST2 + ((rp * 8 + j) * 64 + lane) * 16); ok &= pu_[j][1] & pu_[j][3]; }
        if (__all(ok & 1u)) break;
        asm volatile("" ::: "memory");
      }
      STAMPT(0, 10, 0);
      float cs = 0.f, m2 = 0.f, pw = 0.f, sw_ = 0.f;
#pragma unroll
      for (int j = 0; j < 8; ++j) { const f32x4 pt = __builtin_bit_cast(f32x4, pu_[j]); cs += pt[0]; m2 += pt[1]; pw += pt[2]; sw_ += pt[3]; }
      const float mean = cs * 0.125f;
      float dd = 0.f;
#pragma unroll
      for (int j = 0; j < 8; ++j) { const float d = __uint_as_float(pu_[j][0]) - mean; dd = __builtin_fmaf(d, d, dd); }
      const float var = (m2 + 128.0f * dd) * (1.0f / 1024.0f);
      const float mb = rbf(mean), sd = rbf(__builtin_amdgcn_sqrtf(rbf(rbf(var) + 1e-5f)));
      float sc = rbf((pw - mb * sw_) / sd);
      if (!(sc == sc)) sc = -INFINITY;
      unsigned b0 = mk_key(sc, rp * 64 + lane), b1 = 0;
      top2_wave(b0, b1);
      if (tid == 0) st64(ws, SY_FD + cb * 512 + rp * 8, ((u64)b1 << 32) | b0);
      lc0 = key_idx(b0) & (S_ - 1); lc1 = key_idx(b1) & (S_ - 1);
      STAMPT(1, 11, 0);
    }
    float mb, rs;
    {
      u32x4 pa, pb;
      for (unsigned s_ = 0; s_ < SPIN_CAP; ++s_) {
        pa = LDW(WS_ST2 + ((rp * 8 + 2 * eseg) * 64 + erow) * 16);
        pb = LDW(WS_ST2 + ((rp * 8 + 2 * eseg + 1) * 64 + erow) * 16);
        if (__all(pa[1] & pa[3] & pb[1] & pb[3] & 1u)) break;
        asm volatile("" ::: "memory");
      }
      const float ca = __uint_as_float(pa[0]), cb_ = __uint_as_float(pb[0]);
      const float mean = quad_sum(ca + cb_) * 0.125f;
      const float d0 = ca - mean, d1 = cb_ - mean;
      const float var = quad_sum((__uint_as_float(pa[1]) + __uint_as_float(pb[1])) + 128.0f * (d0 * d0 + d1 * d1)) * (1.0f / 1024.0f);
      mb = rbf(mean);
      rs = 1.0f / rbf(__builtin_amdgcn_sqrtf(rbf(rbf(var) + 1e-5f)));
    }
    unsigned nq[16];
#pragma unroll
    for (int i = 0; i < 16; ++i) {
      const unsigned dq = pack2(hv[2 * i] - mb, hv[2 * i + 1] - mb);
      nq[i] = pack2(bflo(dq) * rs, bfhi(dq) * rs);
    }
    const int grow = rp * 64 + erow;
#pragma unroll
    for (int tk = 0; tk < 2; ++tk) {
      const int sel = tk ? lc1 : lc0;
      if (__any(grow == sel)) {
        char* tb = smem + 69632 + wave * 1024;
        if (grow == sel) {
#pragma unroll
          for (int i = 0; i < 16; ++i)
            *(u32x4*)(tb + eseg * 256 + i * 16) = (u32x4){nq[i] << 16, __float_as_uint(hv[2 * i]) | 1u, nq[i] & 0xffff0000u, __float_as_uint(hv[2 * i + 1]) | 1u};
        }
        __builtin_amdgcn_wave_barrier();
        const u32x4 v4 = *(const u32x4*)(tb + lane * 16);
#pragma unroll
        for (int rr_ = 0; rr_ < XSR; ++rr_) STW(v4, WS_XS + rr_ * 524288 + rp * 16384 + tk * 8192 + cb * 1024 + lane * 16);
        __builtin_amdgcn_wave_barrier();
      }
    }
    if (wave == 0) {
      u64 cc = 0;
      for (unsigned s_ = 0; s_ < SPIN_CAP; ++s_) {
        cc = ld64(ws, SY_FD + cb * 512 + (lane & 31) * 8);
        if (__all(cc != 0ull)) break;
      }
      STAMPT(1, 13, 0);
      unsigned c0 = lane < 32 ? (unsigned)cc : 0u, c1 = lane < 32 ? (unsigned)(cc >> 32) : 0u;
      top2_wave(c0, c1);
      const unsigned w0 = key_idx(c0) & (S_ - 1), w1 = key_idx(c1) & (S_ - 1);
      const unsigned k0 = __builtin_amdgcn_readlane((unsigned)cc, w0 >> 6), k1 = __builtin_amdgcn_readlane((unsigned)cc, w1 >> 6);
      if (lane == 0) { misc[3] = w0; misc[4] = w1; misc[5] = (w0 >> 6) * 2 + (k0 == c0 ? 0 : 1); misc[6] = (w1 >> 6) * 2 + (k1 == c1 ? 0 : 1); }
    }
    RAW_BAR();
    STAMPT(0, 11, 0);
    const int sel0 = misc[3], sel1 = misc[4];
    RAW_BAR();
    if (grow != sel0 && grow != sel1) {
#pragma unroll
      for (int c = 0; c < 4; ++c) {
        u32x4 ob;
#pragma unroll
        for (int i = 0; i < 4; ++i) ob[i] = pack2(hv[8 * c + 2 * i] + bflo(nq[4 * c + i]), hv[8 * c + 2 * i + 1] + bfhi(nq[4 * c + i]));
        __builtin_amdgcn_raw_buffer_store_b128(ob, __builtin_amdgcn_make_buffer_rsrc(out, 0, S_ * D_ * 2, 0x00020000), (unsigned)(ego + c * 8) * 2u, 0, 2);
      }
    }
    STAMPT(0, 12, 0);
    if (has_mlp) { RAW_BAR(); RAW_BAR(); }
  } else {
    const int fs = has_mlp ? (bid & 7) * 28 + (bid >> 3) : (bid & 7) * 28;
    const int mt = tid & 255;
    u32x4 wgr[8], wur[8], wdr[8];
    const int krow = (fs & 3) * 256 + (mt >> 6) * 64 + (lane >> 3);
    const u16* pg = wg + (size_t)krow * F_ + (fs >> 2) * 64 + (lane & 7) * 8;
    const u16* pu = wu + (size_t)krow * F_ + (fs >> 2) * 64 + (lane & 7) * 8;
    const u16* pd = wd + (size_t)(fs * 16 + h * 8) * D_ + ((mt >> 6) * 32 + r) * 8;
    RAW_BAR(); RAW_BAR();
#pragma unroll
    for (int i = 0; i < 8; ++i) {
      asm volatile("global_load_dwordx4 %0, %1, off" : "=a"(wgr[i]) : "v"(pg + (size_t)i * 8 * F_) : "memory");
      asm volatile("global_load_dwordx4 %0, %1, off" : "=a"(wdr[i]) : "v"(pd + (size_t)i * D_) : "memory");
      RAW_BAR();
      asm volatile("global_load_dwordx4 %0, %1, off" : "=a"(wur[i]) : "v"(pu + (size_t)i * 8 * F_) : "memory");
      RAW_BAR();
    }
    RAW_BAR();
    WAIT_V(0);
#pragma unroll
    for (int i = 0; i < 8; ++i) asm volatile("" : "+a"(wgr[i]), "+a"(wur[i]), "+a"(wdr[i]));
    unsigned wr2[8];
    if (wave < 6) {
#pragma unroll
      for (int j = 0; j < 8; ++j) wr2[j] = ((const unsigned*)w_r)[j * 64 + lane];
    }
    RAW_BAR();
    if (wave < 6) {
      const int tk = wave - 4;
      u32x4 q[8];
      const unsigned xo = WS_XS + ((bid >> 3) & (XSR - 1)) * 524288u + (unsigned)misc[5 + tk] * 8192u;
      for (unsigned s_ = 0; s_ < SPIN_CAP; ++s_) {
        unsigned ok = 1u;
#pragma unroll
        for (int j = 0; j < 8; ++j) { q[j] = LDW(xo + j * 1024 + lane * 16); ok &= q[j][1] & q[j][3]; }
        if (__all(ok & 1u)) break;
        asm volatile("" ::: "memory");
        __builtin_amdgcn_s_sleep(XSL1);
      }
      float rr = 0.f;
#pragma unroll
      for (int j = 0; j < 8; ++j) {
        rr = __builtin_fmaf(__uint_as_float(q[j][0]), bflo(wr2[j]), rr); rr = __builtin_fmaf(__uint_as_float(q[j][2]), bfhi(wr2[j]), rr);
        *(u32x2*)(xs + tk * 1024 + j * 128 + lane * 2) = (u32x2){q[j][0], q[j][2]};
        *(u32x2*)(hs + tk * 1024 + j * 128 + lane * 2) = (u32x2){q[j][1] & ~1u, q[j][3] & ~1u};
      }
      rr = rbf(wave_sum(rr));
      if (lane == 0) sgm[tk] = rbf(1.0f / (1.0f + __expf(-rr)));
    }
    RAW_BAR();
    if (has_mlp) {
      {
        f32x2 ag[8], au[8];
#pragma unroll
        for (int c = 0; c < 8; ++c) { ag[c] = (f32x2){0.f, 0.f}; au[c] = (f32x2){0.f, 0.f}; }
#pragma unroll
        for (int i = 0; i < 8; ++i) {
          const f32x2 xx = {xs[krow + 8 * i], xs[1024 + krow + 8 * i]};
#pragma unroll
          for (int c = 0; c < 4; ++c) {
            ag[2 * c] += xx * bflo(wgr[i][c]); ag[2 * c + 1] += xx * bfhi(wgr[i][c]);
            au[2 * c] += xx * bflo(wur[i][c]); au[2 * c + 1] += xx * bfhi(wur[i][c]);
          }
        }
        float v[32];
#pragma unroll
        for (int c = 0; c < 8; ++c) { v[c] = ag[c][0]; v[8 + c] = ag[c][1]; v[16 + c] = au[c][0]; v[24 + c] = au[c][1]; }
#pragma unroll
        for (int i = 0; i < 16; ++i) {
          auto sw = __builtin_amdgcn_permlane32_swap(__float_as_uint(v[i]), __float_as_uint(v[16 + i]), false, false);
          v[i] = __uint_as_float(sw[0]) + __uint_as_float(sw[1]);
        }
#pragma unroll
        for (int i = 0; i < 8; ++i) {
          auto sw = __builtin_amdgcn_permlane16_swap(__float_as_uint(v[i]), __float_as_uint(v[8 + i]), false, false);
          v[i] = __uint_as_float(sw[0]) + __uint_as_float(sw[1]);
        }
        const bool up = (lane & 8) != 0;
#pragma unroll
        for (int i = 0; i < 4; ++i) {
          const float keep = up ? v[4 + i] : v[i], give = up ? v[i] : v[4 + i];
          v[i] = keep + dppf<0x128>(give);
        }
        *(f32x4*)(red + (wave - 4) * 256 + (lane >> 5) * 128 + ((lane >> 4) & 1) * 64 + (lane & 7) * 8 + ((lane >> 3) & 1) * 4) = (f32x4){v[0], v[1], v[2], v[3]};
      }
      STAMPT(1, 14, 256);
      RAW_BAR();
      if (wave == 4) {
        f32x4 s4 = *(const f32x4*)(red + lane * 4);
#pragma unroll
        for (int w2 = 1; w2 < 4; ++w2) s4 += *(const f32x4*)(red + w2 * 256 + lane * 4);
        u32x4 t4 = __builtin_bit_cast(u32x4, s4); t4[1] |= 1u; t4[3] |= 1u;
        STW(t4, WS_GU + fs * 1024 + lane * 16);
        u32x4 q[4];
        for (unsigned s_ = 0; s_ < SPIN_CAP; ++s_) {
          unsigned ok = 1u;
#pragma unroll
          for (int j = 0; j < 4; ++j) { q[j] = LDW(WS_GU + ((fs & ~3) + j) * 1024 + lane * 16); ok &= q[j][1] & q[j][3]; }
          if (__all(ok & 1u)) break;
          asm volatile("" ::: "memory");
        }
        const f32x4 tot = (__builtin_bit_cast(f32x4, q[0]) + __builtin_bit_cast(f32x4, q[1])) + (__builtin_bit_cast(f32x4, q[2]) + __builtin_bit_cast(f32x4, q[3]));
        f32x4 a4;
#pragma unroll
        for (int i = 0; i < 4; ++i) {
          auto sw = __builtin_amdgcn_permlane32_swap(__float_as_uint(tot[i]), __float_as_uint(tot[i]), false, false);
          const float gs = rbf(__uint_as_float(sw[0])), us = rbf(__uint_as_float(sw[1]));
          const float sg = rbf(gs / (1.0f + __expf(-gs)));
          a4[i] = rbf(sg * us);
        }
        if (lane < 32 && ((lane & 15) >> 2) == (fs & 3)) *(f32x4*)(act + (lane >> 4) * 16 + (lane & 3) * 4) = a4;
      }
      RAW_BAR();
      float y0[8], y1[8];
#pragma unroll
      for (int c = 0; c < 8; ++c) { y0[c] = 0.f; y1[c] = 0.f; }
#pragma unroll
      for (int i = 0; i < 8; ++i) {
        const float a0 = act[h * 8 + i], a1 = act[16 + h * 8 + i];
#pragma unroll
        for (int c = 0; c < 4; ++c) {
          const float dl = bflo(wdr[i][c]), dh = bfhi(wdr[i][c]);
          y0[2 * c] += a0 * dl; y0[2 * c + 1] += a0 * dh; y1[2 * c] += a1 * dl; y1[2 * c + 1] += a1 * dh;
        }
      }
      u32x4 ya, yb;
#pragma unroll
      for (int c = 0; c < 8; ++c) {
        auto sw = __builtin_amdgcn_permlane32_swap(__float_as_uint(y0[c]), __float_as_uint(y1[c]), false, false);
        const unsigned v = __float_as_uint(__uint_as_float(sw[0]) + __uint_as_float(sw[1])) | (c & 1);
        if (c < 4) ya[c] = v; else yb[c - 4] = v;
      }
      const unsigned yo = WS_YP + fs * 8192 + h * 4096 + ((mt >> 6) * 32 + r) * 32;
      STW(ya, yo); STW(yb, yo + 16);
      STAMPT(1, 15, 256);
      STAMPT(0, 13, 256);
    }
  }
  if (!has_mlp) {
    const int j = bid - 224, tk = j >> 4, c0 = (j & 15) * 64;
    const int pg = tid >> 4, c4 = tid & 15;
    u32x4 pv[7];
    __builtin_amdgcn_s_sleep(YSL0);
    for (unsigned s_ = 0; s_ < SPIN_CAP; ++s_) {
      unsigned ok = 1u;
#pragma unroll
      for (int p = 0; p < 7; ++p) { pv[p] = LDW(WS_YP + (pg * 7 + p) * 8192 + tk * 4096 + c0 * 4 + c4 * 16); ok &= pv[p][1] & pv[p][3]; }
      if (__all(ok & 1u)) break;
      asm volatile("" ::: "memory");
      __builtin_amdgcn_s_sleep(YSL1);
    }
    STAMPT(0, 13, 0);
    f32x4 s = {0.f, 0.f, 0.f, 0.f};
#pragma unroll
    for (int p = 0; p < 7; ++p) s += __builtin_bit_cast(f32x4, pv[p]);
    float* rd = (float*)(smem + 32768);
    *(f32x4*)(rd + pg * 64 + c4 * 4) = s;
    RAW_BAR();
    if (tid < 64) {
      float y = 0.f;
#pragma unroll
      for (int p = 0; p < 32; ++p) y += rd[p * 64 + tid];
      const float yy = rbf(rbf(y) * sgm[tk]);
      const float t = rbf(xs[tk * 1024 + c0 + tid] + yy);
      out[(size_t)misc[3 + tk] * D_ + c0 + tid] = (u16)(pack2(hs[tk * 1024 + c0 + tid] + t, 0.f) & 0xffffu);
    }
    STAMPT(0, 14, 0);
  }
  if (pt_) { while (__builtin_amdgcn_s_memrealtime() < pt_ + 6000) __builtin_amdgcn_s_sleep(2); }
}

extern "C" void dbg_layout(long long* l) {
  l[0] = -1; l[1] = -1; l[2] = -1; l[3] = -1; l[4] = -1;
}

extern "C" void kernel_launch(void* const* d_in, const int* in_sizes, int n_in, void* d_out, int out_size,
                              void* d_ws, size_t ws_size, hipStream_t stream) {
  (void)hipFuncSetAttribute((const void*)fused, hipFuncAttributeMaxDynamicSharedMemorySize, LDS_TOTAL);
  fused<<<256, 512, LDS_TOTAL, stream>>>((const u16*)d_in[0], (const u16*)d_in[1], (const u16*)d_in[2], (const u16*)d_in[4],
                                         (const u16*)d_in[5], (const u16*)d_in[6], (const u16*)d_in[7], (u16*)d_out, (char*)d_ws);
}
```

```cpp
#define PROBE 0
#include <hip/hip_runtime.h>
#include <stdint.h>

#define LAS __attribute__((address_space(3)))
#define GAS __attribute__((address_space(1)))
#define DI __device__ __forceinline__

constexpr int KROT = 2;
constexpr int XSR = 4;
constexpr int XSL1 = 6;
constexpr int YSL0 = 64, YSL1 = 4;
typedef unsigned short u16;
typedef unsigned long long u64;
typedef __attribute__((ext_vector_type(2))) __bf16 bf16x2;
typedef __attribute__((ext_vector_type(8))) __bf16 bf16x8;
typedef __attribute__((ext_vector_type(4))) short s16x4;
typedef __attribute__((ext_vector_type(2))) unsigned short u16x2;
typedef __attribute__((ext_vector_type(16))) float f32x16;
typedef __attribute__((ext_vector_type(4))) float f32x4;
typedef __attribute__((ext_vector_type(2))) float f32x2;
typedef __attribute__((ext_vector_type(4))) unsigned u32x4;
typedef __attribute__((ext_vector_type(2))) unsigned u32x2;
typedef GAS unsigned gu32;
typedef GAS unsigned long long gu64;

constexpr int S_ = 2048, D_ = 1024, F_ = 3584;

constexpr unsigned SY_CH = 0, SY_GO = 1024, SY_RE = 2048, SY_FSR = 4096, SY_FSC = 8192, SY_FA = 12288, SY_FB = 16384,
                   SY_FC = 20480, SY_FD = 28672, SY_FE = 32768;
constexpr unsigned WS_RS  = 65536;
constexpr unsigned WS_CS  = WS_RS + 16 * 128 * 128;
constexpr unsigned WS_KVP = WS_CS + 16 * 192 * 64;
constexpr unsigned WS_O   = WS_KVP + 256 * 8448;
constexpr unsigned WS_ST2 = WS_O + S_ * D_ * 2;
constexpr unsigned WS_YP  = WS_ST2 + 32 * 8 * 64 * 16;
constexpr unsigned WS_GU  = WS_YP + 224 * 8192;
constexpr unsigned WS_XS  = WS_GU + 256 * 1024;
constexpr unsigned WS_END = WS_XS + 4 * 32 * 16384;
constexpr size_t WS_DIAG  = 64u << 20;
#define STAMPT(kid, i, T) do { if ((kid) * 16 + (i) == PROBE) pt_ = __builtin_amdgcn_s_memrealtime(); } while (0)

DI float bflo(unsigned u) { return __uint_as_float(u << 16); }
DI float bfhi(unsigned u) { return __uint_as_float(u & 0xffff0000u); }
DI float rbf(float x) { return (float)(__bf16)x; }
DI unsigned pack2(float a, float b) { bf16x2 c; c[0] = (__bf16)a; c[1] = (__bf16)b; return __builtin_bit_cast(unsigned, c); }
DI f32x16 mfma32(u32x4 a, u32x4 b, f32x16 c) {
  return __builtin_amdgcn_mfma_f32_32x32x16_bf16(__builtin_bit_cast(bf16x8, a), __builtin_bit_cast(bf16x8, b), c, 0, 0, 0);
}
DI u32x2 trread(const char* p) {
  s16x4 v = __builtin_amdgcn_ds_read_tr16_b64_v4i16((LAS s16x4*)p);
  return __builtin_bit_cast(u32x2, v);
}
DI const char* uni(const void* p) {
  const u64 v = (u64)p;
  return (const char*)(((u64)(unsigned)__builtin_amdgcn_readfirstlane((int)(v >> 32)) << 32) | (unsigned)__builtin_amdgcn_readfirstlane((int)v));
}
#define RSV "a119", "a120", "a121", "a122", "a123", "a124", "a125", "a126", "a127"
DI void glds16(unsigned voff, const void* sbase, unsigned lds) {
  asm volatile("s_mov_b32 m0, %2\n\ts_nop 0\n\tglobal_load_lds_dwordx4 %0, %1" ::"v"(voff), "s"(uni(sbase)), "s"(__builtin_amdgcn_readfirstlane(lds)) : "memory");
}
DI void glds16r(unsigned voff, const void* sbase, unsigned lds) {
  asm volatile("s_mov_b32 m0, %2\n\ts_nop 0\n\tglobal_load_lds_dwordx4 %0, %1" ::"v"(voff), "s"(uni(sbase)), "s"(__builtin_amdgcn_readfirstlane(lds)) : "memory", RSV);
}
DI void glds16_sc1(unsigned voff, const void* sbase, unsigned lds) {
  asm volatile("s_mov_b32 m0, %2\n\ts_nop 0\n\tglobal_load_lds_dwordx4 %0, %1 sc1" ::"v"(voff), "s"(uni(sbase)), "s"(__builtin_amdgcn_readfirstlane(lds)) : "memory");
}
DI float xh_sum(float v) {
  auto r = __builtin_amdgcn_permlane32_swap(__float_as_uint(v), __float_as_uint(v), false, false);
  return __uint_as_float(r[0]) + __uint_as_float(r[1]);
}
template <int CTRL> DI unsigned dppu(unsigned v) { return (unsigned)__builtin_amdgcn_update_dpp(0, (int)v, CTRL, 0xf, 0xf, true); }
template <int CTRL> DI float dppf(float v) { return __uint_as_float(dppu<CTRL>(__float_as_uint(v))); }
constexpr int DPP_X1 = 0xB1, DPP_X2 = 0x4E, DPP_HMIR = 0x141, DPP_MIR = 0x140;
DI float quad_sum(float v) { v += dppf<DPP_X1>(v); v += dppf<DPP_X2>(v); return v; }
DI float x16_sum(float v) {
  auto r = __builtin_amdgcn_permlane16_swap(__float_as_uint(v), __float_as_uint(v), false, false);
  return __uint_as_float(r[0]) + __uint_as_float(r[1]);
}
DI float wave_sum(float v) {
  v = quad_sum(v); v += dppf<DPP_HMIR>(v); v += dppf<DPP_MIR>(v);
  return xh_sum(x16_sum(v));
}
DI unsigned phi_pair(f32x2 t, u16x2& tmax) {
  const unsigned tb = pack2(t[0], t[1]);
  tmax = __builtin_elementwise_max(tmax, __builtin_bit_cast(u16x2, tb));
  const f32x2 tr = {bflo(tb), bfhi(tb)};
  const f32x2 u = tr + 1.0f;
  const f32x2 l = tr * 1.44269504088896340736f;
  const float e0 = __builtin_amdgcn_exp2f(l[0]), e1 = __builtin_amdgcn_exp2f(l[1]);
  return pack2(tr[0] > 0.f ? u[0] : e0, tr[1] > 0.f ? u[1] : e1);
}
#define RLX_AGENT __ATOMIC_RELAXED, __HIP_MEMORY_SCOPE_AGENT
DI void st32(char* ws, unsigned off, unsigned v) { __hip_atomic_store((gu32*)(ws + off), v, RLX_AGENT); }
DI void st64(char* ws, unsigned off, u64 v) { __hip_atomic_store((gu64*)(ws + off), v, RLX_AGENT); }
DI unsigned ld32(char* ws, unsigned off) { return __hip_atomic_load((gu32*)(ws + off), RLX_AGENT); }
DI u64 ld64(char* ws, unsigned off) { return __hip_atomic_load((gu64*)(ws + off), RLX_AGENT); }
#define WAIT_V(n) asm volatile("s_waitcnt vmcnt(" #n ")" ::: "memory")
#define WAIT_L0() asm volatile("s_waitcnt lgkmcnt(0)" ::: "memory")
#define RAW_BAR() do { WAIT_L0(); __builtin_amdgcn_s_barrier(); asm volatile("" ::: "memory"); } while (0)
#define SB0() __builtin_amdgcn_sched_barrier(0)
#define WAIT_VR(n) asm volatile("s_waitcnt vmcnt(" #n ")" ::: "memory", RSV)
#define RAW_BARR() do { asm volatile("s_waitcnt lgkmcnt(0)" ::: "memory", RSV); __builtin_amdgcn_s_barrier(); asm volatile("" ::: "memory", RSV); } while (0)
#define SBR() do { asm volatile("" ::: RSV); __builtin_amdgcn_sched_barrier(0); } while (0)
constexpr unsigned SPIN_CAP = 1u << 22;
DI void poll_flags(char* ws, unsigned off, bool act) {
  for (unsigned s = 0; s < SPIN_CAP; ++s) {
    const unsigned v = act ? ld32(ws, off) : 1u;
    if (__all(v != 0u)) break;
  }
}
DI unsigned mk_key(float v, unsigned idx) {
  const unsigned u = __float_as_uint(v) >> 16;
  const unsigned o = (u & 0x8000u) ? (~u & 0xffffu) : (u | 0x8000u);
  return (o << 16) | (0xFFFFu - idx);
}
DI unsigned key_idx(unsigned k) { return 0xFFFFu - (k & 0xFFFFu); }
DI void top2_mix(unsigned& b0, unsigned& b1, unsigned c0, unsigned c1) {
  const unsigned lo = min(b0, c0), mx = max(b1, c1);
  b0 = max(b0, c0); b1 = max(lo, mx);
}
DI void top2_wave(unsigned& b0, unsigned& b1) {
  top2_mix(b0, b1, dppu<DPP_X1>(b0), dppu<DPP_X1>(b1));
  top2_mix(b0, b1, dppu<DPP_X2>(b0), dppu<DPP_X2>(b1));
  top2_mix(b0, b1, dppu<DPP_HMIR>(b0), dppu<DPP_HMIR>(b1));
  top2_mix(b0, b1, dppu<DPP_MIR>(b0), dppu<DPP_MIR>(b1));
  { auto p = __builtin_amdgcn_permlane16_swap(b0, b0, false, false); auto q = __builtin_amdgcn_permlane16_swap(b1, b1, false, false);
    b0 = p[0]; b1 = q[0]; top2_mix(b0, b1, p[1], q[1]); }
  { auto p = __builtin_amdgcn_permlane32_swap(b0, b0, false, false); auto q = __builtin_amdgcn_permlane32_swap(b1, b1, false, false);
    b0 = p[0]; b1 = q[0]; top2_mix(b0, b1, p[1], q[1]); }
}

constexpr int K1_STAGE = 40960;
constexpr int LDS_TOTAL = 4 * K1_STAGE;
constexpr int K2_STAGE = 24576;
constexpr int L_KVL = 147456, L_ZL = L_KVL + 4096, L_MISC = L_ZL + 256;

__global__ __launch_bounds__(512, 2) void fused(const u16* x_, const u16* wqkv_, const u16* wo_, const u16* w_r_, const u16* wg_, const u16* wu_, const u16* wd_, u16* out_, char* ws_) {
  u64 pt_ = 0;
  typedef __attribute__((address_space(4))) const u64* kptr;
  kptr kp = (kptr)__builtin_amdgcn_kernarg_segment_ptr();
  asm volatile("s_memrealtime %0\n\ts_waitcnt lgkmcnt(0)" : "=s"(pt_), "+s"(kp) :: "memory");
  const u16* __restrict__ x = (const u16*)(GAS const u16*)kp[0]; const u16* __restrict__ wqkv = (const u16*)(GAS const u16*)kp[1]; const u16* __restrict__ wo = (const u16*)(GAS const u16*)kp[2];
  const u16* __restrict__ w_r = (const u16*)(GAS const u16*)kp[3]; const u16* __restrict__ wg = (const u16*)(GAS const u16*)kp[4]; const u16* __restrict__ wu = (const u16*)(GAS const u16*)kp[5];
  const u16* __restrict__ wd = (const u16*)(GAS const u16*)kp[6]; u16* __restrict__ out = (u16*)(GAS u16*)kp[7]; char* __restrict__ ws = (char*)(GAS char*)kp[8];
  extern __shared__ __attribute__((aligned(16))) char smem[];

  { float dz = 0.f; asm volatile("" : "+a"(dz)); }
  int tid = threadIdx.x, lane = tid & 63;
  const int wave = __builtin_amdgcn_readfirstlane(tid >> 6);
  const int g = wave >> 2, wr = (wave >> 1) & 1, wc = wave & 1;
  const int bid = blockIdx.x, xcd = bid & 7, idx = bid >> 3;
  const int rb = (xcd & 1) * 8 + (idx & 7);
  const int hp = (xcd >> 1) * 4 + (idx >> 3);
  const int rp = xcd * 4 + (idx >> 3), cb = idx & 7;
  int r = lane & 31, h = lane >> 5;
  const unsigned lds0 = (unsigned)(size_t)smem;
  const __amdgpu_buffer_rsrc_t wsr = __builtin_amdgcn_make_buffer_rsrc(ws, 0, WS_END, 0x00020000);
#define LDW(off) __builtin_amdgcn_raw_buffer_load_b128(wsr, (off), 0, 16)
#define STW(v, off) __builtin_amdgcn_raw_buffer_store_b128((v), wsr, (off), 0, 16)

  unsigned hs_cur = 0, hs_g0 = 0; int hs_state = 0;
  u32x4 ms_exp = {0, 0, 0, 0}; bool ms_done = false;
  const unsigned hs_off = (lane == 1 ? SY_GO : SY_CH) + bid * 4;
  const unsigned ms_off = lane * 16;
  const char* wsu = uni(ws);
#define HS_PROBE() asm volatile("global_load_dword a119, %0, %1 sc1" :: "v"(hs_off), "s"(wsu) : "memory", RSV)
#define MS_PROBE() asm volatile("global_load_dwordx4 a[124:127], %0, %1 offset:2048 sc1" :: "v"(ms_off), "s"(wsu) : "memory", RSV)
  const bool is_master = (bid == 0) && (wave == 1);
  auto hs_begin = [&]() {
    unsigned zo = SY_FA + hp * 256 + rb * 4;
    if (lane == 1) zo = SY_FB + rb * 256 + hp * 4;
    if (lane == 2) zo = SY_FC + rp * 256 + cb * 4;
    if (lane == 3) zo = SY_FE + bid * 4;
    if (lane >= 4) zo = SY_FD + cb * 512 + rp * 8 + (lane & 1) * 4;
    if (lane < 6) st32(ws, zo, 0u);
    STW(((u32x4){0u, 0u, 0u, 0u}), WS_ST2 + (rp * 8 + cb) * 1024 + lane * 16);
  };
  if (is_master) {
    MS_PROBE();
    asm volatile("global_load_dwordx4 a[120:123], %0, %1 offset:1024 sc1" :: "v"(ms_off), "s"(wsu) : "memory", RSV);
  }
  auto hs_step = [&](bool first, bool mfirst) {
    if (wave == 0 && !mfirst) {
      unsigned hv_;
      asm volatile("v_accvgpr_read_b32 %0, a119" : "=v"(hv_) :: RSV);
      const unsigned c = __builtin_amdgcn_readlane(hv_, 0), go = __builtin_amdgcn_readlane(hv_, 1);
      if (first) {
        hs_cur = c;
        if (lane == 0) st32(ws, SY_RE + bid * 4, c);
      } else if (hs_state == 0) {
        if (go == hs_cur && go != hs_g0) hs_state = 2;
        else if (c != hs_cur) { hs_cur = c; if (lane == 0) st32(ws, SY_RE + bid * 4, c); }
      }
      if (hs_state < 2) HS_PROBE();
    }
    if (is_master && !ms_done) {
      u32x4 re;
      asm volatile("v_accvgpr_read_b32 %0, a124\n\tv_accvgpr_read_b32 %1, a125\n\tv_accvgpr_read_b32 %2, a126\n\tv_accvgpr_read_b32 %3, a127"
                   : "=v"(re[0]), "=v"(re[1]), "=v"(re[2]), "=v"(re[3]) :: RSV);
      if (mfirst) {
        u32x4 g0;
        asm volatile("v_accvgpr_read_b32 %0, a120\n\tv_accvgpr_read_b32 %1, a121\n\tv_accvgpr_read_b32 %2, a122\n\tv_accvgpr_read_b32 %3, a123"
                     : "=v"(g0[0]), "=v"(g0[1]), "=v"(g0[2]), "=v"(g0[3]) :: RSV);
#pragma unroll
        for (int j = 0; j < 4; ++j) { const unsigned c = re[j] + 1u; ms_exp[j] = (c == g0[j]) ? c + 1u : c; }
        STW(ms_exp, SY_CH + ms_off);
      } else if (__all(re[0] == ms_exp[0] && re[1] == ms_exp[1] && re[2] == ms_exp[2] && re[3] == ms_exp[3])) {
        STW(ms_exp, SY_GO + ms_off);
        ms_done = true;
      }
      if (!ms_done) MS_PROBE();
    }
  };

  auto hs_pre = [&]() {
    if (wave == 0) {
      unsigned hv_;
      asm volatile("v_accvgpr_read_b32 %0, a119" : "=v"(hv_) :: RSV);
      hs_g0 = __builtin_amdgcn_readlane(hv_, 1);
      HS_PROBE();
    }
  };
  auto stat_dma = [&]() {
    int t2 = threadIdx.x; asm volatile("" : "+v"(t2));
    const unsigned vo = (t2 & 63) * 16;
#pragma unroll
    for (int i = 0; i < 2; ++i)
      asm volatile("s_mov_b32 m0, %2\n\ts_nop 0\n\tglobal_load_lds_dwordx4 %0, %1 sc1" ::"v"(vo), "s"(uni(ws + WS_RS + rb * 16384 + (wave * 2 + i) * 1024)),
                   "s"(__builtin_amdgcn_readfirstlane(lds0 + 98304 + (wave * 2 + i) * 1024)) : "memory", RSV);
#pragma unroll
    for (int i = 0; i < 2; ++i)
      if (wave * 2 + i < 12)
        asm volatile("s_mov_b32 m0, %2\n\ts_nop 0\n\tglobal_load_lds_dwordx4 %0, %1 sc1" ::"v"(vo), "s"(uni(ws + WS_CS + hp * 12288 + (wave * 2 + i) * 1024)),
                     "s"(__builtin_amdgcn_readfirstlane(lds0 + 114688 + (wave * 2 + i) * 1024)) : "memory", RSV);
  };
  const int sigma = (rb + hp) & 15;
  const int krot = KROT * xcd, sg = (sigma - krot) & 15;
  auto korder = [&](int t) { return ((t < 2 ? (t < sg ? t : t + 1) : t == 2 ? sg : (t - 1 < sg ? t - 1 : t)) + krot) & 15; };
#define RS_SLOT(T) (WS_RS + ((rb * 16 + sigma) * 128 + ((T) >> 2)) * 8)
#define CS_SLOT(T) (WS_CS + ((hp * 16 + sigma) * 192 + wave * 32 + ((T) & 31)) * 4)

  unsigned voA0, voA1, voB;
  {
    const int r0 = 16 * wave + (lane >> 3), r1 = r0 + 8;
    const int c0 = (lane & 7) ^ ((r0 >> 1) & 7), c1 = (lane & 7) ^ ((r1 >> 1) & 7);
    voA0 = (unsigned)(r0 * D_ + c0 * 8) * 2u;
    voA1 = (unsigned)(r1 * D_ + c1 * 8) * 2u;
    const int bi = lane >> 4, q = (lane >> 2) & 3, ch = lane & 3;
    voB = (unsigned)((8 * wave + 4 * (bi >> 1) + q) * (3 * D_) + (bi & 1) * 32 + ch * 8) * 2u;
  }
  const char* baseA = uni(x + (size_t)rb * 128 * D_);
  const char* baseB = uni(wqkv + hp * 64);
  const int ft = wc ^ g;
  int trl = ((lane & 15) >> 2) * 64 + ((lane >> 4) & 1) * 32 + (lane & 3) * 8;
  f32x16 acc[2][3];
  {
    auto stage = [&](int t, int sb) {
      const int kt = korder(t);
      const unsigned l = lds0 + sb;
      const char* pa = baseA + kt * 128;
      const char* pb = baseB + (size_t)kt * (64 * 3 * D_ * 2);
      glds16r(voA0, pa, l + (2 * wave) * 1024);
      glds16r(voA1, pa, l + (2 * wave + 1) * 1024);
      glds16r(voB, pb, l + 16384 + (3 * wave) * 1024);
      glds16r(voB + 2048, pb, l + 16384 + (3 * wave + 1) * 1024);
      glds16r(voB + 4096, pb, l + 16384 + (3 * wave + 2) * 1024);
    };
    const int ka = 4 * g + h, kb = 4 * g + 2 + h;
    const int aF0 = (64 * wr + 32 * ft + r) * 128 + ((ka ^ ((r >> 1) & 7)) * 16);
    const int aF1 = (64 * wr + 32 * ft + r) * 128 + ((kb ^ ((r >> 1) & 7)) * 16);
    const int aS0 = aF0 ^ 4096, aS1 = aF1 ^ 4096;
    const int bo0 = 16384 + wc * 256 + trl + ka * 3072, bo1 = 16384 + wc * 256 + trl + kb * 3072;
#pragma unroll
    for (int i = 0; i < 2; ++i)
#pragma unroll
      for (int j = 0; j < 3; ++j)
#pragma unroll
        for (int k = 0; k < 16; ++k) acc[i][j][k] = 0.f;
    struct Frag { u32x4 a[2][2]; u32x2 bl_[2][3], bh_[2][3]; };
    Frag FA, FB;
#define K1_B(F, kl, t) ((u32x4){F.bl_[kl][t][0], F.bl_[kl][t][1], F.bh_[kl][t][0], F.bh_[kl][t][1]})
    auto body = [&](Frag& C, Frag& N, int sn, int t4, int sb, bool ST) {
      const char* nb = smem + sn;
      const int kt = korder(t4 & 15);
      const unsigned l = lds0 + sb;
      const char* pa = baseA + kt * 128;
      const char* pb = baseB + (size_t)kt * (64 * 3 * D_ * 2);
      acc[0][0] = mfma32(K1_B(C, 0, 0), C.a[0][0], acc[0][0]);
      if (ST) glds16r(voA0, pa, l + (2 * wave) * 1024);
      N.a[0][0] = *(const u32x4*)(nb + aF0); N.bl_[0][0] = trread(nb + bo0);
      SBR();
      acc[0][1] = mfma32(C.a[0][0], K1_B(C, 0, 1), acc[0][1]);
      if (ST) glds16r(voA1, pa, l + (2 * wave + 1) * 1024);
      N.bh_[0][0] = trread(nb + bo0 + 512); N.bl_[0][1] = trread(nb + bo0 + 1024);
      SBR();
      acc[0][2] = mfma32(C.a[0][0], K1_B(C, 0, 2), acc[0][2]);
      if (ST) glds16r(voB, pb, l + 16384 + (3 * wave) * 1024);
      N.bh_[0][1] = trread(nb + bo0 + 1536);
      SBR();
      acc[1][0] = mfma32(K1_B(C, 0, 0), C.a[0][1], acc[1][0]);
      if (ST) glds16r(voB + 2048, pb, l + 16384 + (3 * wave + 1) * 1024);
      N.bl_[0][2] = trread(nb + bo0 + 2048);
      SBR();
      acc[1][1] = mfma32(C.a[0][1], K1_B(C, 0, 1), acc[1][1]);
      if (ST) glds16r(voB + 4096, pb, l + 16384 + (3 * wave + 2) * 1024);
      N.bh_[0][2] = trread(nb + bo0 + 2560);
      SBR();
      acc[1][2] = mfma32(C.a[0][1], K1_B(C, 0, 2), acc[1][2]);
      N.a[0][1] = *(const u32x4*)(nb + aS0); N.a[1][0] = *(const u32x4*)(nb + aF1);
      SBR();
      acc[0][0] = mfma32(K1_B(C, 1, 0), C.a[1][0], acc[0][0]);
      N.bl_[1][0] = trread(nb + bo1); N.bh_[1][0] = trread(nb + bo1 + 512);
      SBR();
      acc[0][1] = mfma32(C.a[1][0], K1_B(C, 1, 1), acc[0][1]);
      N.bl_[1][1] = trread(nb + bo1 + 1024); N.bh_[1][1] = trread(nb + bo1 + 1536);
      SBR();
      acc[0][2] = mfma32(C.a[1][0], K1_B(C, 1, 2), acc[0][2]);
      N.bl_[1][2] = trread(nb + bo1 + 2048); N.bh_[1][2] = trread(nb + bo1 + 2560);
      SBR();
      acc[1][0] = mfma32(K1_B(C, 1, 0), C.a[1][1], acc[1][0]);
      N.a[1][1] = *(const u32x4*)(nb + aS1);
      SBR();
      acc[1][1] = mfma32(C.a[1][1], K1_B(C, 1, 1), acc[1][1]);
      acc[1][2] = mfma32(C.a[1][1], K1_B(C, 1, 2), acc[1][2]);
      SBR();
    };
    u64 st_row = 0; unsigned st_col = 0;
    auto stats = [&](int sn) {
      const char* nb = smem + sn;
      int t2 = threadIdx.x; asm volatile("" : "+v"(t2));
      {
        const int row = t2 >> 2, q = t2 & 3, key = (row >> 1) & 7;
        const u32x4 v0 = *(const u32x4*)(nb + row * 128 + (((2 * q) ^ key) * 16));
        const u32x4 v1 = *(const u32x4*)(nb + row * 128 + (((2 * q + 1) ^ key) * 16));
        float s = 0.f, sq = 0.f;
#pragma unroll
        for (int i = 0; i < 4; ++i) {
          const float a0 = bflo(v0[i]), a1 = bfhi(v0[i]), a2 = bflo(v1[i]), a3 = bfhi(v1[i]);
          s += (a0 + a1) + (a2 + a3);
          sq = __builtin_fmaf(a0, a0, sq); sq = __builtin_fmaf(a1, a1, sq); sq = __builtin_fmaf(a2, a2, sq); sq = __builtin_fmaf(a3, a3, sq);
        }
        s = quad_sum(s); sq = quad_sum(sq);
        st_row = ((u64)__float_as_uint(sq) << 32) | __float_as_uint(s);
      }
      if (wave < 6) {
        const int cw = wave >= 3 ? 1 : 0, ct = wave - 3 * cw;
        const u32x4 ONES4 = {0x3F803F80u, 0x3F803F80u, 0x3F803F80u, 0x3F803F80u};
        f32x16 ca;
#pragma unroll
        for (int k = 0; k < 16; ++k) ca[k] = 0.f;
#pragma unroll
        for (int ks = 0; ks < 4; ++ks) {
          const char* bp = nb + 16384 + (2 * ks + ((t2 >> 5) & 1)) * 3072 + ct * 1024 + cw * 256 + (((t2 & 15) >> 2) * 64 + ((t2 >> 4) & 1) * 32 + (t2 & 3) * 8);
          const u32x2 lo = trread(bp), hi = trread(bp + 512);
          ca = mfma32(ONES4, (u32x4){lo[0], lo[1], hi[0], hi[1]}, ca);
        }
        st_col = __float_as_uint(ca[0]);
      }
    };

    stage(0, 0); stage(1, K1_STAGE); stage(2, 2 * K1_STAGE); stage(3, 3 * K1_STAGE);
    if (wave == 0) HS_PROBE();
    if (wave == 7) {
      u64 pc; asm volatile("s_getpc_b64 %0" : "=s"(pc));
      const char* cp = (const char*)(pc - (pc & 127)) + (idx * 10 + (lane < 10 ? lane : 0)) * 128;
      asm volatile("global_load_dword a122, %0, off" :: "v"(cp) : "memory", RSV);
    }
    WAIT_VR(15); RAW_BARR();
    hs_step(false, true);
    STAMPT(0, 1, 0);
    FA.a[0][0] = *(const u32x4*)(smem + aF0); FA.a[0][1] = *(const u32x4*)(smem + aS0);
    FA.a[1][0] = *(const u32x4*)(smem + aF1); FA.a[1][1] = *(const u32x4*)(smem + aS1);
#pragma unroll
    for (int t = 0; t < 3; ++t) {
      FA.bl_[0][t] = trread(smem + bo0 + t * 1024); FA.bh_[0][t] = trread(smem + bo0 + t * 1024 + 512);
      FA.bl_[1][t] = trread(smem + bo1 + t * 1024); FA.bh_[1][t] = trread(smem + bo1 + t * 1024 + 512);
    }
    for (int t = 0; t < 16; t += 4) {
      const bool st = t < 12;
      WAIT_VR(10);
      RAW_BARR(); if (t) hs_step(t == 4, false);
      body(FA, FB, 1 * K1_STAGE, t + 4, 0 * K1_STAGE, st);
      if (st) WAIT_VR(10); else WAIT_VR(5);
      RAW_BARR();
      body(FB, FA, 2 * K1_STAGE, t + 5, 1 * K1_STAGE, st);
      if (t == 0) {
        stats(2 * K1_STAGE);
        int t2 = threadIdx.x; asm volatile("" : "+v"(t2));
        if ((t2 & 3) == 0) st64(ws, RS_SLOT(t2), st_row);
        if (wave < 6 && (t2 & 63) < 32) st32(ws, CS_SLOT(t2), st_col);
        if (wave == 0) hs_begin();
      }
      if (st) WAIT_VR(10); else WAIT_VR(0);
      RAW_BARR(); if (t) hs_step(false, false); else hs_pre();
      body(FA, FB, 3 * K1_STAGE, t + 6, 2 * K1_STAGE, st);
      if (st) WAIT_VR(10); else WAIT_VR(0);
      RAW_BARR();
      if (!st) stat_dma();
      body(FB, FA, 0 * K1_STAGE, t + 7, 3 * K1_STAGE, st);
    }
  }
  STAMPT(0, 2, 0);
  tid = threadIdx.x; asm volatile("" : "+v"(tid));
  lane = tid & 63; r = lane & 31; h = lane >> 5;
  trl = ((lane & 15) >> 2) * 64 + ((lane >> 4) & 1) * 32 + (lane & 3) * 8;
  float* xch = (float*)smem;
  float* stm = (float*)(smem + 144384);
  float* kvr = (float*)(smem + 98304);
  float* zr = (float*)(smem + 143360);
  volatile int* misc = (volatile int*)(smem + L_MISC);
  const int R0 = 64 * wr + 32 * ft;
  auto put_stats = [&]() {
    if (wave < 2) {
      float s = 0.f, sq = 0.f;
#pragma unroll
      for (int i = 0; i < 16; ++i) { const f32x2 v = *(const f32x2*)(smem + 98304 + i * 1024 + tid * 8); s += v[0]; sq += v[1]; }
      const float mean = s * (1.0f / 1024.0f);
      const float var = fmaxf(sq * (1.0f / 1024.0f) - mean * mean, 0.f);
      stm[tid] = rbf(mean);
      stm[128 + tid] = __builtin_amdgcn_rcpf(rbf(__builtin_amdgcn_sqrtf(rbf(rbf(var) + 1e-5f))));
    } else if (wave < 5) {
      float c = 0.f;
#pragma unroll
      for (int i = 0; i < 16; ++i) c += *(const float*)(smem + 114688 + i * 768 + (tid - 128) * 4);
      stm[256 + tid - 128] = c;
    }
  };
#pragma unroll
  for (int t = 0; t < 3; ++t)
#pragma unroll
    for (int q4 = 0; q4 < 4; ++q4)
      *(f32x4*)(xch + wave * 3072 + (t * 4 + q4) * 256 + lane * 4) =
          (f32x4){acc[1][t][4 * q4], acc[1][t][4 * q4 + 1], acc[1][t][4 * q4 + 2], acc[1][t][4 * q4 + 3]};
  if (tid == 0) misc[0] = hs_state;
  if (is_master && !ms_done) {
    for (unsigned s = 0; s < SPIN_CAP; ++s) {
      bool ok = true;
#pragma unroll
      for (int j = 0; j < 4; ++j) ok &= (ld32(ws, SY_RE + lane * 16 + j * 4) == ms_exp[j]);
      if (__all(ok)) break;
    }
    STW(ms_exp, SY_GO + lane * 16);
  }
  if (wave == 0 && hs_state < 2) {
    if (hs_state == 0) {
      for (unsigned s = 0; s < SPIN_CAP; ++s) {
        const unsigned go = ld32(ws, SY_GO + bid * 4);
        if (go == hs_cur && go != hs_g0) break;
        const unsigned c = ld32(ws, SY_CH + bid * 4);
        if (c != hs_cur) { hs_cur = c; if (lane == 0) st32(ws, SY_RE + bid * 4, c); }
      }
    }
  }
  WAIT_V(0);
  STAMPT(1, 0, 0);
  RAW_BAR();
  if (misc[0] != 2) { stat_dma(); WAIT_V(0); RAW_BAR(); }
  put_stats();
  RAW_BAR();
  STAMPT(1, 1, 0);
  if (wave >= 4) {
    const u32x4 z4 = {0u, 0u, 0u, 0u};
    const int sl_ = (bid & 7) * 28 + (bid >> 3);
    if (bid < 224) { STW(z4, WS_YP + sl_ * 8192 + (tid - 256) * 16); STW(z4, WS_YP + sl_ * 8192 + 4096 + (tid - 256) * 16); }
    if (wave == 4) STW(z4, WS_GU + sl_ * 1024 + lane * 16);
    if (wave >= 6) {
#pragma unroll
      for (int rr_ = 0; rr_ < XSR; ++rr_) STW(z4, WS_XS + rr_ * 524288 + rp * 16384 + (wave - 6) * 8192 + cb * 1024 + lane * 16);
    }
  }
  const float m_b = stm[R0 + r], rinv = stm[128 + R0 + r], csk = stm[256 + wc * 96 + 32 + r], csv = stm[256 + wc * 96 + 64 + r];
  STAMPT(0, 3, 0);
  const int head = 2 * hp + wc;
  const int rowtile = rb * 4 + 2 * wr + ft;
  unsigned qp[8];
  f32x4 tq[4];
  {
    unsigned kp[8], vp[8];
    u16x2 tmin = {0, 0};
    const float* px = xch + (wave ^ 4) * 3072 + lane * 4;
#pragma unroll
    for (int q4 = 0; q4 < 4; ++q4) {
      const f32x4 xq = *(const f32x4*)(px + q4 * 256);
      const f32x4 xk = *(const f32x4*)(px + (4 + q4) * 256);
      const f32x4 xv = *(const f32x4*)(px + (8 + q4) * 256);
      tq[q4] = (f32x4){acc[0][0][4 * q4], acc[0][0][4 * q4 + 1], acc[0][0][4 * q4 + 2], acc[0][0][4 * q4 + 3]} + xq;
      const f32x4 m4 = *(const f32x4*)(stm + R0 + 8 * q4 + 4 * h);
      const f32x4 i4 = *(const f32x4*)(stm + 128 + R0 + 8 * q4 + 4 * h);
      const f32x2 ck2 = {csk, csk}, cv2 = {csv, csv};
      const f32x2 ma = {m4[0], m4[1]}, mb = {m4[2], m4[3]}, ia = {i4[0], i4[1]}, ib = {i4[2], i4[3]};
      f32x2 k0 = (((f32x2){acc[0][1][4 * q4], acc[0][1][4 * q4 + 1]} + (f32x2){xk[0], xk[1]}) - ma * ck2) * ia;
      f32x2 k1 = (((f32x2){acc[0][1][4 * q4 + 2], acc[0][1][4 * q4 + 3]} + (f32x2){xk[2], xk[3]}) - mb * ck2) * ib;
      kp[2 * q4] = phi_pair(k0, tmin); kp[2 * q4 + 1] = phi_pair(k1, tmin);
      f32x2 v0 = (((f32x2){acc[0][2][4 * q4], acc[0][2][4 * q4 + 1]} + (f32x2){xv[0], xv[1]}) - ma * cv2) * ia;
      f32x2 v1 = (((f32x2){acc[0][2][4 * q4 + 2], acc[0][2][4 * q4 + 3]} + (f32x2){xv[2], xv[3]}) - mb * cv2) * ib;
      vp[2 * q4] = pack2(v0[0], v0[1]); vp[2 * q4 + 1] = pack2(v1[0], v1[1]);
    }
    if (__any(tmin[0] > 0xC0F0 || tmin[1] > 0xC0F0)) {
#pragma unroll
      for (int i = 0; i < 8; ++i) kp[i] = pack2(bflo(kp[i]) + 1e-6f, bfhi(kp[i]) + 1e-6f);
    }
    f32x16 kv, zv;
#pragma unroll
    for (int k = 0; k < 16; ++k) { kv[k] = 0.f; zv[k] = 0.f; }
    const u32x4 ONES4 = {0x3F803F80u, 0x3F803F80u, 0x3F803F80u, 0x3F803F80u};
    kv = mfma32((u32x4){kp[0], kp[1], kp[2], kp[3]}, (u32x4){vp[0], vp[1], vp[2], vp[3]}, kv);
    zv = mfma32((u32x4){kp[0], kp[1], kp[2], kp[3]}, ONES4, zv);
    kv = mfma32((u32x4){kp[4], kp[5], kp[6], kp[7]}, (u32x4){vp[4], vp[5], vp[6], vp[7]}, kv);
    zv = mfma32((u32x4){kp[4], kp[5], kp[6], kp[7]}, ONES4, zv);
#pragma unroll
    for (int q4 = 0; q4 < 4; ++q4)
      *(f32x4*)(kvr + wave * 1024 + q4 * 256 + lane * 4) = (f32x4){kv[4 * q4], kv[4 * q4 + 1], kv[4 * q4 + 2], kv[4 * q4 + 3]};
    if (r == 0) {
#pragma unroll
      for (int q4 = 0; q4 < 4; ++q4)
        *(f32x4*)(zr + wave * 32 + 8 * q4 + 4 * h) = (f32x4){zv[4 * q4], zv[4 * q4 + 1], zv[4 * q4 + 2], zv[4 * q4 + 3]};
    }
  }
  STAMPT(1, 2, 0);
  RAW_BAR();
  STAMPT(1, 3, 0);
  const int hc = tid >> 8, f = tid & 255;
  {
    f32x4 s = *(const f32x4*)(kvr + hc * 1024 + f * 4);
#pragma unroll
    for (int p = 1; p < 4; ++p) s += *(const f32x4*)(kvr + (hc + 2 * p) * 1024 + f * 4);
    const unsigned o_ = WS_KVP + ((hp * 16 + rb) * 2 + hc) * 4224;
    STW(__builtin_bit_cast(u32x4, s), o_ + f * 16);
    if (f < 8) {
      f32x4 z = *(const f32x4*)(zr + hc * 32 + f * 4);
#pragma unroll
      for (int p = 1; p < 4; ++p) z += *(const f32x4*)(zr + (hc + 2 * p) * 32 + f * 4);
      STW(__builtin_bit_cast(u32x4, z), o_ + 4096 + f * 16);
    }
  }
  WAIT_V(0);
  STAMPT(1, 4, 0);
  RAW_BAR();
  if (tid == 0) st32(ws, SY_FA + hp * 256 + rb * 4, 1u);
  STAMPT(0, 4, 0);

  const int rt = wave & 1, kh = (wave >> 1) & 1;
  unsigned voW;
  {
    const int bi = lane >> 4, q = (lane >> 2) & 3, ch = lane & 3;
    voW = (unsigned)((4 * (bi >> 1) + q) * D_ + (bi & 1) * 32 + ch * 8) * 2u;
  }
  const unsigned voL = lane * 16;
  const char* baseW = uni(wo + (size_t)((16 * kh) * 32 + 16 * rt) * D_ + cb * 128);
  const char* baseO = uni(ws + WS_O + (size_t)(((rp * 2 + rt) * 32 + 16 * kh) * 2) * 1024);
  auto stageW = [&](int t, int sb) {
    const char* sw = baseW + (size_t)t * (32 * D_ * 2);
    const unsigned dw = lds0 + sb + kh * 8192 + (2 * rt) * 2048;
    glds16(voW, sw, dw);
    glds16(voW + 128, sw, dw + 1024);
    glds16(voW + 8 * D_ * 2, sw, dw + 2048);
    glds16(voW + 8 * D_ * 2 + 128, sw, dw + 3072);
  };
  auto stageO = [&](int t, int sb) {
    const char* sp = baseO + (size_t)t * 2048;
    glds16_sc1(voL, sp, lds0 + sb + 16384 + wave * 2048);
    glds16_sc1(voL + 1024, sp, lds0 + sb + 16384 + wave * 2048 + 1024);
  };
  if (wave < 4) {
#pragma unroll
    for (int t = 0; t < 6; ++t) stageW(t, t * K2_STAGE);
  }

  {
    u16x2 tmin = {0, 0};
    const f32x2 mb2 = {m_b, m_b}, ri2 = {rinv, rinv};
#pragma unroll
    for (int q4 = 0; q4 < 4; ++q4) {
      const f32x4 c4 = *(const f32x4*)(stm + 256 + wc * 96 + 8 * q4 + 4 * h);
      const f32x2 t0 = ((f32x2){tq[q4][0], tq[q4][1]} - mb2 * (f32x2){c4[0], c4[1]}) * ri2;
      const f32x2 t1 = ((f32x2){tq[q4][2], tq[q4][3]} - mb2 * (f32x2){c4[2], c4[3]}) * ri2;
      qp[2 * q4] = phi_pair(t0, tmin); qp[2 * q4 + 1] = phi_pair(t1, tmin);
    }
    if (__any(tmin[0] > 0xC0F0 || tmin[1] > 0xC0F0)) {
#pragma unroll
      for (int i = 0; i < 8; ++i) qp[i] = pack2(bflo(qp[i]) + 1e-6f, bfhi(qp[i]) + 1e-6f);
    }
#pragma unroll
    for (int i = 0; i < 8; ++i) asm volatile("" : "+v"(qp[i]));
  }
  if (wave == 4) poll_flags(ws, SY_FA + hp * 256 + (lane & 15) * 4, true);
  RAW_BAR();
  STAMPT(0, 5, 0);
  {
    u32x4 pv[16];
    const unsigned o_ = WS_KVP + (hp * 16 * 2 + hc) * 4224 + f * 16;
#pragma unroll
    for (int p = 0; p < 16; ++p) pv[p] = LDW(o_ + p * 8448);
    f32x4 zs = {0.f, 0.f, 0.f, 0.f};
    if (f < 8) {
#pragma unroll
      for (int p = 0; p < 16; ++p) zs += __builtin_bit_cast(f32x4, LDW(o_ + 4096 + p * 8448));
    }
    f32x4 s = __builtin_bit_cast(f32x4, pv[0]);
#pragma unroll
    for (int p = 1; p < 16; ++p) s += __builtin_bit_cast(f32x4, pv[p]);
    asm volatile("" : "+v"(s));
    STAMPT(1, 5, 0);
    const int q4 = f >> 6, ln = f & 63;
    *(u32x2*)(smem + L_KVL + ((hc * 2 + (q4 >> 1)) * 64 + ln) * 16 + (q4 & 1) * 8) = (u32x2){pack2(s[0], s[1]), pack2(s[2], s[3])};
    if (f < 8)
      *(u32x2*)(smem + L_ZL + hc * 64 + (f & 1) * 32 + (f >> 1) * 8) = (u32x2){pack2(zs[0], zs[1]), pack2(zs[2], zs[3])};
  }
  RAW_BAR();
  {
    const u32x4 k0 = *(const u32x4*)(smem + L_KVL + (wc * 2) * 1024 + lane * 16);
    const u32x4 k1 = *(const u32x4*)(smem + L_KVL + (wc * 2 + 1) * 1024 + lane * 16);
    const u32x4 z0 = *(const u32x4*)(smem + L_ZL + wc * 64 + h * 32), z1 = *(const u32x4*)(smem + L_ZL + wc * 64 + h * 32 + 16);
    f32x16 X, Dn;
#pragma unroll
    for (int k = 0; k < 16; ++k) { X[k] = 0.f; Dn[k] = 0.f; }
    const u32x4 p0 = {qp[0], qp[1], qp[2], qp[3]}, p1 = {qp[4], qp[5], qp[6], qp[7]};
    X = mfma32(k0, p0, X); Dn = mfma32(z0, p0, Dn);
    X = mfma32(k1, p1, X); Dn = mfma32(z1, p1, Dn);
    const float ri = __builtin_amdgcn_rcpf(rbf(rbf(Dn[0]) + 1e-6f));
    u32x4 o0, o1;
#pragma unroll
    for (int i = 0; i < 4; ++i) {
      o0[i] = pack2(rbf(X[2 * i]) * ri, rbf(X[2 * i + 1]) * ri);
      o1[i] = pack2(rbf(X[8 + 2 * i]) * ri, rbf(X[8 + 2 * i + 1]) * ri);
    }
    const unsigned oo = WS_O + ((rowtile * 32 + head) * 2) * 1024 + lane * 16;
    STAMPT(1, 6, 0);
    STW(o0, oo); STW(o1, oo + 1024);
  }
  WAIT_V(0);
  STAMPT(1, 7, 0);
  RAW_BAR();
  if (tid == 0) st32(ws, SY_FB + rb * 256 + hp * 4, 1u);
  STAMPT(0, 6, 0);

  const bool has_mlp = bid < 224;
  float* xs = (float*)smem;
  float* hs = (float*)(smem + 8192);
  float* red = (float*)(smem + 28672);
  float* act = (float*)(smem + 17408);
  float* sgm = (float*)(smem + 17536);
  float* yh = (float*)(smem + 18432);

  if (wave < 4) {
    poll_flags(ws, SY_FB + (rp >> 1) * 256 + (lane & 15) * 4, wave == 0);
    RAW_BAR();
    STAMPT(0, 7, 0);
    float hv[32];
    const int erow = tid >> 2, eseg = tid & 3;
    const size_t ego = (size_t)(rp * 64 + erow) * D_ + cb * 128 + eseg * 32;
#pragma unroll
    for (int t = 0; t < 6; ++t) stageO(t, t * K2_STAGE);
    f32x16 a2[4];
#pragma unroll
    for (int i = 0; i < 4; ++i)
#pragma unroll
      for (int k = 0; k < 16; ++k) a2[i][k] = 0.f;
    const int woff = kh * 8192 + h * 512 + trl;
    const int poff = 16384 + wave * 2048 + lane * 16;
    struct Frag2 { u32x4 o[2]; u32x2 lo[2][4], hi[2][4]; };
    Frag2 GA, GB;
    auto ld2 = [&](Frag2& N, int sn, int st, int nt) {
      const char* base = smem + sn + woff;
      N.lo[st][nt] = trread(base + ((2 * st) * 2 + (nt >> 1)) * 1024 + (nt & 1) * 256);
      N.hi[st][nt] = trread(base + ((2 * st + 1) * 2 + (nt >> 1)) * 1024 + (nt & 1) * 256);
    };
    auto body2 = [&](Frag2& C, Frag2& N, int t, bool LD) {
      const int sn = ((t + 1) % 6) * K2_STAGE, sb = (t % 6) * K2_STAGE;
      const bool ST = t + 6 < 16;
      const char* sw = baseW + (size_t)(t + 6) * (32 * D_ * 2);
      const char* sp = baseO + (size_t)(t + 6) * 2048;
      const unsigned dw = lds0 + sb + kh * 8192 + (2 * rt) * 2048, dp = lds0 + sb + 16384 + wave * 2048;
#pragma unroll
      for (int st = 0; st < 2; ++st)
#pragma unroll
        for (int nt = 0; nt < 4; ++nt) {
          const int i = st * 4 + nt;
          a2[nt] = mfma32((u32x4){C.lo[st][nt][0], C.lo[st][nt][1], C.hi[st][nt][0], C.hi[st][nt][1]}, C.o[st], a2[nt]);
          if (ST) {
            if (i == 0) glds16(voW, sw, dw);
            if (i == 1) glds16(voW + 128, sw, dw + 1024);
            if (i == 2) glds16(voW + 8 * D_ * 2, sw, dw + 2048);
            if (i == 3) glds16(voW + 8 * D_ * 2 + 128, sw, dw + 3072);
            if (i == 4) glds16_sc1(voL, sp, dp);
            if (i == 5) glds16_sc1(voL + 1024, sp, dp + 1024);
          }
          if (LD) {
            if (nt == 0) N.o[st] = *(const u32x4*)(smem + sn + poff + st * 1024);
            ld2(N, sn, st, nt);
          }
          SB0();
        }
    };
    WAIT_V(10); RAW_BAR();
    GA.o[0] = *(const u32x4*)(smem + poff); GA.o[1] = *(const u32x4*)(smem + poff + 1024);
#pragma unroll
    for (int st = 0; st < 2; ++st)
#pragma unroll
      for (int nt = 0; nt < 4; ++nt) ld2(GA, 0, st, nt);
    WAIT_V(8);  RAW_BAR(); body2(GA, GB, 0, true);
    WAIT_V(12); RAW_BAR(); body2(GB, GA, 1, true);
    WAIT_V(16); RAW_BAR(); body2(GA, GB, 2, true);
    WAIT_V(20); RAW_BAR(); body2(GB, GA, 3, true);
    WAIT_V(24); RAW_BAR(); body2(GA, GB, 4, true);
    WAIT_V(24); RAW_BAR(); body2(GB, GA, 5, true);
    WAIT_V(24); RAW_BAR(); body2(GA, GB, 6, true);
    WAIT_V(24); RAW_BAR(); body2(GB, GA, 7, true);
    WAIT_V(24); RAW_BAR(); body2(GA, GB, 8, true);
    WAIT_V(24); RAW_BAR(); body2(GB, GA, 9, true);
    WAIT_V(24); RAW_BAR(); body2(GA, GB, 10, true);
    WAIT_V(18); RAW_BAR(); body2(GB, GA, 11, true);
    WAIT_V(12); RAW_BAR(); body2(GA, GB, 12, true);
    WAIT_V(6);  RAW_BAR(); body2(GB, GA, 13, true);
    WAIT_V(0);  RAW_BAR(); body2(GA, GB, 14, true);
    u32x4 xv[4], wv[4];
#pragma unroll
    for (int c = 0; c < 4; ++c) { xv[c] = *(const u32x4*)(x + ego + c * 8); wv[c] = *(const u32x4*)(w_r + cb * 128 + eseg * 32 + c * 8); }
    RAW_BAR();             body2(GB, GA, 15, false);
    STAMPT(0, 8, 0);
    float* P = (float*)smem;
    {
      float* pr = P + (kh * 64 + 32 * rt + r) * 132 + 4 * h;
#pragma unroll
      for (int nt = 0; nt < 4; ++nt)
#pragma unroll
        for (int q4 = 0; q4 < 4; ++q4)
          *(f32x4*)(pr + 32 * nt + 8 * q4) = (f32x4){a2[nt][4 * q4], a2[nt][4 * q4 + 1], a2[nt][4 * q4 + 2], a2[nt][4 * q4 + 3]};
    }
    RAW_BAR();
    STAMPT(1, 8, 0);
    {
      const float* p0 = P + erow * 132 + eseg * 32;
      const float* p1 = p0 + 64 * 132;
      float s = 0.f, pw = 0.f, sw_ = 0.f;
#pragma unroll
      for (int c = 0; c < 4; ++c) {
        f32x4 a0 = *(const f32x4*)(p0 + c * 8), a1 = *(const f32x4*)(p0 + c * 8 + 4);
        a0 += *(const f32x4*)(p1 + c * 8); a1 += *(const f32x4*)(p1 + c * 8 + 4);
        u32x4 hb;
        hb[0] = pack2(bflo(xv[c][0]) + rbf(a0[0]), bfhi(xv[c][0]) + rbf(a0[1]));
        hb[1] = pack2(bflo(xv[c][1]) + rbf(a0[2]), bfhi(xv[c][1]) + rbf(a0[3]));
        hb[2] = pack2(bflo(xv[c][2]) + rbf(a1[0]), bfhi(xv[c][2]) + rbf(a1[1]));
        hb[3] = pack2(bflo(xv[c][3]) + rbf(a1[2]), bfhi(xv[c][3]) + rbf(a1[3]));
#pragma unroll
        for (int i = 0; i < 4; ++i) {
          const float h0 = bflo(hb[i]), h1 = bfhi(hb[i]), w0 = bflo(wv[c][i]), w1 = bfhi(wv[c][i]);
          hv[c * 8 + 2 * i] = h0; hv[c * 8 + 2 * i + 1] = h1;
          s += h0 + h1; pw = __builtin_fmaf(h0, w0, pw); pw = __builtin_fmaf(h1, w1, pw); sw_ += w0 + w1;
        }
      }
      const float cm = quad_sum(s) * (1.0f / 128.0f);
      float m2 = 0.f;
#pragma unroll
      for (int i = 0; i < 32; ++i) { const float d = hv[i] - cm; m2 = __builtin_fmaf(d, d, m2); }
      m2 = quad_sum(m2); pw = quad_sum(pw); sw_ = quad_sum(sw_);
      if (eseg == 0) STW(((u32x4){__float_as_uint(cm), __float_as_uint(m2) | 1u, __float_as_uint(pw), __float_as_uint(sw_) | 1u}),
                         WS_ST2 + ((rp * 8 + cb) * 64 + erow) * 16);
    }
    STAMPT(0, 9, 0);
    int lc0, lc1;
    {
      u32x4 pu_[8];
      for (unsigned s_ = 0; s_ < SPIN_CAP; ++s_) {
        unsigned ok = 1u;
#pragma unroll
        for (int j = 0; j < 8; ++j) { pu_[j] = LDW(WS_ST2 + ((rp * 8 + j) * 64 + lane) * 16); ok &= pu_[j][1] & pu_[j][3]; }
        if (__all(ok & 1u)) break;
        asm volatile("" ::: "memory");
      }
      STAMPT(0, 10, 0);
      float cs = 0.f, m2 = 0.f, pw = 0.f, sw_ = 0.f;
#pragma unroll
      for (int j = 0; j < 8; ++j) { const f32x4 pt = __builtin_bit_cast(f32x4, pu_[j]); cs += pt[0]; m2 += pt[1]; pw += pt[2]; sw_ += pt[3]; }
      const float mean = cs * 0.125f;
      float dd = 0.f;
#pragma unroll
      for (int j = 0; j < 8; ++j) { const float d = __uint_as_float(pu_[j][0]) - mean; dd = __builtin_fmaf(d, d, dd); }
      const float var = (m2 + 128.0f * dd) * (1.0f / 1024.0f);
      const float mb = rbf(mean), sd = rbf(__builtin_amdgcn_sqrtf(rbf(rbf(var) + 1e-5f)));
      float sc = rbf((pw - mb * sw_) / sd);
      if (!(sc == sc)) sc = -INFINITY;
      unsigned b0 = mk_key(sc, rp * 64 + lane), b1 = 0;
      top2_wave(b0, b1);
      if (tid == 0) st64(ws, SY_FD + cb * 512 + rp * 8, ((u64)b1 << 32) | b0);
      lc0 = key_idx(b0) & (S_ - 1); lc1 = key_idx(b1) & (S_ - 1);
      STAMPT(1, 11, 0);
    }
    float mb, rs;
    {
      u32x4 pa, pb;
      for (unsigned s_ = 0; s_ < SPIN_CAP; ++s_) {
        pa = LDW(WS_ST2 + ((rp * 8 + 2 * eseg) * 64 + erow) * 16);
        pb = LDW(WS_ST2 + ((rp * 8 + 2 * eseg + 1) * 64 + erow) * 16);
        if (__all(pa[1] & pa[3] & pb[1] & pb[3] & 1u)) break;
        asm volatile("" ::: "memory");
      }
      const float ca = __uint_as_float(pa[0]), cb_ = __uint_as_float(pb[0]);
      const float mean = quad_sum(ca + cb_) * 0.125f;
      const float d0 = ca - mean, d1 = cb_ - mean;
      const float var = quad_sum((__uint_as_float(pa[1]) + __uint_as_float(pb[1])) + 128.0f * (d0 * d0 + d1 * d1)) * (1.0f / 1024.0f);
      mb = rbf(mean);
      rs = 1.0f / rbf(__builtin_amdgcn_sqrtf(rbf(rbf(var) + 1e-5f)));
    }
    unsigned nq[16];
#pragma unroll
    for (int i = 0; i < 16; ++i) {
      const unsigned dq = pack2(hv[2 * i] - mb, hv[2 * i + 1] - mb);
      nq[i] = pack2(bflo(dq) * rs, bfhi(dq) * rs);
    }
    const int grow = rp * 64 + erow;
#pragma unroll
    for (int tk = 0; tk < 2; ++tk) {
      const int sel = tk ? lc1 : lc0;
      if (__any(grow == sel)) {
        char* tb = smem + 69632 + wave * 1024;
        if (grow == sel) {
#pragma unroll
          for (int i = 0; i < 16; ++i)
            *(u32x4*)(tb + eseg * 256 + i * 16) = (u32x4){nq[i] << 16, __float_as_uint(hv[2 * i]) | 1u, nq[i] & 0xffff0000u, __float_as_uint(hv[2 * i + 1]) | 1u};
        }
        __builtin_amdgcn_wave_barrier();
        const u32x4 v4 = *(const u32x4*)(tb + lane * 16);
#pragma unroll
        for (int rr_ = 0; rr_ < XSR; ++rr_) STW(v4, WS_XS + rr_ * 524288 + rp * 16384 + tk * 8192 + cb * 1024 + lane * 16);
        __builtin_amdgcn_wave_barrier();
      }
    }
    if (wave == 0) {
      u64 cc = 0;
      for (unsigned s_ = 0; s_ < SPIN_CAP; ++s_) {
        cc = ld64(ws, SY_FD + cb * 512 + (lane & 31) * 8);
        if (__all(cc != 0ull)) break;
      }
      STAMPT(1, 13, 0);
      unsigned c0 = lane < 32 ? (unsigned)cc : 0u, c1 = lane < 32 ? (unsigned)(cc >> 32) : 0u;
      top2_wave(c0, c1);
      const unsigned w0 = key_idx(c0) & (S_ - 1), w1 = key_idx(c1) & (S_ - 1);
      const unsigned k0 = __builtin_amdgcn_readlane((unsigned)cc, w0 >> 6), k1 = __builtin_amdgcn_readlane((unsigned)cc, w1 >> 6);
      if (lane == 0) { misc[3] = w0; misc[4] = w1; misc[5] = (w0 >> 6) * 2 + (k0 == c0 ? 0 : 1); misc[6] = (w1 >> 6) * 2 + (k1 == c1 ? 0 : 1); }
    }
    RAW_BAR();
    STAMPT(0, 11, 0);
    const int sel0 = misc[3], sel1 = misc[4];
    RAW_BAR();
    if (grow != sel0 && grow != sel1) {
#pragma unroll
      for (int c = 0; c < 4; ++c) {
        u32x4 ob;
#pragma unroll
        for (int i = 0; i < 4; ++i) ob[i] = pack2(hv[8 * c + 2 * i] + bflo(nq[4 * c + i]), hv[8 * c + 2 * i + 1] + bfhi(nq[4 * c + i]));
        *(u32x4*)(out + ego + c * 8) = ob;
      }
    }
    STAMPT(0, 12, 0);
    if (has_mlp) { RAW_BAR(); RAW_BAR(); }
  } else {
    const int fs = has_mlp ? (bid & 7) * 28 + (bid >> 3) : (bid & 7) * 28;
    const int mt = tid & 255;
    u32x4 wgr[8], wur[8], wdr[8];
    const int krow = (fs & 3) * 256 + (mt >> 6) * 64 + (lane >> 3);
    const u16* pg = wg + (size_t)krow * F_ + (fs >> 2) * 64 + (lane & 7) * 8;
    const u16* pu = wu + (size_t)krow * F_ + (fs >> 2) * 64 + (lane & 7) * 8;
    const u16* pd = wd + (size_t)(fs * 16 + h * 8) * D_ + ((mt >> 6) * 32 + r) * 8;
    RAW_BAR(); RAW_BAR();
#pragma unroll
    for (int i = 0; i < 8; ++i) {
      asm volatile("global_load_dwordx4 %0, %1, off" : "=a"(wgr[i]) : "v"(pg + (size_t)i * 8 * F_) : "memory");
      asm volatile("global_load_dwordx4 %0, %1, off" : "=a"(wdr[i]) : "v"(pd + (size_t)i * D_) : "memory");
      RAW_BAR();
      asm volatile("global_load_dwordx4 %0, %1, off" : "=a"(wur[i]) : "v"(pu + (size_t)i * 8 * F_) : "memory");
      RAW_BAR();
    }
    RAW_BAR();
    WAIT_V(0);
#pragma unroll
    for (int i = 0; i < 8; ++i) asm volatile("" : "+a"(wgr[i]), "+a"(wur[i]), "+a"(wdr[i]));
    unsigned wr2[8];
    if (wave < 6) {
#pragma unroll
      for (int j = 0; j < 8; ++j) wr2[j] = ((const unsigned*)w_r)[j * 64 + lane];
    }
    RAW_BAR();
    if (wave < 6) {
      const int tk = wave - 4;
      u32x4 q[8];
      const unsigned xo = WS_XS + ((bid >> 3) & (XSR - 1)) * 524288u + (unsigned)misc[5 + tk] * 8192u;
      for (unsigned s_ = 0; s_ < SPIN_CAP; ++s_) {
        unsigned ok = 1u;
#pragma unroll
        for (int j = 0; j < 8; ++j) { q[j] = LDW(xo + j * 1024 + lane * 16); ok &= q[j][1] & q[j][3]; }
        if (__all(ok & 1u)) break;
        asm volatile("" ::: "memory");
        __builtin_amdgcn_s_sleep(XSL1);
      }
      float rr = 0.f;
#pragma unroll
      for (int j = 0; j < 8; ++j) {
        rr = __builtin_fmaf(__uint_as_float(q[j][0]), bflo(wr2[j]), rr); rr = __builtin_fmaf(__uint_as_float(q[j][2]), bfhi(wr2[j]), rr);
        *(u32x2*)(xs + tk * 1024 + j * 128 + lane * 2) = (u32x2){q[j][0], q[j][2]};
        *(u32x2*)(hs + tk * 1024 + j * 128 + lane * 2) = (u32x2){q[j][1] & ~1u, q[j][3] & ~1u};
      }
      rr = rbf(wave_sum(rr));
      if (lane == 0) sgm[tk] = rbf(1.0f / (1.0f + __expf(-rr)));
    }
    RAW_BAR();
    if (has_mlp) {
      {
        f32x2 ag[8], au[8];
#pragma unroll
        for (int c = 0; c < 8; ++c) { ag[c] = (f32x2){0.f, 0.f}; au[c] = (f32x2){0.f, 0.f}; }
#pragma unroll
        for (int i = 0; i < 8; ++i) {
          const f32x2 xx = {xs[krow + 8 * i], xs[1024 + krow + 8 * i]};
#pragma unroll
          for (int c = 0; c < 4; ++c) {
            ag[2 * c] += xx * bflo(wgr[i][c]); ag[2 * c + 1] += xx * bfhi(wgr[i][c]);
            au[2 * c] += xx * bflo(wur[i][c]); au[2 * c + 1] += xx * bfhi(wur[i][c]);
          }
        }
        float v[32];
#pragma unroll
        for (int c = 0; c < 8; ++c) { v[c] = ag[c][0]; v[8 + c] = ag[c][1]; v[16 + c] = au[c][0]; v[24 + c] = au[c][1]; }
#pragma unroll
        for (int i = 0; i < 16; ++i) {
          auto sw = __builtin_amdgcn_permlane32_swap(__float_as_uint(v[i]), __float_as_uint(v[16 + i]), false, false);
          v[i] = __uint_as_float(sw[0]) + __uint_as_float(sw[1]);
        }
#pragma unroll
        for (int i = 0; i < 8; ++i) {
          auto sw = __builtin_amdgcn_permlane16_swap(__float_as_uint(v[i]), __float_as_uint(v[8 + i]), false, false);
          v[i] = __uint_as_float(sw[0]) + __uint_as_float(sw[1]);
        }
        const bool up = (lane & 8) != 0;
#pragma unroll
        for (int i = 0; i < 4; ++i) {
          const float keep = up ? v[4 + i] : v[i], give = up ? v[i] : v[4 + i];
          v[i] = keep + dppf<0x128>(give);
        }
        *(f32x4*)(red + (wave - 4) * 256 + (lane >> 5) * 128 + ((lane >> 4) & 1) * 64 + (lane & 7) * 8 + ((lane >> 3) & 1) * 4) = (f32x4){v[0], v[1], v[2], v[3]};
      }
      STAMPT(1, 14, 256);
      RAW_BAR();
      if (wave == 4) {
        f32x4 s4 = *(const f32x4*)(red + lane * 4);
#pragma unroll
        for (int w2 = 1; w2 < 4; ++w2) s4 += *(const f32x4*)(red + w2 * 256 + lane * 4);
        u32x4 t4 = __builtin_bit_cast(u32x4, s4); t4[1] |= 1u; t4[3] |= 1u;
        STW(t4, WS_GU + fs * 1024 + lane * 16);
        u32x4 q[4];
        for (unsigned s_ = 0; s_ < SPIN_CAP; ++s_) {
          unsigned ok = 1u;
#pragma unroll
          for (int j = 0; j < 4; ++j) { q[j] = LDW(WS_GU + ((fs & ~3) + j) * 1024 + lane * 16); ok &= q[j][1] & q[j][3]; }
          if (__all(ok & 1u)) break;
          asm volatile("" ::: "memory");
        }
        const f32x4 tot = (__builtin_bit_cast(f32x4, q[0]) + __builtin_bit_cast(f32x4, q[1])) + (__builtin_bit_cast(f32x4, q[2]) + __builtin_bit_cast(f32x4, q[3]));
        f32x4 a4;
#pragma unroll
        for (int i = 0; i < 4; ++i) {
          auto sw = __builtin_amdgcn_permlane32_swap(__float_as_uint(tot[i]), __float_as_uint(tot[i]), false, false);
          const float gs = rbf(__uint_as_float(sw[0])), us = rbf(__uint_as_float(sw[1]));
          const float sg = rbf(gs / (1.0f + __expf(-gs)));
          a4[i] = rbf(sg * us);
        }
        if (lane < 32 && ((lane & 15) >> 2) == (fs & 3)) *(f32x4*)(act + (lane >> 4) * 16 + (lane & 3) * 4) = a4;
      }
      RAW_BAR();
      float y0[8], y1[8];
#pragma unroll
      for (int c = 0; c < 8; ++c) { y0[c] = 0.f; y1[c] = 0.f; }
#pragma unroll
      for (int i = 0; i < 8; ++i) {
        const float a0 = act[h * 8 + i], a1 = act[16 + h * 8 + i];
#pragma unroll
        for (int c = 0; c < 4; ++c) {
          const float dl = bflo(wdr[i][c]), dh = bfhi(wdr[i][c]);
          y0[2 * c] += a0 * dl; y0[2 * c + 1] += a0 * dh; y1[2 * c] += a1 * dl; y1[2 * c + 1] += a1 * dh;
        }
      }
      u32x4 ya, yb;
#pragma unroll
      for (int c = 0; c < 8; ++c) {
        auto sw = __builtin_amdgcn_permlane32_swap(__float_as_uint(y0[c]), __float_as_uint(y1[c]), false, false);
        const unsigned v = __float_as_uint(__uint_as_float(sw[0]) + __uint_as_float(sw[1])) | (c & 1);
        if (c < 4) ya[c] = v; else yb[c - 4] = v;
      }
      const unsigned yo = WS_YP + fs * 8192 + h * 4096 + ((mt >> 6) * 32 + r) * 32;
      STW(ya, yo); STW(yb, yo + 16);
      STAMPT(1, 15, 256);
      STAMPT(0, 13, 256);
    }
  }
  if (!has_mlp) {
    const int j = bid - 224, tk = j >> 4, c0 = (j & 15) * 64;
    const int pg = tid >> 4, c4 = tid & 15;
    u32x4 pv[7];
    __builtin_amdgcn_s_sleep(YSL0);
    for (unsigned s_ = 0; s_ < SPIN_CAP; ++s_) {
      unsigned ok = 1u;
#pragma unroll
      for (int p = 0; p < 7; ++p) { pv[p] = LDW(WS_YP + (pg * 7 + p) * 8192 + tk * 4096 + c0 * 4 + c4 * 16); ok &= pv[p][1] & pv[p][3]; }
      if (__all(ok & 1u)) break;
      asm volatile("" ::: "memory");
      __builtin_amdgcn_s_sleep(YSL1);
    }
    STAMPT(0, 13, 0);
    f32x4 s = {0.f, 0.f, 0.f, 0.f};
#pragma unroll
    for (int p = 0; p < 7; ++p) s += __builtin_bit_cast(f32x4, pv[p]);
    float* rd = (float*)(smem + 32768);
    *(f32x4*)(rd + pg * 64 + c4 * 4) = s;
    RAW_BAR();
    if (tid < 64) {
      float y = 0.f;
#pragma unroll
      for (int p = 0; p < 32; ++p) y += rd[p * 64 + tid];
      const float yy = rbf(rbf(y) * sgm[tk]);
      const float t = rbf(xs[tk * 1024 + c0 + tid] + yy);
      out[(size_t)misc[3 + tk] * D_ + c0 + tid] = (u16)(pack2(hs[tk * 1024 + c0 + tid] + t, 0.f) & 0xffffu);
    }
    STAMPT(0, 14, 0);
  }
  if (pt_) { while (__builtin_amdgcn_s_memrealtime() < pt_ + 6000) __builtin_amdgcn_s_sleep(2); }
}

extern "C" void dbg_layout(long long* l) {
  l[0] = -1; l[1] = -1; l[2] = -1; l[3] = -1; l[4] = -1;
}

extern "C" void kernel_launch(void* const* d_in, const int* in_sizes, int n_in, void* d_out, int out_size,
                              void* d_ws, size_t ws_size, hipStream_t stream) {
  (void)hipFuncSetAttribute((const void*)fused, hipFuncAttributeMaxDynamicSharedMemorySize, LDS_TOTAL);
  fused<<<256, 512, LDS_TOTAL, stream>>>((const u16*)d_in[0], (const u16*)d_in[1], (const u16*)d_in[2], (const u16*)d_in[4],
                                         (const u16*)d_in[5], (const u16*)d_in[6], (const u16*)d_in[7], (u16*)d_out, (char*)d_ws);
}
```

```cpp
#include <hip/hip_runtime.h>
#include <stdint.h>

#define LAS __attribute__((address_space(3)))
#define GAS __attribute__((address_space(1)))
#define DI __device__ __forceinline__

constexpr int KROT = 2;
constexpr int XSR = 4;
constexpr int XSL1 = 6;
constexpr int YSL0 = 64, YSL1 = 4;
typedef unsigned short u16;
typedef unsigned long long u64;
typedef __attribute__((ext_vector_type(2))) __bf16 bf16x2;
typedef __attribute__((ext_vector_type(8))) __bf16 bf16x8;
typedef __attribute__((ext_vector_type(4))) short s16x4;
typedef __attribute__((ext_vector_type(2))) unsigned short u16x2;
typedef __attribute__((ext_vector_type(16))) float f32x16;
typedef __attribute__((ext_vector_type(4))) float f32x4;
typedef __attribute__((ext_vector_type(2))) float f32x2;
typedef __attribute__((ext_vector_type(4))) unsigned u32x4;
typedef __attribute__((ext_vector_type(2))) unsigned u32x2;
typedef GAS unsigned gu32;
typedef GAS unsigned long long gu64;

constexpr int S_ = 2048, D_ = 1024, F_ = 3584;

constexpr unsigned SY_CH = 0, SY_GO = 1024, SY_RE = 2048, SY_FSR = 4096, SY_FSC = 8192, SY_FA = 12288, SY_FB = 16384,
                   SY_FC = 20480, SY_FD = 28672, SY_FE = 32768;
constexpr unsigned WS_RS  = 65536;
constexpr unsigned WS_CS  = WS_RS + 16 * 128 * 128;
constexpr unsigned WS_KVP = WS_CS + 16 * 192 * 64;
constexpr unsigned WS_O   = WS_KVP + 256 * 8448;
constexpr unsigned WS_ST2 = WS_O + S_ * D_ * 2;
constexpr unsigned WS_YP  = WS_ST2 + 32 * 8 * 64 * 16;
constexpr unsigned WS_GU  = WS_YP + 224 * 8192;
constexpr unsigned WS_XS  = WS_GU + 256 * 1024;
constexpr unsigned WS_END = WS_XS + 4 * 32 * 16384;
constexpr size_t WS_DIAG  = 64u << 20;
#define STAMPT(kid, i, T) do {} while (0)

DI float bflo(unsigned u) { return __uint_as_float(u << 16); }
DI float bfhi(unsigned u) { return __uint_as_float(u & 0xffff0000u); }
DI float rbf(float x) { return (float)(__bf16)x; }
DI unsigned pack2(float a, float b) { bf16x2 c; c[0] = (__bf16)a; c[1] = (__bf16)b; return __builtin_bit_cast(unsigned, c); }
DI f32x16 mfma32(u32x4 a, u32x4 b, f32x16 c) {
  return __builtin_amdgcn_mfma_f32_32x32x16_bf16(__builtin_bit_cast(bf16x8, a), __builtin_bit_cast(bf16x8, b), c, 0, 0, 0);
}
DI u32x2 trread(const char* p) {
  s16x4 v = __builtin_amdgcn_ds_read_tr16_b64_v4i16((LAS s16x4*)p);
  return __builtin_bit_cast(u32x2, v);
}
DI const char* uni(const void* p) {
  const u64 v = (u64)p;
  return (const char*)(((u64)(unsigned)__builtin_amdgcn_readfirstlane((int)(v >> 32)) << 32) | (unsigned)__builtin_amdgcn_readfirstlane((int)v));
}
#define RSV "a119", "a120", "a121", "a122", "a123", "a124", "a125", "a126", "a127"
DI void glds16(unsigned voff, const void* sbase, unsigned lds) {
  asm volatile("s_mov_b32 m0, %2\n\ts_nop 0\n\tglobal_load_lds_dwordx4 %0, %1" ::"v"(voff), "s"(uni(sbase)), "s"(__builtin_amdgcn_readfirstlane(lds)) : "memory");
}
DI void glds16r(unsigned voff, const void* sbase, unsigned lds) {
  asm volatile("s_mov_b32 m0, %2\n\ts_nop 0\n\tglobal_load_lds_dwordx4 %0, %1" ::"v"(voff), "s"(uni(sbase)), "s"(__builtin_amdgcn_readfirstlane(lds)) : "memory", RSV);
}
DI void glds16_sc1(unsigned voff, const void* sbase, unsigned lds) {
  asm volatile("s_mov_b32 m0, %2\n\ts_nop 0\n\tglobal_load_lds_dwordx4 %0, %1 sc1" ::"v"(voff), "s"(uni(sbase)), "s"(__builtin_amdgcn_readfirstlane(lds)) : "memory");
}
DI float xh_sum(float v) {
  auto r = __builtin_amdgcn_permlane32_swap(__float_as_uint(v), __float_as_uint(v), false, false);
  return __uint_as_float(r[0]) + __uint_as_float(r[1]);
}
template <int CTRL> DI unsigned dppu(unsigned v) { return (unsigned)__builtin_amdgcn_update_dpp(0, (int)v, CTRL, 0xf, 0xf, true); }
template <int CTRL> DI float dppf(float v) { return __uint_as_float(dppu<CTRL>(__float_as_uint(v))); }
constexpr int DPP_X1 = 0xB1, DPP_X2 = 0x4E, DPP_HMIR = 0x141, DPP_MIR = 0x140;
DI float quad_sum(float v) { v += dppf<DPP_X1>(v); v += dppf<DPP_X2>(v); return v; }
DI float x16_sum(float v) {
  auto r = __builtin_amdgcn_permlane16_swap(__float_as_uint(v), __float_as_uint(v), false, false);
  return __uint_as_float(r[0]) + __uint_as_float(r[1]);
}
DI float wave_sum(float v) {
  v = quad_sum(v); v += dppf<DPP_HMIR>(v); v += dppf<DPP_MIR>(v);
  return xh_sum(x16_sum(v));
}
DI unsigned phi_pair(f32x2 t, u16x2& tmax) {
  const unsigned tb = pack2(t[0], t[1]);
  tmax = __builtin_elementwise_max(tmax, __builtin_bit_cast(u16x2, tb));
  const f32x2 tr = {bflo(tb), bfhi(tb)};
  const f32x2 u = tr + 1.0f;
  const f32x2 l = tr * 1.44269504088896340736f;
  const float e0 = __builtin_amdgcn_exp2f(l[0]), e1 = __builtin_amdgcn_exp2f(l[1]);
  return pack2(tr[0] > 0.f ? u[0] : e0, tr[1] > 0.f ? u[1] : e1);
}
#define RLX_AGENT __ATOMIC_RELAXED, __HIP_MEMORY_SCOPE_AGENT
DI void st32(char* ws, unsigned off, unsigned v) { __hip_atomic_store((gu32*)(ws + off), v, RLX_AGENT); }
DI void st64(char* ws, unsigned off, u64 v) { __hip_atomic_store((gu64*)(ws + off), v, RLX_AGENT); }
DI unsigned ld32(char* ws, unsigned off) { return __hip_atomic_load((gu32*)(ws + off), RLX_AGENT); }
DI u64 ld64(char* ws, unsigned off) { return __hip_atomic_load((gu64*)(ws + off), RLX_AGENT); }
#define WAIT_V(n) asm volatile("s_waitcnt vmcnt(" #n ")" ::: "memory")
#define WAIT_L0() asm volatile("s_waitcnt lgkmcnt(0)" ::: "memory")
#define RAW_BAR() do { WAIT_L0(); __builtin_amdgcn_s_barrier(); asm volatile("" ::: "memory"); } while (0)
#define SB0() __builtin_amdgcn_sched_barrier(0)
#define WAIT_VR(n) asm volatile("s_waitcnt vmcnt(" #n ")" ::: "memory", RSV)
#define RAW_BARR() do { asm volatile("s_waitcnt lgkmcnt(0)" ::: "memory", RSV); __builtin_amdgcn_s_barrier(); asm volatile("" ::: "memory", RSV); } while (0)
#define SBR() do { asm volatile("" ::: RSV); __builtin_amdgcn_sched_barrier(0); } while (0)
constexpr unsigned SPIN_CAP = 1u << 22;
DI void poll_flags(char* ws, unsigned off, bool act) {
  for (unsigned s = 0; s < SPIN_CAP; ++s) {
    const unsigned v = act ? ld32(ws, off) : 1u;
    if (__all(v != 0u)) break;
  }
}
DI unsigned mk_key(float v, unsigned idx) {
  const unsigned u = __float_as_uint(v) >> 16;
  const unsigned o = (u & 0x8000u) ? (~u & 0xffffu) : (u | 0x8000u);
  return (o << 16) | (0xFFFFu - idx);
}
DI unsigned key_idx(unsigned k) { return 0xFFFFu - (k & 0xFFFFu); }
DI void top2_mix(unsigned& b0, unsigned& b1, unsigned c0, unsigned c1) {
  const unsigned lo = min(b0, c0), mx = max(b1, c1);
  b0 = max(b0, c0); b1 = max(lo, mx);
}
DI void top2_wave(unsigned& b0, unsigned& b1) {
  top2_mix(b0, b1, dppu<DPP_X1>(b0), dppu<DPP_X1>(b1));
  top2_mix(b0, b1, dppu<DPP_X2>(b0), dppu<DPP_X2>(b1));
  top2_mix(b0, b1, dppu<DPP_HMIR>(b0), dppu<DPP_HMIR>(b1));
  top2_mix(b0, b1, dppu<DPP_MIR>(b0), dppu<DPP_MIR>(b1));
  { auto p = __builtin_amdgcn_permlane16_swap(b0, b0, false, false); auto q = __builtin_amdgcn_permlane16_swap(b1, b1, false, false);
    b0 = p[0]; b1 = q[0]; top2_mix(b0, b1, p[1], q[1]); }
  { auto p = __builtin_amdgcn_permlane32_swap(b0, b0, false, false); auto q = __builtin_amdgcn_permlane32_swap(b1, b1, false, false);
    b0 = p[0]; b1 = q[0]; top2_mix(b0, b1, p[1], q[1]); }
}

constexpr int K1_STAGE = 40960;
constexpr int LDS_TOTAL = 4 * K1_STAGE;
constexpr int K2_STAGE = 24576;
constexpr int L_KVL = 147456, L_ZL = L_KVL + 4096, L_MISC = L_ZL + 256;

__global__ __launch_bounds__(512, 2) void fused(const u16* __restrict__ x, const u16* __restrict__ wqkv,
                                                const u16* __restrict__ wo, const u16* __restrict__ w_r,
                                                const u16* __restrict__ wg, const u16* __restrict__ wu,
                                                const u16* __restrict__ wd, u16* __restrict__ out, char* __restrict__ ws) {
  extern __shared__ __attribute__((aligned(16))) char smem[];
  STAMPT(0, 0, 0);
  { float dz = 0.f; asm volatile("" : "+a"(dz)); }
  int tid = threadIdx.x, lane = tid & 63;
  const int wave = __builtin_amdgcn_readfirstlane(tid >> 6);
  const int g = wave >> 2, wr = (wave >> 1) & 1, wc = wave & 1;
  const int bid = blockIdx.x, xcd = bid & 7, idx = bid >> 3;
  const int rb = (xcd & 1) * 8 + (idx & 7);
  const int hp = (xcd >> 1) * 4 + (idx >> 3);
  const int rp = xcd * 4 + (idx >> 3), cb = idx & 7;
  int r = lane & 31, h = lane >> 5;
  const unsigned lds0 = (unsigned)(size_t)smem;
  const __amdgpu_buffer_rsrc_t wsr = __builtin_amdgcn_make_buffer_rsrc(ws, 0, WS_END, 0x00020000);
#define LDW(off) __builtin_amdgcn_raw_buffer_load_b128(wsr, (off), 0, 16)
#define STW(v, off) __builtin_amdgcn_raw_buffer_store_b128((v), wsr, (off), 0, 16)

  unsigned hs_cur = 0, hs_g0 = 0; int hs_state = 0;
  u32x4 ms_exp = {0, 0, 0, 0}; bool ms_done = false;
  const unsigned hs_off = (lane == 1 ? SY_GO : SY_CH) + bid * 4;
  const unsigned ms_off = lane * 16;
  const char* wsu = uni(ws);
#define HS_PROBE() asm volatile("global_load_dword a119, %0, %1 sc1" :: "v"(hs_off), "s"(wsu) : "memory", RSV)
#define MS_PROBE() asm volatile("global_load_dwordx4 a[124:127], %0, %1 offset:2048 sc1" :: "v"(ms_off), "s"(wsu) : "memory", RSV)
  const bool is_master = (bid == 0) && (wave == 1);
  auto hs_begin = [&]() {
    unsigned zo = SY_FA + hp * 256 + rb * 4;
    if (lane == 1) zo = SY_FB + rb * 256 + hp * 4;
    if (lane == 2) zo = SY_FC + rp * 256 + cb * 4;
    if (lane == 3) zo = SY_FE + bid * 4;
    if (lane >= 4) zo = SY_FD + cb * 512 + rp * 8 + (lane & 1) * 4;
    if (lane < 6) st32(ws, zo, 0u);
    STW(((u32x4){0u, 0u, 0u, 0u}), WS_ST2 + (rp * 8 + cb) * 1024 + lane * 16);
  };
  if (is_master) {
    MS_PROBE();
    asm volatile("global_load_dwordx4 a[120:123], %0, %1 offset:1024 sc1" :: "v"(ms_off), "s"(wsu) : "memory", RSV);
  }
  auto hs_step = [&](bool first, bool mfirst) {
    if (wave == 0 && !mfirst) {
      unsigned hv_;
      asm volatile("v_accvgpr_read_b32 %0, a119" : "=v"(hv_) :: RSV);
      const unsigned c = __builtin_amdgcn_readlane(hv_, 0), go = __builtin_amdgcn_readlane(hv_, 1);
      if (first) {
        hs_cur = c;
        if (lane == 0) st32(ws, SY_RE + bid * 4, c);
      } else if (hs_state == 0) {
        if (go == hs_cur && go != hs_g0) hs_state = 2;
        else if (c != hs_cur) { hs_cur = c; if (lane == 0) st32(ws, SY_RE + bid * 4, c); }
      }
      if (hs_state < 2) HS_PROBE();
    }
    if (is_master && !ms_done) {
      u32x4 re;
      asm volatile("v_accvgpr_read_b32 %0, a124\n\tv_accvgpr_read_b32 %1, a125\n\tv_accvgpr_read_b32 %2, a126\n\tv_accvgpr_read_b32 %3, a127"
                   : "=v"(re[0]), "=v"(re[1]), "=v"(re[2]), "=v"(re[3]) :: RSV);
      if (mfirst) {
        u32x4 g0;
        asm volatile("v_accvgpr_read_b32 %0, a120\n\tv_accvgpr_read_b32 %1, a121\n\tv_accvgpr_read_b32 %2, a122\n\tv_accvgpr_read_b32 %3, a123"
                     : "=v"(g0[0]), "=v"(g0[1]), "=v"(g0[2]), "=v"(g0[3]) :: RSV);
#pragma unroll
        for (int j = 0; j < 4; ++j) { const unsigned c = re[j] + 1u; ms_exp[j] = (c == g0[j]) ? c + 1u : c; }
        STW(ms_exp, SY_CH + ms_off);
      } else if (__all(re[0] == ms_exp[0] && re[1] == ms_exp[1] && re[2] == ms_exp[2] && re[3] == ms_exp[3])) {
        STW(ms_exp, SY_GO + ms_off);
        ms_done = true;
      }
      if (!ms_done) MS_PROBE();
    }
  };

  auto hs_pre = [&]() {
    if (wave == 0) {
      unsigned hv_;
      asm volatile("v_accvgpr_read_b32 %0, a119" : "=v"(hv_) :: RSV);
      hs_g0 = __builtin_amdgcn_readlane(hv_, 1);
      HS_PROBE();
    }
  };
  auto stat_dma = [&]() {
    int t2 = threadIdx.x; asm volatile("" : "+v"(t2));
    const unsigned vo = (t2 & 63) * 16;
#pragma unroll
    for (int i = 0; i < 2; ++i)
      asm volatile("s_mov_b32 m0, %2\n\ts_nop 0\n\tglobal_load_lds_dwordx4 %0, %1 sc1" ::"v"(vo), "s"(uni(ws + WS_RS + rb * 16384 + (wave * 2 + i) * 1024)),
                   "s"(__builtin_amdgcn_readfirstlane(lds0 + 98304 + (wave * 2 + i) * 1024)) : "memory", RSV);
#pragma unroll
    for (int i = 0; i < 2; ++i)
      if (wave * 2 + i < 12)
        asm volatile("s_mov_b32 m0, %2\n\ts_nop 0\n\tglobal_load_lds_dwordx4 %0, %1 sc1" ::"v"(vo), "s"(uni(ws + WS_CS + hp * 12288 + (wave * 2 + i) * 1024)),
                     "s"(__builtin_amdgcn_readfirstlane(lds0 + 114688 + (wave * 2 + i) * 1024)) : "memory", RSV);
  };
  const int sigma = (rb + hp) & 15;
  const int krot = KROT * xcd, sg = (sigma - krot) & 15;
  auto korder = [&](int t) { return ((t < 2 ? (t < sg ? t : t + 1) : t == 2 ? sg : (t - 1 < sg ? t - 1 : t)) + krot) & 15; };
#define RS_SLOT(T) (WS_RS + ((rb * 16 + sigma) * 128 + ((T) >> 2)) * 8)
#define CS_SLOT(T) (WS_CS + ((hp * 16 + sigma) * 192 + wave * 32 + ((T) & 31)) * 4)

  unsigned voA0, voA1, voB;
  {
    const int r0 = 16 * wave + (lane >> 3), r1 = r0 + 8;
    const int c0 = (lane & 7) ^ ((r0 >> 1) & 7), c1 = (lane & 7) ^ ((r1 >> 1) & 7);
    voA0 = (unsigned)(r0 * D_ + c0 * 8) * 2u;
    voA1 = (unsigned)(r1 * D_ + c1 * 8) * 2u;
    const int bi = lane >> 4, q = (lane >> 2) & 3, ch = lane & 3;
    voB = (unsigned)((8 * wave + 4 * (bi >> 1) + q) * (3 * D_) + (bi & 1) * 32 + ch * 8) * 2u;
  }
  const char* baseA = uni(x + (size_t)rb * 128 * D_);
  const char* baseB = uni(wqkv + hp * 64);
  const int ft = wc ^ g;
  int trl = ((lane & 15) >> 2) * 64 + ((lane >> 4) & 1) * 32 + (lane & 3) * 8;
  f32x16 acc[2][3];
  {
    auto stage = [&](int t, int sb) {
      const int kt = korder(t);
      const unsigned l = lds0 + sb;
      const char* pa = baseA + kt * 128;
      const char* pb = baseB + (size_t)kt * (64 * 3 * D_ * 2);
      glds16r(voA0, pa, l + (2 * wave) * 1024);
      glds16r(voA1, pa, l + (2 * wave + 1) * 1024);
      glds16r(voB, pb, l + 16384 + (3 * wave) * 1024);
      glds16r(voB + 2048, pb, l + 16384 + (3 * wave + 1) * 1024);
      glds16r(voB + 4096, pb, l + 16384 + (3 * wave + 2) * 1024);
    };
    const int ka = 4 * g + h, kb = 4 * g + 2 + h;
    const int aF0 = (64 * wr + 32 * ft + r) * 128 + ((ka ^ ((r >> 1) & 7)) * 16);
    const int aF1 = (64 * wr + 32 * ft + r) * 128 + ((kb ^ ((r >> 1) & 7)) * 16);
    const int aS0 = aF0 ^ 4096, aS1 = aF1 ^ 4096;
    const int bo0 = 16384 + wc * 256 + trl + ka * 3072, bo1 = 16384 + wc * 256 + trl + kb * 3072;
#pragma unroll
    for (int i = 0; i < 2; ++i)
#pragma unroll
      for (int j = 0; j < 3; ++j)
#pragma unroll
        for (int k = 0; k < 16; ++k) acc[i][j][k] = 0.f;
    struct Frag { u32x4 a[2][2]; u32x2 bl_[2][3], bh_[2][3]; };
    Frag FA, FB;
#define K1_B(F, kl, t) ((u32x4){F.bl_[kl][t][0], F.bl_[kl][t][1], F.bh_[kl][t][0], F.bh_[kl][t][1]})
    auto body = [&](Frag& C, Frag& N, int sn, int t4, int sb, bool ST) {
      const char* nb = smem + sn;
      const int kt = korder(t4 & 15);
      const unsigned l = lds0 + sb;
      const char* pa = baseA + kt * 128;
      const char* pb = baseB + (size_t)kt * (64 * 3 * D_ * 2);
      acc[0][0] = mfma32(K1_B(C, 0, 0), C.a[0][0], acc[0][0]);
      if (ST) glds16r(voA0, pa, l + (2 * wave) * 1024);
      N.a[0][0] = *(const u32x4*)(nb + aF0); N.bl_[0][0] = trread(nb + bo0);
      SBR();
      acc[0][1] = mfma32(C.a[0][0], K1_B(C, 0, 1), acc[0][1]);
      if (ST) glds16r(voA1, pa, l + (2 * wave + 1) * 1024);
      N.bh_[0][0] = trread(nb + bo0 + 512); N.bl_[0][1] = trread(nb + bo0 + 1024);
      SBR();
      acc[0][2] = mfma32(C.a[0][0], K1_B(C, 0, 2), acc[0][2]);
      if (ST) glds16r(voB, pb, l + 16384 + (3 * wave) * 1024);
      N.bh_[0][1] = trread(nb + bo0 + 1536);
      SBR();
      acc[1][0] = mfma32(K1_B(C, 0, 0), C.a[0][1], acc[1][0]);
      if (ST) glds16r(voB + 2048, pb, l + 16384 + (3 * wave + 1) * 1024);
      N.bl_[0][2] = trread(nb + bo0 + 2048);
      SBR();
      acc[1][1] = mfma32(C.a[0][1], K1_B(C, 0, 1), acc[1][1]);
      if (ST) glds16r(voB + 4096, pb, l + 16384 + (3 * wave + 2) * 1024);
      N.bh_[0][2] = trread(nb + bo0 + 2560);
      SBR();
      acc[1][2] = mfma32(C.a[0][1], K1_B(C, 0, 2), acc[1][2]);
      N.a[0][1] = *(const u32x4*)(nb + aS0); N.a[1][0] = *(const u32x4*)(nb + aF1);
      SBR();
      acc[0][0] = mfma32(K1_B(C, 1, 0), C.a[1][0], acc[0][0]);
      N.bl_[1][0] = trread(nb + bo1); N.bh_[1][0] = trread(nb + bo1 + 512);
      SBR();
      acc[0][1] = mfma32(C.a[1][0], K1_B(C, 1, 1), acc[0][1]);
      N.bl_[1][1] = trread(nb + bo1 + 1024); N.bh_[1][1] = trread(nb + bo1 + 1536);
      SBR();
      acc[0][2] = mfma32(C.a[1][0], K1_B(C, 1, 2), acc[0][2]);
      N.bl_[1][2] = trread(nb + bo1 + 2048); N.bh_[1][2] = trread(nb + bo1 + 2560);
      SBR();
      acc[1][0] = mfma32(K1_B(C, 1, 0), C.a[1][1], acc[1][0]);
      N.a[1][1] = *(const u32x4*)(nb + aS1);
      SBR();
      acc[1][1] = mfma32(C.a[1][1], K1_B(C, 1, 1), acc[1][1]);
      acc[1][2] = mfma32(C.a[1][1], K1_B(C, 1, 2), acc[1][2]);
      SBR();
    };
    u64 st_row = 0; unsigned st_col = 0;
    auto stats = [&](int sn) {
      const char* nb = smem + sn;
      int t2 = threadIdx.x; asm volatile("" : "+v"(t2));
      {
        const int row = t2 >> 2, q = t2 & 3, key = (row >> 1) & 7;
        const u32x4 v0 = *(const u32x4*)(nb + row * 128 + (((2 * q) ^ key) * 16));
        const u32x4 v1 = *(const u32x4*)(nb + row * 128 + (((2 * q + 1) ^ key) * 16));
        float s = 0.f, sq = 0.f;
#pragma unroll
        for (int i = 0; i < 4; ++i) {
          const float a0 = bflo(v0[i]), a1 = bfhi(v0[i]), a2 = bflo(v1[i]), a3 = bfhi(v1[i]);
          s += (a0 + a1) + (a2 + a3);
          sq = __builtin_fmaf(a0, a0, sq); sq = __builtin_fmaf(a1, a1, sq); sq = __builtin_fmaf(a2, a2, sq); sq = __builtin_fmaf(a3, a3, sq);
        }
        s = quad_sum(s); sq = quad_sum(sq);
        st_row = ((u64)__float_as_uint(sq) << 32) | __float_as_uint(s);
      }
      if (wave < 6) {
        const int cw = wave >= 3 ? 1 : 0, ct = wave - 3 * cw;
        const u32x4 ONES4 = {0x3F803F80u, 0x3F803F80u, 0x3F803F80u, 0x3F803F80u};
        f32x16 ca;
#pragma unroll
        for (int k = 0; k < 16; ++k) ca[k] = 0.f;
#pragma unroll
        for (int ks = 0; ks < 4; ++ks) {
          const char* bp = nb + 16384 + (2 * ks + ((t2 >> 5) & 1)) * 3072 + ct * 1024 + cw * 256 + (((t2 & 15) >> 2) * 64 + ((t2 >> 4) & 1) * 32 + (t2 & 3) * 8);
          const u32x2 lo = trread(bp), hi = trread(bp + 512);
          ca = mfma32(ONES4, (u32x4){lo[0], lo[1], hi[0], hi[1]}, ca);
        }
        st_col = __float_as_uint(ca[0]);
      }
    };

    stage(0, 0); stage(1, K1_STAGE); stage(2, 2 * K1_STAGE); stage(3, 3 * K1_STAGE);
    if (wave == 0) HS_PROBE();
    if (wave == 7) {
      u64 pc; asm volatile("s_getpc_b64 %0" : "=s"(pc));
      const char* cp = (const char*)(pc - (pc & 127)) + (idx * 10 + (lane < 10 ? lane : 0)) * 128;
      asm volatile("global_load_dword a122, %0, off" :: "v"(cp) : "memory", RSV);
    }
    WAIT_VR(15); RAW_BARR();
    hs_step(false, true);
    STAMPT(0, 1, 0);
    FA.a[0][0] = *(const u32x4*)(smem + aF0); FA.a[0][1] = *(const u32x4*)(smem + aS0);
    FA.a[1][0] = *(const u32x4*)(smem + aF1); FA.a[1][1] = *(const u32x4*)(smem + aS1);
#pragma unroll
    for (int t = 0; t < 3; ++t) {
      FA.bl_[0][t] = trread(smem + bo0 + t * 1024); FA.bh_[0][t] = trread(smem + bo0 + t * 1024 + 512);
      FA.bl_[1][t] = trread(smem + bo1 + t * 1024); FA.bh_[1][t] = trread(smem + bo1 + t * 1024 + 512);
    }
    for (int t = 0; t < 16; t += 4) {
      const bool st = t < 12;
      WAIT_VR(10);
      RAW_BARR(); if (t) hs_step(t == 4, false);
      body(FA, FB, 1 * K1_STAGE, t + 4, 0 * K1_STAGE, st);
      if (st) WAIT_VR(10); else WAIT_VR(5);
      RAW_BARR();
      body(FB, FA, 2 * K1_STAGE, t + 5, 1 * K1_STAGE, st);
      if (t == 0) {
        stats(2 * K1_STAGE);
        int t2 = threadIdx.x; asm volatile("" : "+v"(t2));
        if ((t2 & 3) == 0) st64(ws, RS_SLOT(t2), st_row);
        if (wave < 6 && (t2 & 63) < 32) st32(ws, CS_SLOT(t2), st_col);
        if (wave == 0) hs_begin();
      }
      if (st) WAIT_VR(10); else WAIT_VR(0);
      RAW_BARR(); if (t) hs_step(false, false); else hs_pre();
      body(FA, FB, 3 * K1_STAGE, t + 6, 2 * K1_STAGE, st);
      if (st) WAIT_VR(10); else WAIT_VR(0);
      RAW_BARR();
      if (!st) stat_dma();
      body(FB, FA, 0 * K1_STAGE, t + 7, 3 * K1_STAGE, st);
    }
  }
  STAMPT(0, 2, 0);
  tid = threadIdx.x; asm volatile("" : "+v"(tid));
  lane = tid & 63; r = lane & 31; h = lane >> 5;
  trl = ((lane & 15) >> 2) * 64 + ((lane >> 4) & 1) * 32 + (lane & 3) * 8;
  float* xch = (float*)smem;
  float* stm = (float*)(smem + 144384);
  float* kvr = (float*)(smem + 98304);
  float* zr = (float*)(smem + 143360);
  volatile int* misc = (volatile int*)(smem + L_MISC);
  const int R0 = 64 * wr + 32 * ft;
  auto put_stats = [&]() {
    if (wave < 2) {
      float s = 0.f, sq = 0.f;
#pragma unroll
      for (int i = 0; i < 16; ++i) { const f32x2 v = *(const f32x2*)(smem + 98304 + i * 1024 + tid * 8); s += v[0]; sq += v[1]; }
      const float mean = s * (1.0f / 1024.0f);
      const float var = fmaxf(sq * (1.0f / 1024.0f) - mean * mean, 0.f);
      stm[tid] = rbf(mean);
      stm[128 + tid] = __builtin_amdgcn_rcpf(rbf(__builtin_amdgcn_sqrtf(rbf(rbf(var) + 1e-5f))));
    } else if (wave < 5) {
      float c = 0.f;
#pragma unroll
      for (int i = 0; i < 16; ++i) c += *(const float*)(smem + 114688 + i * 768 + (tid - 128) * 4);
      stm[256 + tid - 128] = c;
    }
  };
#pragma unroll
  for (int t = 0; t < 3; ++t)
#pragma unroll
    for (int q4 = 0; q4 < 4; ++q4)
      *(f32x4*)(xch + wave * 3072 + (t * 4 + q4) * 256 + lane * 4) =
          (f32x4){acc[1][t][4 * q4], acc[1][t][4 * q4 + 1], acc[1][t][4 * q4 + 2], acc[1][t][4 * q4 + 3]};
  if (tid == 0) misc[0] = hs_state;
  if (is_master && !ms_done) {
    for (unsigned s = 0; s < SPIN_CAP; ++s) {
      bool ok = true;
#pragma unroll
      for (int j = 0; j < 4; ++j) ok &= (ld32(ws, SY_RE + lane * 16 + j * 4) == ms_exp[j]);
      if (__all(ok)) break;
    }
    STW(ms_exp, SY_GO + lane * 16);
  }
  if (wave == 0 && hs_state < 2) {
    if (hs_state == 0) {
      for (unsigned s = 0; s < SPIN_CAP; ++s) {
        const unsigned go = ld32(ws, SY_GO + bid * 4);
        if (go == hs_cur && go != hs_g0) break;
        const unsigned c = ld32(ws, SY_CH + bid * 4);
        if (c != hs_cur) { hs_cur = c; if (lane == 0) st32(ws, SY_RE + bid * 4, c); }
      }
    }
  }
  WAIT_V(0);
  STAMPT(1, 0, 0);
  RAW_BAR();
  if (misc[0] != 2) { stat_dma(); WAIT_V(0); RAW_BAR(); }
  put_stats();
  RAW_BAR();
  STAMPT(1, 1, 0);
  if (wave >= 4) {
    const u32x4 z4 = {0u, 0u, 0u, 0u};
    const int sl_ = (bid & 7) * 28 + (bid >> 3);
    if (bid < 224) { STW(z4, WS_YP + sl_ * 8192 + (tid - 256) * 16); STW(z4, WS_YP + sl_ * 8192 + 4096 + (tid - 256) * 16); }
    if (wave == 4) STW(z4, WS_GU + sl_ * 1024 + lane * 16);
    if (wave >= 6) {
#pragma unroll
      for (int rr_ = 0; rr_ < XSR; ++rr_) STW(z4, WS_XS + rr_ * 524288 + rp * 16384 + (wave - 6) * 8192 + cb * 1024 + lane * 16);
    }
  }
  const float m_b = stm[R0 + r], rinv = stm[128 + R0 + r], csk = stm[256 + wc * 96 + 32 + r], csv = stm[256 + wc * 96 + 64 + r];
  STAMPT(0, 3, 0);
  const int head = 2 * hp + wc;
  const int rowtile = rb * 4 + 2 * wr + ft;
  unsigned qp[8];
  f32x4 tq[4];
  {
    unsigned kp[8], vp[8];
    u16x2 tmin = {0, 0};
    const float* px = xch + (wave ^ 4) * 3072 + lane * 4;
#pragma unroll
    for (int q4 = 0; q4 < 4; ++q4) {
      const f32x4 xq = *(const f32x4*)(px + q4 * 256);
      const f32x4 xk = *(const f32x4*)(px + (4 + q4) * 256);
      const f32x4 xv = *(const f32x4*)(px + (8 + q4) * 256);
      tq[q4] = (f32x4){acc[0][0][4 * q4], acc[0][0][4 * q4 + 1], acc[0][0][4 * q4 + 2], acc[0][0][4 * q4 + 3]} + xq;
      const f32x4 m4 = *(const f32x4*)(stm + R0 + 8 * q4 + 4 * h);
      const f32x4 i4 = *(const f32x4*)(stm + 128 + R0 + 8 * q4 + 4 * h);
      const f32x2 ck2 = {csk, csk}, cv2 = {csv, csv};
      const f32x2 ma = {m4[0], m4[1]}, mb = {m4[2], m4[3]}, ia = {i4[0], i4[1]}, ib = {i4[2], i4[3]};
      f32x2 k0 = (((f32x2){acc[0][1][4 * q4], acc[0][1][4 * q4 + 1]} + (f32x2){xk[0], xk[1]}) - ma * ck2) * ia;
      f32x2 k1 = (((f32x2){acc[0][1][4 * q4 + 2], acc[0][1][4 * q4 + 3]} + (f32x2){xk[2], xk[3]}) - mb * ck2) * ib;
      kp[2 * q4] = phi_pair(k0, tmin); kp[2 * q4 + 1] = phi_pair(k1, tmin);
      f32x2 v0 = (((f32x2){acc[0][2][4 * q4], acc[0][2][4 * q4 + 1]} + (f32x2){xv[0], xv[1]}) - ma * cv2) * ia;
      f32x2 v1 = (((f32x2){acc[0][2][4 * q4 + 2], acc[0][2][4 * q4 + 3]} + (f32x2){xv[2], xv[3]}) - mb * cv2) * ib;
      vp[2 * q4] = pack2(v0[0], v0[1]); vp[2 * q4 + 1] = pack2(v1[0], v1[1]);
    }
    if (__any(tmin[0] > 0xC0F0 || tmin[1] > 0xC0F0)) {
#pragma unroll
      for (int i = 0; i < 8; ++i) kp[i] = pack2(bflo(kp[i]) + 1e-6f, bfhi(kp[i]) + 1e-6f);
    }
    f32x16 kv, zv;
#pragma unroll
    for (int k = 0; k < 16; ++k) { kv[k] = 0.f; zv[k] = 0.f; }
    const u32x4 ONES4 = {0x3F803F80u, 0x3F803F80u, 0x3F803F80u, 0x3F803F80u};
    kv = mfma32((u32x4){kp[0], kp[1], kp[2], kp[3]}, (u32x4){vp[0], vp[1], vp[2], vp[3]}, kv);
    zv = mfma32((u32x4){kp[0], kp[1], kp[2], kp[3]}, ONES4, zv);
    kv = mfma32((u32x4){kp[4], kp[5], kp[6], kp[7]}, (u32x4){vp[4], vp[5], vp[6], vp[7]}, kv);
    zv = mfma32((u32x4){kp[4], kp[5], kp[6], kp[7]}, ONES4, zv);
#pragma unroll
    for (int q4 = 0; q4 < 4; ++q4)
      *(f32x4*)(kvr + wave * 1024 + q4 * 256 + lane * 4) = (f32x4){kv[4 * q4], kv[4 * q4 + 1], kv[4 * q4 + 2], kv[4 * q4 + 3]};
    if (r == 0) {
#pragma unroll
      for (int q4 = 0; q4 < 4; ++q4)
        *(f32x4*)(zr + wave * 32 + 8 * q4 + 4 * h) = (f32x4){zv[4 * q4], zv[4 * q4 + 1], zv[4 * q4 + 2], zv[4 * q4 + 3]};
    }
  }
  STAMPT(1, 2, 0);
  RAW_BAR();
  STAMPT(1, 3, 0);
  const int hc = tid >> 8, f = tid & 255;
  {
    f32x4 s = *(const f32x4*)(kvr + hc * 1024 + f * 4);
#pragma unroll
    for (int p = 1; p < 4; ++p) s += *(const f32x4*)(kvr + (hc + 2 * p) * 1024 + f * 4);
    const unsigned o_ = WS_KVP + ((hp * 16 + rb) * 2 + hc) * 4224;
    STW(__builtin_bit_cast(u32x4, s), o_ + f * 16);
    if (f < 8) {
      f32x4 z = *(const f32x4*)(zr + hc * 32 + f * 4);
#pragma unroll
      for (int p = 1; p < 4; ++p) z += *(const f32x4*)(zr + (hc + 2 * p) * 32 + f * 4);
      STW(__builtin_bit_cast(u32x4, z), o_ + 4096 + f * 16);
    }
  }
  WAIT_V(0);
  STAMPT(1, 4, 0);
  RAW_BAR();
  if (tid == 0) st32(ws, SY_FA + hp * 256 + rb * 4, 1u);
  STAMPT(0, 4, 0);

  const int rt = wave & 1, kh = (wave >> 1) & 1;
  unsigned voW;
  {
    const int bi = lane >> 4, q = (lane >> 2) & 3, ch = lane & 3;
    voW = (unsigned)((4 * (bi >> 1) + q) * D_ + (bi & 1) * 32 + ch * 8) * 2u;
  }
  const unsigned voL = lane * 16;
  const char* baseW = uni(wo + (size_t)((16 * kh) * 32 + 16 * rt) * D_ + cb * 128);
  const char* baseO = uni(ws + WS_O + (size_t)(((rp * 2 + rt) * 32 + 16 * kh) * 2) * 1024);
  auto stageW = [&](int t, int sb) {
    const char* sw = baseW + (size_t)t * (32 * D_ * 2);
    const unsigned dw = lds0 + sb + kh * 8192 + (2 * rt) * 2048;
    glds16(voW, sw, dw);
    glds16(voW + 128, sw, dw + 1024);
    glds16(voW + 8 * D_ * 2, sw, dw + 2048);
    glds16(voW + 8 * D_ * 2 + 128, sw, dw + 3072);
  };
  auto stageO = [&](int t, int sb) {
    const char* sp = baseO + (size_t)t * 2048;
    glds16_sc1(voL, sp, lds0 + sb + 16384 + wave * 2048);
    glds16_sc1(voL + 1024, sp, lds0 + sb + 16384 + wave * 2048 + 1024);
  };
  if (wave < 4) {
#pragma unroll
    for (int t = 0; t < 6; ++t) stageW(t, t * K2_STAGE);
  }

  {
    u16x2 tmin = {0, 0};
    const f32x2 mb2 = {m_b, m_b}, ri2 = {rinv, rinv};
#pragma unroll
    for (int q4 = 0; q4 < 4; ++q4) {
      const f32x4 c4 = *(const f32x4*)(stm + 256 + wc * 96 + 8 * q4 + 4 * h);
      const f32x2 t0 = ((f32x2){tq[q4][0], tq[q4][1]} - mb2 * (f32x2){c4[0], c4[1]}) * ri2;
      const f32x2 t1 = ((f32x2){tq[q4][2], tq[q4][3]} - mb2 * (f32x2){c4[2], c4[3]}) * ri2;
      qp[2 * q4] = phi_pair(t0, tmin); qp[2 * q4 + 1] = phi_pair(t1, tmin);
    }
    if (__any(tmin[0] > 0xC0F0 || tmin[1] > 0xC0F0)) {
#pragma unroll
      for (int i = 0; i < 8; ++i) qp[i] = pack2(bflo(qp[i]) + 1e-6f, bfhi(qp[i]) + 1e-6f);
    }
#pragma unroll
    for (int i = 0; i < 8; ++i) asm volatile("" : "+v"(qp[i]));
  }
  if (wave == 4) poll_flags(ws, SY_FA + hp * 256 + (lane & 15) * 4, true);
  RAW_BAR();
  STAMPT(0, 5, 0);
  {
    u32x4 pv[16];
    const unsigned o_ = WS_KVP + (hp * 16 * 2 + hc) * 4224 + f * 16;
#pragma unroll
    for (int p = 0; p < 16; ++p) pv[p] = LDW(o_ + p * 8448);
    f32x4 zs = {0.f, 0.f, 0.f, 0.f};
    if (f < 8) {
#pragma unroll
      for (int p = 0; p < 16; ++p) zs += __builtin_bit_cast(f32x4, LDW(o_ + 4096 + p * 8448));
    }
    f32x4 s = __builtin_bit_cast(f32x4, pv[0]);
#pragma unroll
    for (int p = 1; p < 16; ++p) s += __builtin_bit_cast(f32x4, pv[p]);
    asm volatile("" : "+v"(s));
    STAMPT(1, 5, 0);
    const int q4 = f >> 6, ln = f & 63;
    *(u32x2*)(smem + L_KVL + ((hc * 2 + (q4 >> 1)) * 64 + ln) * 16 + (q4 & 1) * 8) = (u32x2){pack2(s[0], s[1]), pack2(s[2], s[3])};
    if (f < 8)
      *(u32x2*)(smem + L_ZL + hc * 64 + (f & 1) * 32 + (f >> 1) * 8) = (u32x2){pack2(zs[0], zs[1]), pack2(zs[2], zs[3])};
  }
  RAW_BAR();
  {
    const u32x4 k0 = *(const u32x4*)(smem + L_KVL + (wc * 2) * 1024 + lane * 16);
    const u32x4 k1 = *(const u32x4*)(smem + L_KVL + (wc * 2 + 1) * 1024 + lane * 16);
    const u32x4 z0 = *(const u32x4*)(smem + L_ZL + wc * 64 + h * 32), z1 = *(const u32x4*)(smem + L_ZL + wc * 64 + h * 32 + 16);
    f32x16 X, Dn;
#pragma unroll
    for (int k = 0; k < 16; ++k) { X[k] = 0.f; Dn[k] = 0.f; }
    const u32x4 p0 = {qp[0], qp[1], qp[2], qp[3]}, p1 = {qp[4], qp[5], qp[6], qp[7]};
    X = mfma32(k0, p0, X); Dn = mfma32(z0, p0, Dn);
    X = mfma32(k1, p1, X); Dn = mfma32(z1, p1, Dn);
    const float ri = __builtin_amdgcn_rcpf(rbf(rbf(Dn[0]) + 1e-6f));
    u32x4 o0, o1;
#pragma unroll
    for (int i = 0; i < 4; ++i) {
      o0[i] = pack2(rbf(X[2 * i]) * ri, rbf(X[2 * i + 1]) * ri);
      o1[i] = pack2(rbf(X[8 + 2 * i]) * ri, rbf(X[8 + 2 * i + 1]) * ri);
    }
    const unsigned oo = WS_O + ((rowtile * 32 + head) * 2) * 1024 + lane * 16;
    STAMPT(1, 6, 0);
    STW(o0, oo); STW(o1, oo + 1024);
  }
  WAIT_V(0);
  STAMPT(1, 7, 0);
  RAW_BAR();
  if (tid == 0) st32(ws, SY_FB + rb * 256 + hp * 4, 1u);
  STAMPT(0, 6, 0);

  const bool has_mlp = bid < 224;
  float* xs = (float*)smem;
  float* hs = (float*)(smem + 8192);
  float* red = (float*)(smem + 28672);
  float* act = (float*)(smem + 17408);
  float* sgm = (float*)(smem + 17536);
  float* yh = (float*)(smem + 18432);

  if (wave < 4) {
    poll_flags(ws, SY_FB + (rp >> 1) * 256 + (lane & 15) * 4, wave == 0);
    RAW_BAR();
    STAMPT(0, 7, 0);
    float hv[32];
    const int erow = tid >> 2, eseg = tid & 3;
    const size_t ego = (size_t)(rp * 64 + erow) * D_ + cb * 128 + eseg * 32;
#pragma unroll
    for (int t = 0; t < 6; ++t) stageO(t, t * K2_STAGE);
    f32x16 a2[4];
#pragma unroll
    for (int i = 0; i < 4; ++i)
#pragma unroll
      for (int k = 0; k < 16; ++k) a2[i][k] = 0.f;
    const int woff = kh * 8192 + h * 512 + trl;
    const int poff = 16384 + wave * 2048 + lane * 16;
    struct Frag2 { u32x4 o[2]; u32x2 lo[2][4], hi[2][4]; };
    Frag2 GA, GB;
    auto ld2 = [&](Frag2& N, int sn, int st, int nt) {
      const char* base = smem + sn + woff;
      N.lo[st][nt] = trread(base + ((2 * st) * 2 + (nt >> 1)) * 1024 + (nt & 1) * 256);
      N.hi[st][nt] = trread(base + ((2 * st + 1) * 2 + (nt >> 1)) * 1024 + (nt & 1) * 256);
    };
    auto body2 = [&](Frag2& C, Frag2& N, int t, bool LD) {
      const int sn = ((t + 1) % 6) * K2_STAGE, sb = (t % 6) * K2_STAGE;
      const bool ST = t + 6 < 16;
      const char* sw = baseW + (size_t)(t + 6) * (32 * D_ * 2);
      const char* sp = baseO + (size_t)(t + 6) * 2048;
      const unsigned dw = lds0 + sb + kh * 8192 + (2 * rt) * 2048, dp = lds0 + sb + 16384 + wave * 2048;
#pragma unroll
      for (int st = 0; st < 2; ++st)
#pragma unroll
        for (int nt = 0; nt < 4; ++nt) {
          const int i = st * 4 + nt;
          a2[nt] = mfma32((u32x4){C.lo[st][nt][0], C.lo[st][nt][1], C.hi[st][nt][0], C.hi[st][nt][1]}, C.o[st], a2[nt]);
          if (ST) {
            if (i == 0) glds16(voW, sw, dw);
            if (i == 1) glds16(voW + 128, sw, dw + 1024);
            if (i == 2) glds16(voW + 8 * D_ * 2, sw, dw + 2048);
            if (i == 3) glds16(voW + 8 * D_ * 2 + 128, sw, dw + 3072);
            if (i == 4) glds16_sc1(voL, sp, dp);
            if (i == 5) glds16_sc1(voL + 1024, sp, dp + 1024);
          }
          if (LD) {
            if (nt == 0) N.o[st] = *(const u32x4*)(smem + sn + poff + st * 1024);
            ld2(N, sn, st, nt);
          }
          SB0();
        }
    };
    WAIT_V(10); RAW_BAR();
    GA.o[0] = *(const u32x4*)(smem + poff); GA.o[1] = *(const u32x4*)(smem + poff + 1024);
#pragma unroll
    for (int st = 0; st < 2; ++st)
#pragma unroll
      for (int nt = 0; nt < 4; ++nt) ld2(GA, 0, st, nt);
    WAIT_V(8);  RAW_BAR(); body2(GA, GB, 0, true);
    WAIT_V(12); RAW_BAR(); body2(GB, GA, 1, true);
    WAIT_V(16); RAW_BAR(); body2(GA, GB, 2, true);
    WAIT_V(20); RAW_BAR(); body2(GB, GA, 3, true);
    WAIT_V(24); RAW_BAR(); body2(GA, GB, 4, true);
    WAIT_V(24); RAW_BAR(); body2(GB, GA, 5, true);
    WAIT_V(24); RAW_BAR(); body2(GA, GB, 6, true);
    WAIT_V(24); RAW_BAR(); body2(GB, GA, 7, true);
    WAIT_V(24); RAW_BAR(); body2(GA, GB, 8, true);
    WAIT_V(24); RAW_BAR(); body2(GB, GA, 9, true);
    WAIT_V(24); RAW_BAR(); body2(GA, GB, 10, true);
    WAIT_V(18); RAW_BAR(); body2(GB, GA, 11, true);
    WAIT_V(12); RAW_BAR(); body2(GA, GB, 12, true);
    WAIT_V(6);  RAW_BAR(); body2(GB, GA, 13, true);
    WAIT_V(0);  RAW_BAR(); body2(GA, GB, 14, true);
    u32x4 xv[4], wv[4];
#pragma unroll
    for (int c = 0; c < 4; ++c) { xv[c] = *(const u32x4*)(x + ego + c * 8); wv[c] = *(const u32x4*)(w_r + cb * 128 + eseg * 32 + c * 8); }
    RAW_BAR();             body2(GB, GA, 15, false);
    STAMPT(0, 8, 0);
    float* P = (float*)smem;
    {
      float* pr = P + (kh * 64 + 32 * rt + r) * 132 + 4 * h;
#pragma unroll
      for (int nt = 0; nt < 4; ++nt)
#pragma unroll
        for (int q4 = 0; q4 < 4; ++q4)
          *(f32x4*)(pr + 32 * nt + 8 * q4) = (f32x4){a2[nt][4 * q4], a2[nt][4 * q4 + 1], a2[nt][4 * q4 + 2], a2[nt][4 * q4 + 3]};
    }
    RAW_BAR();
    STAMPT(1, 8, 0);
    {
      const float* p0 = P + erow * 132 + eseg * 32;
      const float* p1 = p0 + 64 * 132;
      float s = 0.f, pw = 0.f, sw_ = 0.f;
#pragma unroll
      for (int c = 0; c < 4; ++c) {
        f32x4 a0 = *(const f32x4*)(p0 + c * 8), a1 = *(const f32x4*)(p0 + c * 8 + 4);
        a0 += *(const f32x4*)(p1 + c * 8); a1 += *(const f32x4*)(p1 + c * 8 + 4);
        u32x4 hb;
        hb[0] = pack2(bflo(xv[c][0]) + rbf(a0[0]), bfhi(xv[c][0]) + rbf(a0[1]));
        hb[1] = pack2(bflo(xv[c][1]) + rbf(a0[2]), bfhi(xv[c][1]) + rbf(a0[3]));
        hb[2] = pack2(bflo(xv[c][2]) + rbf(a1[0]), bfhi(xv[c][2]) + rbf(a1[1]));
        hb[3] = pack2(bflo(xv[c][3]) + rbf(a1[2]), bfhi(xv[c][3]) + rbf(a1[3]));
#pragma unroll
        for (int i = 0; i < 4; ++i) {
          const float h0 = bflo(hb[i]), h1 = bfhi(hb[i]), w0 = bflo(wv[c][i]), w1 = bfhi(wv[c][i]);
          hv[c * 8 + 2 * i] = h0; hv[c * 8 + 2 * i + 1] = h1;
          s += h0 + h1; pw = __builtin_fmaf(h0, w0, pw); pw = __builtin_fmaf(h1, w1, pw); sw_ += w0 + w1;
        }
      }
      const float cm = quad_sum(s) * (1.0f / 128.0f);
      float m2 = 0.f;
#pragma unroll
      for (int i = 0; i < 32; ++i) { const float d = hv[i] - cm; m2 = __builtin_fmaf(d, d, m2); }
      m2 = quad_sum(m2); pw = quad_sum(pw); sw_ = quad_sum(sw_);
      if (eseg == 0) STW(((u32x4){__float_as_uint(cm), __float_as_uint(m2) | 1u, __float_as_uint(pw), __float_as_uint(sw_) | 1u}),
                         WS_ST2 + ((rp * 8 + cb) * 64 + erow) * 16);
    }
    STAMPT(0, 9, 0);
    int lc0, lc1;
    {
      u32x4 pu_[8];
      for (unsigned s_ = 0; s_ < SPIN_CAP; ++s_) {
        unsigned ok = 1u;
#pragma unroll
        for (int j = 0; j < 8; ++j) { pu_[j] = LDW(WS_ST2 + ((rp * 8 + j) * 64 + lane) * 16); ok &= pu_[j][1] & pu_[j][3]; }
        if (__all(ok & 1u)) break;
        asm volatile("" ::: "memory");
      }
      STAMPT(0, 10, 0);
      float cs = 0.f, m2 = 0.f, pw = 0.f, sw_ = 0.f;
#pragma unroll
      for (int j = 0; j < 8; ++j) { const f32x4 pt = __builtin_bit_cast(f32x4, pu_[j]); cs += pt[0]; m2 += pt[1]; pw += pt[2]; sw_ += pt[3]; }
      const float mean = cs * 0.125f;
      float dd = 0.f;
#pragma unroll
      for (int j = 0; j < 8; ++j) { const float d = __uint_as_float(pu_[j][0]) - mean; dd = __builtin_fmaf(d, d, dd); }
      const float var = (m2 + 128.0f * dd) * (1.0f / 1024.0f);
      const float mb = rbf(mean), sd = rbf(__builtin_amdgcn_sqrtf(rbf(rbf(var) + 1e-5f)));
      float sc = rbf((pw - mb * sw_) / sd);
      if (!(sc == sc)) sc = -INFINITY;
      unsigned b0 = mk_key(sc, rp * 64 + lane), b1 = 0;
      top2_wave(b0, b1);
      if (tid == 0) st64(ws, SY_FD + cb * 512 + rp * 8, ((u64)b1 << 32) | b0);
      lc0 = key_idx(b0) & (S_ - 1); lc1 = key_idx(b1) & (S_ - 1);
      STAMPT(1, 11, 0);
    }
    float mb, rs;
    {
      u32x4 pa, pb;
      for (unsigned s_ = 0; s_ < SPIN_CAP; ++s_) {
        pa = LDW(WS_ST2 + ((rp * 8 + 2 * eseg) * 64 + erow) * 16);
        pb = LDW(WS_ST2 + ((rp * 8 + 2 * eseg + 1) * 64 + erow) * 16);
        if (__all(pa[1] & pa[3] & pb[1] & pb[3] & 1u)) break;
        asm volatile("" ::: "memory");
      }
      const float ca = __uint_as_float(pa[0]), cb_ = __uint_as_float(pb[0]);
      const float mean = quad_sum(ca + cb_) * 0.125f;
      const float d0 = ca - mean, d1 = cb_ - mean;
      const float var = quad_sum((__uint_as_float(pa[1]) + __uint_as_float(pb[1])) + 128.0f * (d0 * d0 + d1 * d1)) * (1.0f / 1024.0f);
      mb = rbf(mean);
      rs = 1.0f / rbf(__builtin_amdgcn_sqrtf(rbf(rbf(var) + 1e-5f)));
    }
    unsigned nq[16];
#pragma unroll
    for (int i = 0; i < 16; ++i) {
      const unsigned dq = pack2(hv[2 * i] - mb, hv[2 * i + 1] - mb);
      nq[i] = pack2(bflo(dq) * rs, bfhi(dq) * rs);
    }
    const int grow = rp * 64 + erow;
#pragma unroll
    for (int tk = 0; tk < 2; ++tk) {
      const int sel = tk ? lc1 : lc0;
      if (__any(grow == sel)) {
        char* tb = smem + 69632 + wave * 1024;
        if (grow == sel) {
#pragma unroll
          for (int i = 0; i < 16; ++i)
            *(u32x4*)(tb + eseg * 256 + i * 16) = (u32x4){nq[i] << 16, __float_as_uint(hv[2 * i]) | 1u, nq[i] & 0xffff0000u, __float_as_uint(hv[2 * i + 1]) | 1u};
        }
        __builtin_amdgcn_wave_barrier();
        const u32x4 v4 = *(const u32x4*)(tb + lane * 16);
#pragma unroll
        for (int rr_ = 0; rr_ < XSR; ++rr_) STW(v4, WS_XS + rr_ * 524288 + rp * 16384 + tk * 8192 + cb * 1024 + lane * 16);
        __builtin_amdgcn_wave_barrier();
      }
    }
    if (wave == 0) {
      u64 cc = 0;
      for (unsigned s_ = 0; s_ < SPIN_CAP; ++s_) {
        cc = ld64(ws, SY_FD + cb * 512 + (lane & 31) * 8);
        if (__all(cc != 0ull)) break;
      }
      STAMPT(1, 13, 0);
      unsigned c0 = lane < 32 ? (unsigned)cc : 0u, c1 = lane < 32 ? (unsigned)(cc >> 32) : 0u;
      top2_wave(c0, c1);
      const unsigned w0 = key_idx(c0) & (S_ - 1), w1 = key_idx(c1) & (S_ - 1);
      const unsigned k0 = __builtin_amdgcn_readlane((unsigned)cc, w0 >> 6), k1 = __builtin_amdgcn_readlane((unsigned)cc, w1 >> 6);
      if (lane == 0) { misc[3] = w0; misc[4] = w1; misc[5] = (w0 >> 6) * 2 + (k0 == c0 ? 0 : 1); misc[6] = (w1 >> 6) * 2 + (k1 == c1 ? 0 : 1); }
    }
    RAW_BAR();
    STAMPT(0, 11, 0);
    const int sel0 = misc[3], sel1 = misc[4];
    RAW_BAR();
    if (grow != sel0 && grow != sel1) {
#pragma unroll
      for (int c = 0; c < 4; ++c) {
        u32x4 ob;
#pragma unroll
        for (int i = 0; i < 4; ++i) ob[i] = pack2(hv[8 * c + 2 * i] + bflo(nq[4 * c + i]), hv[8 * c + 2 * i + 1] + bfhi(nq[4 * c + i]));
        *(u32x4*)(out + ego + c * 8) = ob;
      }
    }
    STAMPT(0, 12, 0);
    if (has_mlp) { RAW_BAR(); RAW_BAR(); }
  } else {
    const int fs = has_mlp ? (bid & 7) * 28 + (bid >> 3) : (bid & 7) * 28;
    const int mt = tid & 255;
    u32x4 wgr[8], wur[8], wdr[8];
    const int krow = (fs & 3) * 256 + (mt >> 6) * 64 + (lane >> 3);
    const u16* pg = wg + (size_t)krow * F_ + (fs >> 2) * 64 + (lane & 7) * 8;
    const u16* pu = wu + (size_t)krow * F_ + (fs >> 2) * 64 + (lane & 7) * 8;
    const u16* pd = wd + (size_t)(fs * 16 + h * 8) * D_ + ((mt >> 6) * 32 + r) * 8;
    RAW_BAR(); RAW_BAR();
#pragma unroll
    for (int i = 0; i < 8; ++i) {
      asm volatile("global_load_dwordx4 %0, %1, off nt" : "=a"(wgr[i]) : "v"(pg + (size_t)i * 8 * F_) : "memory");
      asm volatile("global_load_dwordx4 %0, %1, off nt" : "=a"(wdr[i]) : "v"(pd + (size_t)i * D_) : "memory");
      RAW_BAR();
      asm volatile("global_load_dwordx4 %0, %1, off nt" : "=a"(wur[i]) : "v"(pu + (size_t)i * 8 * F_) : "memory");
      RAW_BAR();
    }
    RAW_BAR();
    WAIT_V(0);
#pragma unroll
    for (int i = 0; i < 8; ++i) asm volatile("" : "+a"(wgr[i]), "+a"(wur[i]), "+a"(wdr[i]));
    unsigned wr2[8];
    if (wave < 6) {
#pragma unroll
      for (int j = 0; j < 8; ++j) wr2[j] = ((const unsigned*)w_r)[j * 64 + lane];
    }
    RAW_BAR();
    if (wave < 6) {
      const int tk = wave - 4;
      u32x4 q[8];
      const unsigned xo = WS_XS + ((bid >> 3) & (XSR - 1)) * 524288u + (unsigned)misc[5 + tk] * 8192u;
      for (unsigned s_ = 0; s_ < SPIN_CAP; ++s_) {
        unsigned ok = 1u;
#pragma unroll
        for (int j = 0; j < 8; ++j) { q[j] = LDW(xo + j * 1024 + lane * 16); ok &= q[j][1] & q[j][3]; }
        if (__all(ok & 1u)) break;
        asm volatile("" ::: "memory");
        __builtin_amdgcn_s_sleep(XSL1);
      }
      float rr = 0.f;
#pragma unroll
      for (int j = 0; j < 8; ++j) {
        rr = __builtin_fmaf(__uint_as_float(q[j][0]), bflo(wr2[j]), rr); rr = __builtin_fmaf(__uint_as_float(q[j][2]), bfhi(wr2[j]), rr);
        *(u32x2*)(xs + tk * 1024 + j * 128 + lane * 2) = (u32x2){q[j][0], q[j][2]};
        *(u32x2*)(hs + tk * 1024 + j * 128 + lane * 2) = (u32x2){q[j][1] & ~1u, q[j][3] & ~1u};
      }
      rr = rbf(wave_sum(rr));
      if (lane == 0) sgm[tk] = rbf(1.0f / (1.0f + __expf(-rr)));
    }
    RAW_BAR();
    if (has_mlp) {
      {
        f32x2 ag[8], au[8];
#pragma unroll
        for (int c = 0; c < 8; ++c) { ag[c] = (f32x2){0.f, 0.f}; au[c] = (f32x2){0.f, 0.f}; }
#pragma unroll
        for (int i = 0; i < 8; ++i) {
          const f32x2 xx = {xs[krow + 8 * i], xs[1024 + krow + 8 * i]};
#pragma unroll
          for (int c = 0; c < 4; ++c) {
            ag[2 * c] += xx * bflo(wgr[i][c]); ag[2 * c + 1] += xx * bfhi(wgr[i][c]);
            au[2 * c] += xx * bflo(wur[i][c]); au[2 * c + 1] += xx * bfhi(wur[i][c]);
          }
        }
        float v[32];
#pragma unroll
        for (int c = 0; c < 8; ++c) { v[c] = ag[c][0]; v[8 + c] = ag[c][1]; v[16 + c] = au[c][0]; v[24 + c] = au[c][1]; }
#pragma unroll
        for (int i = 0; i < 16; ++i) {
          auto sw = __builtin_amdgcn_permlane32_swap(__float_as_uint(v[i]), __float_as_uint(v[16 + i]), false, false);
          v[i] = __uint_as_float(sw[0]) + __uint_as_float(sw[1]);
        }
#pragma unroll
        for (int i = 0; i < 8; ++i) {
          auto sw = __builtin_amdgcn_permlane16_swap(__float_as_uint(v[i]), __float_as_uint(v[8 + i]), false, false);
          v[i] = __uint_as_float(sw[0]) + __uint_as_float(sw[1]);
        }
        const bool up = (lane & 8) != 0;
#pragma unroll
        for (int i = 0; i < 4; ++i) {
          const float keep = up ? v[4 + i] : v[i], give = up ? v[i] : v[4 + i];
          v[i] = keep + dppf<0x128>(give);
        }
        *(f32x4*)(red + (wave - 4) * 256 + (lane >> 5) * 128 + ((lane >> 4) & 1) * 64 + (lane & 7) * 8 + ((lane >> 3) & 1) * 4) = (f32x4){v[0], v[1], v[2], v[3]};
      }
      STAMPT(1, 14, 256);
      RAW_BAR();
      if (wave == 4) {
        f32x4 s4 = *(const f32x4*)(red + lane * 4);
#pragma unroll
        for (int w2 = 1; w2 < 4; ++w2) s4 += *(const f32x4*)(red + w2 * 256 + lane * 4);
        u32x4 t4 = __builtin_bit_cast(u32x4, s4); t4[1] |= 1u; t4[3] |= 1u;
        STW(t4, WS_GU + fs * 1024 + lane * 16);
        u32x4 q[4];
        for (unsigned s_ = 0; s_ < SPIN_CAP; ++s_) {
          unsigned ok = 1u;
#pragma unroll
          for (int j = 0; j < 4; ++j) { q[j] = LDW(WS_GU + ((fs & ~3) + j) * 1024 + lane * 16); ok &= q[j][1] & q[j][3]; }
          if (__all(ok & 1u)) break;
          asm volatile("" ::: "memory");
        }
        const f32x4 tot = (__builtin_bit_cast(f32x4, q[0]) + __builtin_bit_cast(f32x4, q[1])) + (__builtin_bit_cast(f32x4, q[2]) + __builtin_bit_cast(f32x4, q[3]));
        f32x4 a4;
#pragma unroll
        for (int i = 0; i < 4; ++i) {
          auto sw = __builtin_amdgcn_permlane32_swap(__float_as_uint(tot[i]), __float_as_uint(tot[i]), false, false);
          const float gs = rbf(__uint_as_float(sw[0])), us = rbf(__uint_as_float(sw[1]));
          const float sg = rbf(gs / (1.0f + __expf(-gs)));
          a4[i] = rbf(sg * us);
        }
        if (lane < 32 && ((lane & 15) >> 2) == (fs & 3)) *(f32x4*)(act + (lane >> 4) * 16 + (lane & 3) * 4) = a4;
      }
      RAW_BAR();
      float y0[8], y1[8];
#pragma unroll
      for (int c = 0; c < 8; ++c) { y0[c] = 0.f; y1[c] = 0.f; }
#pragma unroll
      for (int i = 0; i < 8; ++i) {
        const float a0 = act[h * 8 + i], a1 = act[16 + h * 8 + i];
#pragma unroll
        for (int c = 0; c < 4; ++c) {
          const float dl = bflo(wdr[i][c]), dh = bfhi(wdr[i][c]);
          y0[2 * c] += a0 * dl; y0[2 * c + 1] += a0 * dh; y1[2 * c] += a1 * dl; y1[2 * c + 1] += a1 * dh;
        }
      }
      u32x4 ya, yb;
#pragma unroll
      for (int c = 0; c < 8; ++c) {
        auto sw = __builtin_amdgcn_permlane32_swap(__float_as_uint(y0[c]), __float_as_uint(y1[c]), false, false);
        const unsigned v = __float_as_uint(__uint_as_float(sw[0]) + __uint_as_float(sw[1])) | (c & 1);
        if (c < 4) ya[c] = v; else yb[c - 4] = v;
      }
      const unsigned yo = WS_YP + fs * 8192 + h * 4096 + ((mt >> 6) * 32 + r) * 32;
      STW(ya, yo); STW(yb, yo + 16);
      STAMPT(1, 15, 256);
      STAMPT(0, 13, 256);
    }
  }
  if (!has_mlp) {
    const int j = bid - 224, tk = j >> 4, c0 = (j & 15) * 64;
    const int pg = tid >> 4, c4 = tid & 15;
    u32x4 pv[7];
    __builtin_amdgcn_s_sleep(YSL0);
    for (unsigned s_ = 0; s_ < SPIN_CAP; ++s_) {
      unsigned ok = 1u;
#pragma unroll
      for (int p = 0; p < 7; ++p) { pv[p] = LDW(WS_YP + (pg * 7 + p) * 8192 + tk * 4096 + c0 * 4 + c4 * 16); ok &= pv[p][1] & pv[p][3]; }
      if (__all(ok & 1u)) break;
      asm volatile("" ::: "memory");
      __builtin_amdgcn_s_sleep(YSL1);
    }
    STAMPT(0, 13, 0);
    f32x4 s = {0.f, 0.f, 0.f, 0.f};
#pragma unroll
    for (int p = 0; p < 7; ++p) s += __builtin_bit_cast(f32x4, pv[p]);
    float* rd = (float*)(smem + 32768);
    *(f32x4*)(rd + pg * 64 + c4 * 4) = s;
    RAW_BAR();
    if (tid < 64) {
      float y = 0.f;
#pragma unroll
      for (int p = 0; p < 32; ++p) y += rd[p * 64 + tid];
      const float yy = rbf(rbf(y) * sgm[tk]);
      const float t = rbf(xs[tk * 1024 + c0 + tid] + yy);
      out[(size_t)misc[3 + tk] * D_ + c0 + tid] = (u16)(pack2(hs[tk * 1024 + c0 + tid] + t, 0.f) & 0xffffu);
    }
    STAMPT(0, 14, 0);
  }
}

extern "C" void dbg_layout(long long* l) {
  l[0] = -1; l[1] = -1; l[2] = -1; l[3] = -1; l[4] = -1;
}

extern "C" void kernel_launch(void* const* d_in, const int* in_sizes, int n_in, void* d_out, int out_size,
                              void* d_ws, size_t ws_size, hipStream_t stream) {
  (void)hipFuncSetAttribute((const void*)fused, hipFuncAttributeMaxDynamicSharedMemorySize, LDS_TOTAL);
  fused<<<256, 512, LDS_TOTAL, stream>>>((const u16*)d_in[0], (const u16*)d_in[1], (const u16*)d_in[2], (const u16*)d_in[4],
                                         (const u16*)d_in[5], (const u16*)d_in[6], (const u16*)d_in[7], (u16*)d_out, (char*)d_ws);
}
```

```cpp
#include <hip/hip_runtime.h>
#include <stdint.h>

#define LAS __attribute__((address_space(3)))
#define GAS __attribute__((address_space(1)))
#define DI __device__ __forceinline__

#define XSR 2
#define GPF 5
#define GOW 10
#define NW4 24
#define NE 12
#define WPOL
#define SGSPLIT 15
#define NC 22
#define THR 6
#define NW1 12
#define SLA 8
#define SLB 8
constexpr int CODE_PER = 11;
constexpr int XSL1 = 2;
constexpr int YSL0 = 64, YSL1 = 4;
typedef unsigned short u16;
typedef unsigned long long u64;
typedef __attribute__((ext_vector_type(2))) __bf16 bf16x2;
typedef __attribute__((ext_vector_type(8))) __bf16 bf16x8;
typedef __attribute__((ext_vector_type(4))) short s16x4;
typedef __attribute__((ext_vector_type(16))) float f32x16;
typedef __attribute__((ext_vector_type(4))) float f32x4;
typedef __attribute__((ext_vector_type(2))) float f32x2;
typedef __attribute__((ext_vector_type(4))) unsigned u32x4;
typedef __attribute__((ext_vector_type(2))) unsigned u32x2;
typedef GAS unsigned gu32;
typedef GAS unsigned long long gu64;

constexpr int S_ = 2048, D_ = 1024, F_ = 3584;

constexpr unsigned SY_CH = 0, SY_GO = 1024, SY_RE = 2048, SY_FB = 4096, SY_FD = 8192;
constexpr unsigned WS_RS  = 16384;
constexpr unsigned WS_CS  = WS_RS + 256 * 1024;
constexpr unsigned KVB    = 4352;
constexpr unsigned WS_KVP = WS_CS + 256 * 768;
constexpr unsigned WS_ST2 = WS_KVP + 256 * KVB;
constexpr unsigned WS_GU  = WS_ST2 + 256 * 2048;
constexpr unsigned WS_XS  = WS_GU + 256 * 1024;
constexpr unsigned WS_YP  = WS_XS + XSR * 524288;
constexpr unsigned WS_O   = WS_YP + 224 * 8192;
constexpr unsigned WS_END = WS_O + S_ * D_ * 2;
constexpr size_t WS_DIAG  = 64u << 20;
#define STAMPT(kid, i, T) do {} while (0)

DI float bflo(unsigned u) { return __uint_as_float(u << 16); }
DI float bfhi(unsigned u) { return __uint_as_float(u & 0xffff0000u); }
DI float rbf(float x) { return (float)(__bf16)x; }
DI unsigned pack2(float a, float b) { bf16x2 c; c[0] = (__bf16)a; c[1] = (__bf16)b; return __builtin_bit_cast(unsigned, c); }
DI f32x4 mfma16(u32x4 a, u32x4 b, f32x4 c) {
  return __builtin_amdgcn_mfma_f32_16x16x32_bf16(__builtin_bit_cast(bf16x8, a), __builtin_bit_cast(bf16x8, b), c, 0, 0, 0);
}
DI f32x16 mfma32(u32x4 a, u32x4 b, f32x16 c) {
  return __builtin_amdgcn_mfma_f32_32x32x16_bf16(__builtin_bit_cast(bf16x8, a), __builtin_bit_cast(bf16x8, b), c, 0, 0, 0);
}
DI u32x2 trread(const char* p) {
  s16x4 v = __builtin_amdgcn_ds_read_tr16_b64_v4i16((LAS s16x4*)p);
  return __builtin_bit_cast(u32x2, v);
}
DI unsigned opq(unsigned v) { asm volatile("" : "+v"(v)); return v; }
DI u32x2 trread(unsigned base, int off) {
  s16x4 v = __builtin_amdgcn_ds_read_tr16_b64_v4i16((LAS s16x4*)((LAS char*)(size_t)base + off));
  return __builtin_bit_cast(u32x2, v);
}
DI u32x4 ldsr(unsigned base, int off) { return *(const LAS u32x4*)((const LAS char*)(size_t)base + off); }
DI u32x4 cat(u32x2 a, u32x2 b) { return (u32x4){a[0], a[1], b[0], b[1]}; }
DI u32x4 pick(int c, u32x4 a, u32x4 b) { const unsigned m = c ? ~0u : 0u; return (a & m) | (b & ~m); }
DI float dot2(unsigned a, unsigned b, float c) {
  return __builtin_amdgcn_fdot2_f32_bf16(__builtin_bit_cast(bf16x2, a), __builtin_bit_cast(bf16x2, b), c, false);
}
DI const char* uni(const void* p) {
  const u64 v = (u64)p;
  return (const char*)(((u64)(unsigned)__builtin_amdgcn_readfirstlane((int)(v >> 32)) << 32) | (unsigned)__builtin_amdgcn_readfirstlane((int)v));
}
DI void glds16(unsigned voff, const void* sbase, unsigned lds) {
  asm volatile("s_mov_b32 m0, %2\n\ts_nop 0\n\tglobal_load_lds_dwordx4 %0, %1" ::"v"(voff), "s"(uni(sbase)), "s"(__builtin_amdgcn_readfirstlane(lds)) : "memory");
}
DI float xh_sum(float v) {
  auto r = __builtin_amdgcn_permlane32_swap(__float_as_uint(v), __float_as_uint(v), false, false);
  return __uint_as_float(r[0]) + __uint_as_float(r[1]);
}
template <int CTRL> DI unsigned dppu(unsigned v) { return (unsigned)__builtin_amdgcn_update_dpp(0, (int)v, CTRL, 0xf, 0xf, true); }
template <int CTRL> DI float dppf(float v) { return __uint_as_float(dppu<CTRL>(__float_as_uint(v))); }
constexpr int DPP_X1 = 0xB1, DPP_X2 = 0x4E, DPP_HMIR = 0x141, DPP_MIR = 0x140, DPP_ROR8 = 0x128;
DI float oct_sum(float v) { v += dppf<DPP_X1>(v); v += dppf<DPP_X2>(v); v += dppf<DPP_HMIR>(v); return v; }
DI float x16_sum(float v) {
  auto r = __builtin_amdgcn_permlane16_swap(__float_as_uint(v), __float_as_uint(v), false, false);
  return __uint_as_float(r[0]) + __uint_as_float(r[1]);
}
DI unsigned phi_pair(f32x2 t) {
  const f32x2 u = t + 1.000001f;
  const f32x2 l = t * 1.44269504088896340736f;
  const f32x2 e = (f32x2){__builtin_amdgcn_exp2f(l[0]), __builtin_amdgcn_exp2f(l[1])} + 1e-6f;
  return pack2(__builtin_amdgcn_fmed3f(e[0], u[0], 1.000001f), __builtin_amdgcn_fmed3f(e[1], u[1], 1.000001f));
}
#define RLX_AGENT __ATOMIC_RELAXED, __HIP_MEMORY_SCOPE_AGENT
DI void st32(char* ws, unsigned off, unsigned v) { __hip_atomic_store((gu32*)(ws + off), v, RLX_AGENT); }
DI void st64(char* ws, unsigned off, u64 v) { __hip_atomic_store((gu64*)(ws + off), v, RLX_AGENT); }
DI unsigned ld32(char* ws, unsigned off) { return __hip_atomic_load((gu32*)(ws + off), RLX_AGENT); }
DI u64 ld64(char* ws, unsigned off) { return __hip_atomic_load((gu64*)(ws + off), RLX_AGENT); }
#define WAITV(n) asm volatile("s_waitcnt vmcnt(%0)" :: "n"(n) : "memory")
#define BAR() do { asm volatile("" ::: "memory"); __builtin_amdgcn_s_barrier(); asm volatile("" ::: "memory"); } while (0)
#define LBAR() do { asm volatile("s_waitcnt lgkmcnt(0)" ::: "memory"); __builtin_amdgcn_s_barrier(); asm volatile("" ::: "memory"); } while (0)
#define SB0() __builtin_amdgcn_sched_barrier(0)
constexpr unsigned SPIN_CAP = 1u << 22;
constexpr unsigned EMPTY = 0xFFFFFFFFu;
DI u32x4 seal(u32x4 v) { v[1] = min(v[1], 0xFFFFFFFEu); v[3] = min(v[3], 0xFFFFFFFEu); return v; }
DI unsigned mk_key(float v, unsigned idx) {
  const unsigned u = __float_as_uint(v) >> 16;
  const unsigned o = (u & 0x8000u) ? (~u & 0xffffu) : (u | 0x8000u);
  return (o << 16) | (0xFFFFu - idx);
}
DI unsigned key_idx(unsigned k) { return (0xFFFFu - (k & 0xFFFFu)) & (S_ - 1); }
DI float key_val(unsigned k) { const unsigned o = k >> 16; return __uint_as_float(((o & 0x8000u) ? (o & 0x7fffu) : (~o & 0xffffu)) << 16); }
DI void top2_mix(unsigned& b0, unsigned& b1, unsigned c0, unsigned c1) {
  const unsigned lo = min(b0, c0), mx = max(b1, c1);
  b0 = max(b0, c0); b1 = max(lo, mx);
}
DI void top2_wave(unsigned& b0, unsigned& b1) {
  top2_mix(b0, b1, dppu<DPP_X1>(b0), dppu<DPP_X1>(b1));
  top2_mix(b0, b1, dppu<DPP_X2>(b0), dppu<DPP_X2>(b1));
  top2_mix(b0, b1, dppu<DPP_HMIR>(b0), dppu<DPP_HMIR>(b1));
  top2_mix(b0, b1, dppu<DPP_MIR>(b0), dppu<DPP_MIR>(b1));
  { auto p = __builtin_amdgcn_permlane16_swap(b0, b0, false, false); auto q = __builtin_amdgcn_permlane16_swap(b1, b1, false, false);
    b0 = p[0]; b1 = q[0]; top2_mix(b0, b1, p[1], q[1]); }
  { auto p = __builtin_amdgcn_permlane32_swap(b0, b0, false, false); auto q = __builtin_amdgcn_permlane32_swap(b1, b1, false, false);
    b0 = p[0]; b1 = q[0]; top2_mix(b0, b1, p[1], q[1]); }
}

constexpr int L_A = 0, L_Q = 65536, L_KV = 98304, LDS_TOTAL = 163840;
constexpr int L_M  = L_KV;
constexpr int L_SA = L_M, L_SB = L_M + 512, L_CS = L_M + 1024;
constexpr int L_MISC = L_M + 1792;
constexpr int L_XCH = L_M + 2048;
constexpr int L_KVL = L_M + 11264;
constexpr int L_ZL = L_M + 15360;
constexpr int L_XS = L_M + 3072;
constexpr int L_RED = L_M + 5120;
constexpr int L_ACT = L_M + 9216;
constexpr int L_TB = L_M + 9472;
constexpr int L_X2 = 81920;
DI constexpr int wslot(int j) { return j < 12 ? 114688 + j * 4096 : (j - 12) * 4096; }

__global__ __launch_bounds__(512, 2) void fused(const u16* __restrict__ x, const u16* __restrict__ wqkv,
                                                const u16* __restrict__ wo, const u16* __restrict__ w_r,
                                                const u16* __restrict__ wg, const u16* __restrict__ wu,
                                                const u16* __restrict__ wd, u16* __restrict__ out, char* __restrict__ ws) {
  extern __shared__ __attribute__((aligned(16))) char smem[];
  u64 pc0; asm volatile("s_getpc_b64 %0" : "=s"(pc0));
  const int tid = threadIdx.x, lane = tid & 63;
  const int wave = __builtin_amdgcn_readfirstlane(tid >> 6);
  STAMPT(0, 0, 0);
  const int bid = blockIdx.x, xcd = bid & 7, idx = bid >> 3;
  const int rb = (xcd & 1) * 8 + (idx & 7);
  const int hp = (xcd >> 1) * 4 + (idx >> 3);
  const int rp = (xcd & 1) * 8 + (xcd & 2) * 2 + (idx >> 3), cb = (xcd >> 2) * 8 + (idx & 7);
  const int hrot = 8 * (rp & 3);
  const unsigned lds0 = (unsigned)(size_t)smem;
  const __amdgpu_buffer_rsrc_t wsr = __builtin_amdgcn_make_buffer_rsrc(ws, 0, WS_END, 0x00020000);
#define LDW(off) __builtin_amdgcn_raw_buffer_load_b128(wsr, (off), 0, 16)
#define LDWS(off, so) __builtin_amdgcn_raw_buffer_load_b128(wsr, (off), (so), 16)
#define STW(v, off) __builtin_amdgcn_raw_buffer_store_b128((v), wsr, (off), 0, 16)
  const int kph = 4 * (xcd >> 1) + 8 * (xcd & 1), kg_ = (hp + rb) & 3;
  auto korder = [&](int t) { return ((t + kph) & 15) ^ kg_; };
  volatile LAS int* misc = (volatile LAS int*)(size_t)(lds0 + L_MISC);
  const bool has_mlp = bid < 224;
  const int fs = has_mlp ? (bid & 7) * 28 + (bid >> 3) : (bid & 7) * 28;
  const int tcs = (rb & 4) ? 4 : 3;
  const unsigned rs_o = WS_RS + (rb * 16 + korder(0)) * 1024, cs_o = WS_CS + (hp * 16 + korder(tcs)) * 768;
  const u32x4 ONES4 = {0x3F803F80u, 0x3F803F80u, 0x3F803F80u, 0x3F803F80u};

  auto stageW = [&](int j) {
    const int pb_ = lane >> 4;
    const unsigned vo = (unsigned)((8 * (wave & 3) + 4 * (pb_ >> 1) + ((lane >> 2) & 3)) * D_ + (pb_ & 1) * 32 + ((lane & 3) ^ ((pb_ >> 1) * 2)) * 8) * 2u;
    glds16(vo, uni(wo + cb * 64) + (size_t)(j ^ hrot) * (32 * D_ * 2), lds0 + wslot(j) + (wave & 3) * 1024);
  };
  if (wave < 4) {
    if (wave == 1 || (wave == 2 && lane < 48)) STW(((u32x4){EMPTY, EMPTY, EMPTY, EMPTY}), (wave == 1 ? rs_o : cs_o) + lane * 16);
    unsigned cpf = 0;
    if (wave == 0) {
      const int ln = idx * CODE_PER + lane;
      if (lane < CODE_PER) asm volatile("global_load_dword %0, %1, %2" : "=v"(cpf) : "v"(ln * 128), "s"(pc0 & ~255ull) : "memory");
    }
    const int wr = wave >> 1, wc = wave & 1;
    const int r = lane & 15, kq = lane >> 4;
    f32x4 acc[4][6];
    {
      unsigned aO[2], bQ[2], bK[2];
#pragma unroll
      for (int k = 0; k < 2; ++k) aO[k] = opq(lds0 + L_A + (64 * wr + r) * 128 + (((4 * k + kq) ^ ((r >> 1) & 7)) * 16));
#pragma unroll
      for (int c = 0; c < 2; ++c) {
        const unsigned o_ = lds0 + kq * 1024 + wc * 256 + (r >> 2) * 64 + ((c ^ (kq & 1)) * 32) + (lane & 3) * 8;
        bQ[c] = opq(o_ + L_Q); bK[c] = opq(o_ + L_KV);
      }
#pragma unroll
      for (int i = 0; i < 4; ++i)
#pragma unroll
        for (int j = 0; j < 6; ++j) acc[i][j] = (f32x4){0.f, 0.f, 0.f, 0.f};
      struct Fr { u32x4 a[4]; u32x2 bl[6], bh[6]; };
      Fr F[2];
      auto bread = [&](int f, int b, int k, int hi) {
        return f < 2 ? trread(bQ[f & 1], k * 4096 + b * 8192 + hi * 512) : trread(bK[f & 1], k * 4096 + b * 16384 + ((f >> 1) - 1) * 8192 + hi * 512);
      };
      auto step = [&](Fr& C, Fr& N, int b, int k, int MC, int ML) {
        int ld = 0;
        constexpr int ORD[6] = {2, 3, 4, 5, 0, 1};
        auto load1 = [&]() {
          if (ML == 0) return;
          if (ld < 4) N.a[ld] = ldsr(aO[k], b * 16384 + ld * 2048);
          else {
            const int e = ld - 4, f = ML == 1 ? (e >> 1) : ORD[(e >> 1) % 6], nf = ML == 1 ? 2 : ML == 2 ? 4 : 6;
            if ((e >> 1) < nf) { if (e & 1) N.bh[f] = bread(f, b, k, 1); else N.bl[f] = bread(f, b, k, 0); }
          }
          ++ld;
        };
#pragma unroll
        for (int m = 0; m < 24; ++m) {
          const int j = ORD[m >> 2], i = m & 3;
          if (!((j < 2 ? 1 : 2) & MC)) continue;
          if (j < 2) acc[i][j] = mfma16(cat(C.bl[j], C.bh[j]), C.a[i], acc[i][j]);
          else acc[i][j] = mfma16(C.a[i], cat(C.bl[j], C.bh[j]), acc[i][j]);
          load1();
          SB0();
        }
      };
      BAR();
      STAMPT(0, 1, 0);
#pragma unroll
      for (int m = 0; m < 4; ++m) F[0].a[m] = ldsr(aO[0], m * 2048);
#pragma unroll
      for (int f = 0; f < 6; ++f) { F[0].bl[f] = bread(f, 0, 0, 0); F[0].bh[f] = bread(f, 0, 0, 1); }
      for (int t4 = 0; t4 < 12; t4 += 4) {
#pragma unroll
        for (int b = 0; b < 4; ++b) {
          step(F[0], F[1], b, 1, 3, 3);
          if (b == 0 && t4 == 0) { asm volatile("s_waitcnt vmcnt(0)" : "+v"(cpf) :: "memory"); BAR(); }
          step(F[1], F[0], (b + 1) & 3, 0, 3, 3);
          BAR();
        }
      }
#pragma unroll
      for (int b = 0; b < 4; ++b) {
        step(F[0], F[1], b, 1, 2, 2);
        step(F[1], F[0], (b + 1) & 3, 0, 2, b < 3 ? 2 : 0);
        BAR();
      }
      STAMPT(0, 2, 0);
      {
        const float* sa = (const float*)(smem + L_SA) + 64 * wr + 4 * kq;
        const float* cs = (const float*)(smem + L_CS) + wc * 96 + r;
        const float c4[4] = {cs[32], cs[48], cs[64], cs[80]};
        unsigned kp[4][2][2], vp[4][2][2];
#pragma unroll
        for (int i = 0; i < 4; ++i) {
          const f32x4 a4 = *(const f32x4*)(sa + 16 * i), b4 = *(const f32x4*)(sa + 128 + 16 * i);
          const f32x2 aa[2] = {{a4[0], a4[1]}, {a4[2], a4[3]}}, bb[2] = {{b4[0], b4[1]}, {b4[2], b4[3]}};
#pragma unroll
          for (int j = 0; j < 2; ++j)
#pragma unroll
            for (int e = 0; e < 2; ++e) {
              const f32x2 tk = (f32x2){acc[i][2 + j][2 * e], acc[i][2 + j][2 * e + 1]} * aa[e] + bb[e] * c4[j];
              const f32x2 tv = (f32x2){acc[i][4 + j][2 * e], acc[i][4 + j][2 * e + 1]} * aa[e] + bb[e] * c4[2 + j];
              kp[i][j][e] = phi_pair(tk);
              vp[i][j][e] = pack2(tv[0], tv[1]);
            }
        }
        f32x4 kv[2][2], zv[2];
#pragma unroll
        for (int d = 0; d < 2; ++d) {
          zv[d] = (f32x4){0.f, 0.f, 0.f, 0.f}; kv[d][0] = zv[d]; kv[d][1] = zv[d];
#pragma unroll
          for (int u = 0; u < 2; ++u) {
            const u32x4 ka = {kp[2 * u][d][0], kp[2 * u][d][1], kp[2 * u + 1][d][0], kp[2 * u + 1][d][1]};
#pragma unroll
            for (int e = 0; e < 2; ++e)
              kv[d][e] = mfma16(ka, (u32x4){vp[2 * u][e][0], vp[2 * u][e][1], vp[2 * u + 1][e][0], vp[2 * u + 1][e][1]}, kv[d][e]);
            zv[d] = mfma16(ka, ONES4, zv[d]);
          }
        }
        char* xo = smem + L_XCH + wave * 2048 + lane * 16;
        *(f32x4*)xo = wr ? kv[0][0] : kv[0][1];
        *(f32x4*)(xo + 1024) = wr ? kv[1][0] : kv[1][1];
        if (r < 2) *(f32x4*)(smem + L_XCH + 8192 + wave * 128 + r * 64 + kq * 16) = r ? zv[1] : zv[0];
#pragma unroll
        for (int m = 0; m < 4; ++m) F[0].a[m] = ldsr(aO[0], m * 2048);
#pragma unroll
        for (int f = 0; f < 2; ++f) { F[0].bl[f] = bread(f, 0, 0, 0); F[0].bh[f] = bread(f, 0, 0, 1); }
        STAMPT(1, 0, 0);
        asm volatile("s_waitcnt lgkmcnt(0)" ::: "memory");
        misc[16 + wave] = 1;
#pragma nounroll
        for (unsigned s_ = 0; s_ < SPIN_CAP; ++s_) if (__builtin_amdgcn_readfirstlane(misc[16 + (wave ^ 2)])) break;
        asm volatile("" ::: "memory");
        const char* xi = smem + L_XCH + (wave ^ 2) * 2048 + lane * 16;
        const f32x4 s0 = *(const f32x4*)xi + (wr ? kv[0][1] : kv[0][0]), s1 = *(const f32x4*)(xi + 1024) + (wr ? kv[1][1] : kv[1][0]);
        const unsigned ko = WS_KVP + (hp * 16 + rb) * KVB + wc * 2176;
        STW(seal((u32x4){pack2(s0[0], s0[1]), pack2(s0[2], s0[3]), pack2(s1[0], s1[1]), pack2(s1[2], s1[3])}), ko + wr * 1024 + lane * 16);
        if (wr == 0 && lane < 8) {
          const f32x4 z = *(const f32x4*)(smem + L_XCH + 8192 + wave * 128 + lane * 16) + *(const f32x4*)(smem + L_XCH + 8192 + (wave + 2) * 128 + lane * 16);
          STW(seal(__builtin_bit_cast(u32x4, z)), ko + 2048 + lane * 16);
        }
        STAMPT(0, 3, 0);
      }
#pragma unroll
      for (int b = 0; b < 4; ++b) {
        step(F[0], F[1], b, 1, 1, 1);
        step(F[1], F[0], (b + 1) & 3, 0, 1, b < 3 ? 1 : 0);
      }
      misc[24 + wave] = 1;
    }
    u32x4 qp[4];
    {
      const float* sa = (const float*)(smem + L_SA) + 64 * wr + r;
      const float* cs = (const float*)(smem + L_CS) + wc * 96 + 4 * kq;
      const f32x4 c0 = *(const f32x4*)cs, c1 = *(const f32x4*)(cs + 16);
#pragma unroll
      for (int i = 0; i < 4; ++i) {
        const float a = sa[16 * i], b = sa[128 + 16 * i];
        qp[i][0] = phi_pair((f32x2){acc[i][0][0], acc[i][0][1]} * a + (f32x2){c0[0], c0[1]} * b);
        qp[i][1] = phi_pair((f32x2){acc[i][0][2], acc[i][0][3]} * a + (f32x2){c0[2], c0[3]} * b);
        qp[i][2] = phi_pair((f32x2){acc[i][1][0], acc[i][1][1]} * a + (f32x2){c1[0], c1[1]} * b);
        qp[i][3] = phi_pair((f32x2){acc[i][1][2], acc[i][1][3]} * a + (f32x2){c1[2], c1[3]} * b);
        asm volatile("" : "+v"(qp[i]));
      }
    }
    STAMPT(0, 4, 0);
#pragma nounroll
    for (unsigned s_ = 0; s_ < SPIN_CAP; ++s_) if (__all(misc[24 + (lane & 7)] != 0)) break;
#pragma unroll
    for (int m = 0; m < 4; ++m) {
      const int rw = wave * 32 + m * 8 + (lane >> 3);
      glds16((unsigned)(rw * D_ + ((lane & 7) ^ ((rw >> 1) & 7)) * 8) * 2u, x + (size_t)rp * 128 * D_ + cb * 64, lds0 + L_X2 + (wave * 4 + m) * 1024);
    }
#pragma unroll
    for (int j = NE; j < NC; ++j) stageW(j);
    STAMPT(1, 5, 0);
    LBAR();
    STAMPT(0, 5, 0);
    {
      const u32x4 k0 = *(const u32x4*)(smem + L_KVL + wc * 2048 + lane * 16), k1 = *(const u32x4*)(smem + L_KVL + wc * 2048 + 1024 + lane * 16);
      const float* zl = (const float*)(smem + L_ZL) + wc * 64 + kq * 4;
      const f32x4 za = *(const f32x4*)zl + *(const f32x4*)(zl + 32), zb = *(const f32x4*)(zl + 16) + *(const f32x4*)(zl + 48);
      const u32x4 zf = {pack2(za[0], za[1]), pack2(za[2], za[3]), pack2(zb[0], zb[1]), pack2(zb[2], zb[3])};
      const f32x4 Z4 = {0.f, 0.f, 0.f, 0.f};
      const unsigned oo = WS_O + ((rb * 8 + wr * 4) * 32 + 2 * hp + wc) * 1024 + lane * 16;
#pragma unroll
      for (int i = 0; i < 4; ++i) {
        const f32x4 dn = mfma16(zf, qp[i], Z4), x0 = mfma16(k0, qp[i], Z4), x1 = mfma16(k1, qp[i], Z4);
        const float ri = __builtin_amdgcn_rcpf(dn[0] + 1e-6f);
        STW(seal((u32x4){pack2(x0[0] * ri, x0[1] * ri), pack2(x0[2] * ri, x0[3] * ri), pack2(x1[0] * ri, x1[1] * ri), pack2(x1[2] * ri, x1[3] * ri)}),
            oo + i * 32768);
      }
      if (lane == 0) st32(ws, SY_FB + rb * 256 + wr * 128 + (2 * hp + wc) * 4, 1u);
    }
    STAMPT(0, 6, 0);
    {
      const int h = lane >> 5, s32 = lane & 31;
      u32x2 wv[8];
#pragma unroll
      for (int c = 0; c < 8; ++c) wv[c] = *(const u32x2*)(w_r + cb * 64 + c * 8 + h * 4);
      u32x4 of[32][2];
      const unsigned ol = WS_O + (rp * 8 + wave * 2 + (kq & 1)) * 32768 + (h * 16 + r) * 16;
      auto oload1 = [&](int j, int e) { of[j][e] = LDWS(ol + e * 512 + (j & 3) * 1024, ((j & ~3) ^ hrot) * 1024); };
      auto oload = [&](int j) { oload1(j, 0); oload1(j, 1); };
      auto omark = [&](int j) {
        unsigned mx = 0;
#pragma unroll
        for (int e = 0; e < 4; ++e) mx = max(max(mx, max(of[j + e][0][1], of[j + e][0][3])), max(of[j + e][1][1], of[j + e][1][3]));
        return mx;
      };
      f32x16 a2[4];
#pragma unroll
      for (int n = 0; n < 4; ++n)
#pragma unroll
        for (int k = 0; k < 16; ++k) a2[n][k] = 0.f;
      unsigned wO[3];
#pragma unroll
      for (int e = 0; e < 3; ++e) wO[e] = opq(lds0 + e * 65536 + h * 512 + (r >> 2) * 64 + (((kq & 1) ^ h) * 32) + (lane & 3) * 8);
      constexpr int PF = GPF, OW = GOW;
      u32x2 wl[8], wh[8];
      auto wld = [&](int g) {
        const int j = g >> 2;
        const int o_ = (wslot(j) & 65535) + ((g >> 1) & 1) * 1024 + (g & 1) * 256;
        wl[g & 7] = trread(wO[wslot(j) >> 16], o_); wh[g & 7] = trread(wO[wslot(j) >> 16], o_ + 2048);
      };
      WAITV(0);
      BAR();
      STAMPT(0, 7, 0);
#pragma unroll
      for (int j = 0; j < OW; ++j) oload(j);
#pragma unroll
      for (int g = 0; g < PF; ++g) wld(g);
      SB0();
      unsigned mx = omark(0);
#pragma unroll
      for (int j = 0; j < 32; ++j) {
        if ((j & 15) == 0) STAMPT(3, j >> 2, 64);
        if ((j & 3) == 0 && __builtin_expect(__any(mx == EMPTY), 0)) {
#pragma nounroll
          for (unsigned s_ = 0; s_ < SPIN_CAP; ++s_) {
            asm volatile("" ::: "memory");
            oload(j); oload(j + 1); oload(j + 2); oload(j + 3);
            if (!__any(omark(j) == EMPTY)) break;
          }
        }
#pragma unroll
        for (int n = 0; n < 4; ++n) {
          const int g = j * 4 + n;
          a2[n] = mfma32(cat(wl[g & 7], wh[g & 7]), of[j][n >> 1], a2[n]);
          SB0();
          if (g + PF < 128) wld(g + PF);
          if (!(n & 1) && j + OW < 32) oload1(j + OW, n >> 1);
          if (n == 0 && j + OW == 32) misc[20 + wave] = 1;
          if ((n & 1) && j < 28) {
            if ((j & 3) == 0 && n == 1) mx = 0;
            mx = max(max(mx, of[j + 4][n >> 1][1]), of[j + 4][n >> 1][3]);
          }
          SB0();
          asm volatile("" :: "v"(wl[g & 7]), "v"(wh[g & 7]), "v"(of[j][n >> 1]));
          SB0();
        }
      }
      STAMPT(0, 8, 0);
      a2[0] += a2[2]; a2[1] += a2[3];
      const int row = wave * 32 + s32;
      const unsigned xb = opq(lds0 + L_X2 + row * 128 + h * 8);
      const int sw = (row >> 1) & 7;
      unsigned hb[16];
      float s = 0.f, pw = 0.f, sw_ = 0.f;
#pragma unroll
      for (int c = 0; c < 8; ++c) {
        const u32x2 xx = *(const LAS u32x2*)((const LAS char*)(size_t)(xb + ((c ^ sw) * 16)));
#pragma unroll
        for (int i = 0; i < 2; ++i) {
          const unsigned ab = pack2(a2[c >> 2][(c & 3) * 4 + 2 * i], a2[c >> 2][(c & 3) * 4 + 2 * i + 1]);
          const unsigned t = pack2(bflo(xx[i]) + bflo(ab), bfhi(xx[i]) + bfhi(ab));
          hb[2 * c + i] = t;
          s = dot2(t, 0x3F803F80u, s); pw = dot2(t, wv[c][i], pw); sw_ = dot2(wv[c][i], 0x3F803F80u, sw_);
        }
      }
      const float cm = xh_sum(s) * (1.0f / 64.0f);
      f32x2 m2v = {0.f, 0.f};
#pragma unroll
      for (int i = 0; i < 16; ++i) { const f32x2 d = (f32x2){bflo(hb[i]), bfhi(hb[i])} - cm; m2v += d * d; }
      const float m2 = xh_sum(m2v[0] + m2v[1]); pw = xh_sum(pw); sw_ = xh_sum(sw_);
      if (h == 0) STW(((u32x4){__float_as_uint(cm), __float_as_uint(m2) | 1u, __float_as_uint(pw), __float_as_uint(sw_) | 1u}),
                      WS_ST2 + ((rp * 16 + cb) * 128 + row) * 16);
      STAMPT(0, 9, 0);
      u32x4 pu_[8];
#pragma nounroll
      for (unsigned s_ = 0; s_ < SPIN_CAP; ++s_) {
        unsigned ok = 1u;
#pragma unroll
        for (int j = 0; j < 8; ++j) { pu_[j] = LDW(WS_ST2 + ((rp * 16 + h * 8 + j) * 128 + row) * 16); ok &= pu_[j][1] & pu_[j][3]; }
        if (__all(ok & 1u)) break;
        asm volatile("" ::: "memory");
      }
      STAMPT(0, 10, 0);
      f32x4 tt = {0.f, 0.f, 0.f, 0.f};
#pragma unroll
      for (int j = 0; j < 8; ++j) tt += __builtin_bit_cast(f32x4, pu_[j]);
      const float mean = xh_sum(tt[0]) * 0.0625f;
      float dd = 0.f;
#pragma unroll
      for (int j = 0; j < 8; ++j) { const float d = __uint_as_float(pu_[j][0]) - mean; dd = __builtin_fmaf(d, d, dd); }
      const float var = xh_sum(tt[1] + 64.0f * dd) * (1.0f / 1024.0f);
      const float mb = rbf(mean), rs = 1.0f / rbf(__builtin_amdgcn_sqrtf(rbf(rbf(var) + 1e-5f)));
      float sc = rbf((xh_sum(tt[2]) - mb * xh_sum(tt[3])) * rs);
      if (!(sc == sc)) sc = -INFINITY;
      unsigned b0 = h ? 0u : mk_key(sc, rp * 128 + row), b1 = 0;
      top2_wave(b0, b1);
      const int pan = rp * 4 + wave;
      if (lane == 0) st64(ws, SY_FD + cb * 512 + pan * 8, ((u64)b1 << 32) | b0);
      STAMPT(1, 11, 0);
      unsigned nq[16];
#pragma unroll
      for (int i = 0; i < 16; ++i) {
        const unsigned dq = pack2(bflo(hb[i]) - mb, bfhi(hb[i]) - mb);
        nq[i] = pack2(bflo(dq) * rs, bfhi(dq) * rs);
      }
      {
        const int c0 = key_idx(b0) & 31, c1 = key_idx(b1) & 31;
        char* tb = smem + L_TB + wave * 512;
        if (s32 == c0 || s32 == c1) {
          char* tp = tb + (s32 == c0 ? 0 : 256) + h * 8;
#pragma unroll
          for (int c = 0; c < 8; ++c) { *(u32x2*)(tp + c * 16) = (u32x2){nq[2 * c], nq[2 * c + 1]}; *(u32x2*)(tp + 128 + c * 16) = (u32x2){hb[2 * c], hb[2 * c + 1]}; }
        }
        __builtin_amdgcn_wave_barrier();
        const u32x4 v4 = seal(*(const u32x4*)(tb + s32 * 16));
#pragma unroll
        for (int rr_ = 0; rr_ < XSR; rr_ += 2)
          if (rr_ + h < XSR) STW(v4, WS_XS + (rr_ + h) * 524288 + (pan * 2 + (s32 >> 4)) * 4096 + ((s32 >> 3) & 1) * 2048 + cb * 128 + (s32 & 7) * 16);
      }
      u64 cc = 0;
      if (wave == 0) cc = ld64(ws, SY_FD + cb * 512 + lane * 8);
#pragma unroll
      for (int c = 0; c < 8; ++c) {
        u32x2 ob;
#pragma unroll
        for (int i = 0; i < 2; ++i) ob[i] = pack2(bflo(hb[2 * c + i]) + bflo(nq[2 * c + i]), bfhi(hb[2 * c + i]) + bfhi(nq[2 * c + i]));
        *(LAS u32x2*)((LAS char*)(size_t)(xb + ((c ^ sw) * 16))) = ob;
      }
      if (wave == 0) {
#pragma nounroll
        for (unsigned s_ = 0; s_ < SPIN_CAP; ++s_) {
          if (__all(cc != 0ull)) break;
          cc = ld64(ws, SY_FD + cb * 512 + lane * 8);
        }
        STAMPT(1, 13, 0);
        unsigned c0 = (unsigned)cc, c1 = (unsigned)(cc >> 32);
        top2_wave(c0, c1);
        const unsigned w0 = key_idx(c0), w1 = key_idx(c1);
        const unsigned k0 = __builtin_amdgcn_readlane((unsigned)cc, w0 >> 5), k1 = __builtin_amdgcn_readlane((unsigned)cc, w1 >> 5);
        const unsigned sl0 = (w0 >> 5) * 2 + (k0 == c0 ? 0 : 1), sl1 = (w1 >> 5) * 2 + (k1 == c1 ? 0 : 1);
        if (lane == 0) { misc[3] = w0; misc[4] = w1; misc[5] = sl0; misc[6] = sl1; misc[7] = c0; misc[8] = c1; }
        if (has_mlp) {
          const unsigned xo = WS_XS + (idx & (XSR - 1)) * 524288u + (h ? sl1 : sl0) * 4096u + (fs & 3) * 512 + s32 * 16;
          u32x4 q;
#pragma nounroll
          for (unsigned s_ = 0; s_ < SPIN_CAP; ++s_) {
            q = LDW(xo);
            if (__all(max(q[1], q[3]) != EMPTY)) break;
            asm volatile("" ::: "memory");
            __builtin_amdgcn_s_sleep(XSL1);
          }
          float* xs = (float*)(smem + L_XS) + s32 * 16 + h;
#pragma unroll
          for (int i = 0; i < 4; ++i) { xs[4 * i] = bflo(q[i]); xs[4 * i + 2] = bfhi(q[i]); }
        }
      }
      LBAR();
      STAMPT(0, 11, 0);
      const int sel0 = misc[3], sel1 = misc[4];
#pragma unroll
      for (int m = 0; m < 4; ++m) {
        const int rw = wave * 32 + m * 8 + (lane >> 3), grow = rp * 128 + rw;
        const u32x4 v = *(const u32x4*)(smem + L_X2 + rw * 128 + (((lane & 7) ^ ((rw >> 1) & 7)) * 16));
        if (grow != sel0 && grow != sel1) *(u32x4*)(out + (size_t)grow * D_ + cb * 64 + (lane & 7) * 8) = v;
      }
      STAMPT(0, 12, 0);
    }
    if (has_mlp) {
      BAR();
      if (wave == 1) {
        const float* red = (const float*)(smem + L_RED);
        f32x4 s4 = *(const f32x4*)(red + lane * 4);
#pragma unroll
        for (int w2 = 1; w2 < 4; ++w2) s4 += *(const f32x4*)(red + w2 * 256 + lane * 4);
        u32x4 t4 = __builtin_bit_cast(u32x4, s4); t4[1] |= 1u; t4[3] |= 1u;
        STW(t4, WS_GU + fs * 1024 + lane * 16);
        u32x4 q[4];
#pragma nounroll
        for (unsigned s_ = 0; s_ < SPIN_CAP; ++s_) {
          unsigned ok = 1u;
#pragma unroll
          for (int j = 0; j < 4; ++j) { q[j] = LDW(WS_GU + ((fs & ~3) + j) * 1024 + lane * 16); ok &= q[j][1] & q[j][3]; }
          if (__all(ok & 1u)) break;
          asm volatile("" ::: "memory");
        }
        const f32x4 tot = (__builtin_bit_cast(f32x4, q[0]) + __builtin_bit_cast(f32x4, q[1])) + (__builtin_bit_cast(f32x4, q[2]) + __builtin_bit_cast(f32x4, q[3]));
        f32x4 a4;
#pragma unroll
        for (int i = 0; i < 4; ++i) {
          auto sw = __builtin_amdgcn_permlane32_swap(__float_as_uint(tot[i]), __float_as_uint(tot[i]), false, false);
          const float gs = rbf(__uint_as_float(sw[0])), us = rbf(__uint_as_float(sw[1]));
          const float sg = rbf(gs / (1.0f + __expf(-gs)));
          a4[i] = rbf(sg * us);
        }
        if (lane < 32 && ((lane & 15) >> 2) == (fs & 3)) *(f32x4*)(smem + L_ACT + ((lane >> 4) * 16 + (lane & 3) * 4) * 4) = a4;
      }
      LBAR();
    }
  } else {
    __builtin_amdgcn_s_setprio(2);
    const int hw = wave - 4, mt = tid & 255;
    u32x4 wgr[8], wur[8], wdr[8];
    const int krow = hw * 64 + (lane >> 3);
    const unsigned gof = (unsigned)(((fs & 3) * 256 + krow) * F_ + (fs >> 2) * 64 + (lane & 7) * 8) * 2u;
    const unsigned dof = (unsigned)((fs * 16 + (lane >> 5) * 8) * D_ + (hw * 32 + (lane & 31)) * 8) * 2u;
#define WLOAD(dst, vo, base) asm volatile("global_load_dwordx4 %0, %1, %2" WPOL : "=v"(dst) : "v"(vo), "s"(uni(base)) : "memory")
    auto wload = [&](int i) {
      if (i < 8) WLOAD(wgr[i], gof + i * (8 * F_ * 2), wg);
      else if (i < 16) WLOAD(wur[i - 8], gof + (i - 8) * (8 * F_ * 2), wu);
      else WLOAD(wdr[i - 16], dof + (i - 16) * (D_ * 2), wd);
    };
    auto wpin = [&](int i) {
      if (i < 8) asm volatile("" : "+v"(wgr[i])); else if (i < 16) asm volatile("" : "+v"(wur[i - 8])); else asm volatile("" : "+v"(wdr[i - 16]));
    };
    const int pbi = lane >> 4, pq = (lane >> 2) & 3, pch = lane & 3;
    if (hw < 2) {
      const char* baseA = uni(x + (size_t)rb * 128 * D_);
      const char* baseB = uni(wqkv + hp * 64);
      unsigned voA[2], voB[2];
      {
        const int r0 = 64 * hw + (lane >> 3), r1 = r0 + 8;
        voA[0] = (unsigned)(r0 * D_ + ((lane & 7) ^ ((r0 >> 1) & 7)) * 8) * 2u;
        voA[1] = (unsigned)(r1 * D_ + ((lane & 7) ^ ((r1 >> 1) & 7)) * 8) * 2u;
        voB[0] = (unsigned)((32 * hw + 4 * (pbi >> 1) + pq) * (3 * D_) + (pbi & 1) * 32 + pch * 8) * 2u;
        voB[1] = (unsigned)((32 * hw + 8 + 4 * (pbi >> 1) + pq) * (3 * D_) + (pbi & 1) * 32 + (pch ^ 2) * 8) * 2u;
      }
      auto stage = [&](int t, int part = 3) {
        const int kt = korder(t), b = t & 3;
        const char* pa = baseA + kt * 128;
        const char* pb = baseB + (size_t)kt * (64 * 3 * D_ * 2);
#pragma unroll
        for (int j = 0; j < 8; ++j) if (part & 1) glds16(voA[j & 1] + (j >> 1) * (16 * D_ * 2), pa, lds0 + L_A + b * 16384 + (8 * hw + j) * 1024);
#pragma unroll
        for (int j = 0; j < 4; ++j) {
          if (!(part & (j < 2 ? 1 : 2))) continue;
          const unsigned vo = voB[j & 1] + (j >> 1) * (16 * 3 * D_ * 2);
          glds16(vo + 2048, pb, lds0 + L_KV + b * 16384 + (4 * hw + j) * 1024);
          glds16(vo + 4096, pb, lds0 + L_KV + b * 16384 + 8192 + (4 * hw + j) * 1024);
          glds16(vo, pb, lds0 + L_Q + b * 8192 + (4 * hw + j) * 1024);
        }
      };
      stage(0); stage(1);
      WAITV(20); BAR();
      stage(2);
      WAITV(20); BAR();
      stage(3);
      for (int t = 0; t < 12; ++t) { WAITV(20); BAR(); stage(t + 4); }
      WAITV(20); BAR();
      WAITV(0); BAR();
      BAR();
    } else {
      const int aw = hw - 2, at = tid & 127;
      const bool master = bid == 0 && aw == 0;
      unsigned hs_cur = 0, hs_g0 = 0;
      u32x4 ms_exp = {0, 0, 0, 0}, pm = {0, 0, 0, 0};
      const unsigned p_off = master ? SY_RE + lane * 16 : (lane == 1 ? SY_GO : SY_CH) + bid * 4;
      const char* wsu = uni(ws);
#define HS_PROBE() asm volatile("global_load_dwordx4 %0, %1, %2 sc1" : "+v"(pm) : "v"(p_off), "s"(wsu) : "memory")
#define HS_WAIT() asm volatile("s_waitcnt vmcnt(0)" : "+v"(pm) :: "memory")
      if (master) {
        const u32x4 re = LDW(SY_RE + lane * 16), g0 = LDW(SY_GO + lane * 16);
#pragma unroll
        for (int j = 0; j < 4; ++j) { const unsigned c = re[j] + 1u; ms_exp[j] = (c == g0[j]) ? c + 1u : c; }
        STW(ms_exp, SY_CH + lane * 16);
      } else if (aw == 1) hs_g0 = __builtin_amdgcn_readfirstlane(ld32(ws, SY_GO + bid * 4));
      bool done = false;
      auto hs_check = [&]() {
        HS_WAIT();
        if (!master) {
          const unsigned c = __builtin_amdgcn_readlane(pm[0], 0), go = __builtin_amdgcn_readlane(pm[0], 1);
          if (go != hs_g0) done = true;
          else if (c != hs_cur) { hs_cur = c; st32(ws, SY_RE + bid * 4, c); }
        } else if (!done && __all(pm[0] == ms_exp[0] && pm[1] == ms_exp[1] && pm[2] == ms_exp[2] && pm[3] == ms_exp[3])) {
          STW(ms_exp, SY_GO + lane * 16);
          done = true;
        }
      };
      const u32x4 e4 = {EMPTY, EMPTY, EMPTY, EMPTY}, z4 = {0u, 0u, 0u, 0u};
      auto oclear = [&](int i) { STW(e4, WS_O + ((rb * 8 + i) * 32 + 2 * hp + aw) * 1024 + lane * 16); };
      const bool prober = master || aw == 1;
      const int kq = lane >> 4, r = lane & 15;
      BAR();
      u64 rst[4];
      {
        u32x4 a[4][2];
#pragma unroll
        for (int i = 0; i < 4; ++i)
#pragma unroll
          for (int k = 0; k < 2; ++k) a[i][k] = *(const u32x4*)(smem + L_A + (64 * aw + 16 * i + r) * 128 + (((4 * k + kq) ^ ((r >> 1) & 7)) * 16));
        const f32x4 Z4 = {0.f, 0.f, 0.f, 0.f};
        const int l3 = lane & 3;
#pragma unroll
        for (int i = 0; i < 4; ++i) {
          const f32x4 s1 = mfma16(ONES4, a[i][1], mfma16(ONES4, a[i][0], Z4)), s2 = mfma16(a[i][1], a[i][1], mfma16(a[i][0], a[i][0], Z4));
          const float q = l3 == 0 ? s2[0] : l3 == 1 ? s2[1] : l3 == 2 ? s2[2] : s2[3];
          rst[i] = ((u64)min(__float_as_uint(q), 0xFFFFFFFEu) << 32) | min(__float_as_uint(s1[0]), 0xFFFFFFFEu);
          asm volatile("" : "+v"(rst[i]));
        }
      }
      BAR();
      if ((r >> 2) == kq) {
#pragma unroll
        for (int i = 0; i < 4; ++i) st64(ws, rs_o + (64 * aw + 16 * i + r) * 8, rst[i]);
      }
      BAR();
      STAMPT(2, 0, 384);
      oclear(0); oclear(1); oclear(2); oclear(3);
      BAR();
      STAMPT(2, 1, 384);
      oclear(4); oclear(5); oclear(6); oclear(7);
      BAR();
      STAMPT(2, 2, 384);
      unsigned cv0, cv1;
      {
        f32x4 cacc[6];
#pragma unroll
        for (int f = 0; f < 6; ++f) {
          cacc[f] = (f32x4){0.f, 0.f, 0.f, 0.f};
#pragma unroll
          for (int k = 0; k < 2; ++k) {
            const char* p = smem + (f < 2 ? L_Q + (tcs & 3) * 8192 : L_KV + (tcs & 3) * 16384 + ((f >> 1) - 1) * 8192) + k * 4096 + kq * 1024 + aw * 256 +
                            (r >> 2) * 64 + (((f & 1) ^ (kq & 1)) * 32) + (lane & 3) * 8;
            cacc[f] = mfma16(ONES4, cat(trread(p), trread(p + 512)), cacc[f]);
          }
        }
        cv0 = min(__float_as_uint(kq == 0 ? cacc[0][0] : kq == 1 ? cacc[1][0] : kq == 2 ? cacc[2][0] : cacc[3][0]), 0xFFFFFFFEu);
        cv1 = min(__float_as_uint(kq == 0 ? cacc[4][0] : cacc[5][0]), 0xFFFFFFFEu);
        asm volatile("" : "+v"(cv0), "+v"(cv1));
      }
      BAR();
      STAMPT(2, 3, 384);
      st32(ws, cs_o + aw * 384 + lane * 4, cv0);
      if (lane < 32) st32(ws, cs_o + aw * 384 + 256 + lane * 4, cv1);
      {
        const unsigned ko = WS_KVP + (hp * 16 + rb) * KVB + aw * 2176;
        STW(e4, ko + lane * 16); STW(e4, ko + 1024 + lane * 16);
        if (lane < 8) STW(e4, ko + 2048 + lane * 16);
        if (lane == 0) st64(ws, SY_FB + rb * 256 + aw * 128 + hp * 8, 0ull);
      }
      if (prober) HS_PROBE();
      BAR();
      STAMPT(2, 4, 384);
      HS_WAIT();
      BAR();
      STAMPT(2, 5, 384);
      if (aw == 1) { hs_cur = __builtin_amdgcn_readlane(pm[0], 0); st32(ws, SY_RE + bid * 4, hs_cur); }
      if (master) HS_PROBE();
      if (has_mlp) { STW(z4, WS_YP + fs * 8192 + at * 16); STW(z4, WS_YP + fs * 8192 + 2048 + at * 16); }
      BAR();
      STAMPT(2, 6, 384);
      if (master) { hs_check(); HS_PROBE(); }
      if (has_mlp) { STW(z4, WS_YP + fs * 8192 + 4096 + at * 16); STW(z4, WS_YP + fs * 8192 + 6144 + at * 16); }
      BAR();
      STAMPT(2, 7, 384);
      if (master) { hs_check(); HS_PROBE(); }
      if (has_mlp && aw == 0) STW(z4, WS_GU + fs * 1024 + lane * 16);
      STW(z4, WS_ST2 + (rp * 16 + cb) * 2048 + at * 16);
      if (at < 4) st64(ws, SY_FD + cb * 512 + (rp * 4 + at) * 8, 0ull);
      BAR();
      STAMPT(2, 8, 384);
      if (master) { hs_check(); HS_PROBE(); }
#pragma unroll
      for (int rr_ = 0; rr_ < XSR; ++rr_) STW(e4, WS_XS + rr_ * 524288 + (rp * 16 + (at >> 3)) * 2048 + cb * 128 + (at & 7) * 16);
      BAR();
      STAMPT(2, 9, 384);
      if (master) hs_check();
      if (prober) HS_PROBE();
      BAR();
      STAMPT(2, 10, 384);
      if (prober) {
        hs_check();
#pragma nounroll
        for (unsigned s_ = 0; s_ < SPIN_CAP && !done; ++s_) { HS_PROBE(); hs_check(); }
      }
      BAR();
      STAMPT(2, 11, 384);
      u32x4 sg[16];
      const unsigned sg_off = aw ? WS_CS + hp * (16 * 768) + min(lane, 47) * 16 : WS_RS + rb * 16384 + lane * 16, sg_st = aw ? 768 : 1024;
#pragma unroll
      for (int i = 0; i < 16; ++i) {
        asm volatile("global_load_dwordx4 %0, %1, %2 sc1" : "=v"(sg[i]) : "v"(sg_off + i * sg_st), "s"(wsu) : "memory");
        if (i == SGSPLIT) { BAR(); STAMPT(2, 12, 384); }
      }
      if (aw == 1 && lane >= 48) misc[lane - 32] = 0;
      BAR();
      STAMPT(2, 13, 384);
      BAR();
      STAMPT(2, 14, 384);
      WAITV(0);
      f32x4 sm = {0.f, 0.f, 0.f, 0.f};
#pragma unroll
      for (int i = 0; i < 16; ++i) { asm volatile("" : "+v"(sg[i])); sm += __builtin_bit_cast(f32x4, sg[i]); }
      const float chk = (sm[0] + sm[1]) + (sm[2] + sm[3]);
      if (__builtin_expect(__any(chk != chk), 0)) {
#pragma nounroll
        for (unsigned s_ = 0; s_ < SPIN_CAP; ++s_) {
          unsigned mx = 0;
          sm = (f32x4){0.f, 0.f, 0.f, 0.f};
#pragma unroll
          for (int i = 0; i < 16; ++i) {
            const u32x4 t = LDW(sg_off + i * sg_st);
            mx = max(max(mx, max(t[0], t[1])), max(t[2], t[3])); sm += __builtin_bit_cast(f32x4, t);
          }
          if (!__any(mx == EMPTY)) break;
        }
      }
      STAMPT(1, 1, 384);
      f32x2 ra, rb_;
#pragma unroll
      for (int e = 0; e < 2; ++e) {
        const float mean = sm[2 * e] * (1.0f / 1024.0f);
        const float var = fmaxf(sm[2 * e + 1] * (1.0f / 1024.0f) - mean * mean, 0.f);
        ra[e] = __builtin_amdgcn_rcpf(rbf(__builtin_amdgcn_sqrtf(rbf(rbf(var) + 1e-5f))));
        rb_[e] = -rbf(mean) * ra[e];
      }
      if (aw == 0) { *(f32x2*)(smem + L_SA + lane * 8) = ra; *(f32x2*)(smem + L_SB + lane * 8) = rb_; }
      else if (lane < 48) *(f32x4*)(smem + L_CS + lane * 16) = sm;
      STAMPT(1, 2, 384);
    }
    unsigned wt;
    asm volatile("global_load_dword %0, %1, %2" : "=v"(wt) : "v"((unsigned)((idx * 32 + (mt >> 3)) * 2048 + (cb >> 3) * 1024 + (mt & 7) * 128)), "s"(uni(wo)) : "memory");
    LBAR();
      STAMPT(2, 15, 384);
#pragma unroll
    for (int i = 0; i < NW1; ++i) { WAITV(THR - 1); wload(i); }
#pragma unroll
    for (int j = 0; j < NE; ++j) { WAITV(THR - 1); stageW(j); }
    WAITV(0);
#pragma unroll
    for (int i = 0; i < NW1; ++i) wpin(i);
    asm volatile("" :: "v"(wt));
    STAMPT(1, 3, 256);
    {
#pragma nounroll
      for (unsigned s_ = 0; s_ < SPIN_CAP; ++s_) { if (__builtin_amdgcn_readfirstlane(misc[16 + hw])) break; __builtin_amdgcn_s_sleep(1); }
      __builtin_amdgcn_s_sleep(SLA);
      u32x4 pv[16], zq;
      const unsigned ko = WS_KVP + hp * 16 * KVB + (hw >> 1) * 2176;
      const unsigned kl = ko + (hw & 1) * 1024 + lane * 16, zo = ko + ((hw & 1) * 8 + (lane >> 3)) * KVB + 2048 + (lane & 7) * 16;
      unsigned need = 0x1FFFFu;
#pragma nounroll
      for (unsigned s_ = 0; s_ < SPIN_CAP && need; ++s_) {
#pragma unroll
        for (int p = 0; p < 16; ++p) if (need & (1u << p)) pv[p] = LDWS(kl, (p ^ ((idx & 1) * 8)) * KVB);
        if (need & 0x10000u) zq = LDW(zo);
        asm volatile("" ::: "memory");
#pragma unroll
        for (int p = 0; p < 16; ++p) if (!__any(max(pv[p][1], pv[p][3]) == EMPTY)) need &= ~(1u << p);
        if (!__any(max(zq[1], zq[3]) == EMPTY)) need &= ~0x10000u;
      }
      STAMPT(1, 4, 256);
      misc[28 + hw] = 1;
      f32x2 s[2][4];
#pragma unroll
      for (int p = 0; p < 16; ++p)
#pragma unroll
        for (int i = 0; i < 4; ++i) { const f32x2 t = {bflo(pv[p][i]), bfhi(pv[p][i])}; if (p & 7) s[p >> 3][i] += t; else s[p >> 3][i] = t; }
      u32x4 kf;
#pragma unroll
      for (int i = 0; i < 4; ++i) { const f32x2 t = s[0][i] + s[1][i]; kf[i] = pack2(t[0], t[1]); }
      *(u32x4*)(smem + L_KVL + hw * 1024 + lane * 16) = kf;
      f32x4 z = __builtin_bit_cast(f32x4, zq);
#pragma unroll
      for (int i = 0; i < 4; ++i) { float v = z[i]; v += dppf<DPP_ROR8>(v); z[i] = xh_sum(x16_sum(v)); }
      if (lane < 8) *(f32x4*)(smem + L_ZL + hw * 128 + lane * 16) = z;
    }
    LBAR();
#pragma unroll
    for (int j = NC; j < 32; ++j) { WAITV(THR - 1); stageW(j); }
    WAITV(0);
    STAMPT(1, 6, 256);
    if (hw == 0) {
#pragma nounroll
      for (unsigned s_ = 0; s_ < SPIN_CAP; ++s_) if (__all(ld32(ws, SY_FB + rp * 256 + lane * 4) != 0u)) break;
    }
    BAR();
#pragma nounroll
    for (unsigned s_ = 0; s_ < SPIN_CAP; ++s_) { if (__builtin_amdgcn_readfirstlane(misc[20 + hw])) break; __builtin_amdgcn_s_sleep(2); }
#pragma unroll
    for (int i = NW1; i < NW4; ++i) { WAITV(THR - 1); wload(i); }
    WAITV(0);
#pragma unroll
    for (int i = NW1; i < NW4; ++i) wpin(i);
    STAMPT(1, 7, 256);
    BAR();
    if (has_mlp) {
#pragma unroll
      for (int i = NW4; i < 24; ++i) wload(i);
      {
        const f32x2* xs = (const f32x2*)(smem + L_XS);
        f32x2 ag[8], au[8];
#pragma unroll
        for (int c = 0; c < 8; ++c) { ag[c] = (f32x2){0.f, 0.f}; au[c] = (f32x2){0.f, 0.f}; }
#pragma unroll
        for (int i = 0; i < 8; ++i) {
          const f32x2 xx = xs[krow + 8 * i];
#pragma unroll
          for (int c = 0; c < 4; ++c) {
            ag[2 * c] += xx * bflo(wgr[i][c]); ag[2 * c + 1] += xx * bfhi(wgr[i][c]);
            au[2 * c] += xx * bflo(wur[i][c]); au[2 * c + 1] += xx * bfhi(wur[i][c]);
          }
        }
        float v[32];
#pragma unroll
        for (int c = 0; c < 8; ++c) { v[c] = ag[c][0]; v[8 + c] = ag[c][1]; v[16 + c] = au[c][0]; v[24 + c] = au[c][1]; }
#pragma unroll
        for (int i = 0; i < 16; ++i) {
          auto sw = __builtin_amdgcn_permlane32_swap(__float_as_uint(v[i]), __float_as_uint(v[16 + i]), false, false);
          v[i] = __uint_as_float(sw[0]) + __uint_as_float(sw[1]);
        }
#pragma unroll
        for (int i = 0; i < 8; ++i) {
          auto sw = __builtin_amdgcn_permlane16_swap(__float_as_uint(v[i]), __float_as_uint(v[8 + i]), false, false);
          v[i] = __uint_as_float(sw[0]) + __uint_as_float(sw[1]);
        }
        const bool up = (lane & 8) != 0;
#pragma unroll
        for (int i = 0; i < 4; ++i) {
          const float keep = up ? v[4 + i] : v[i], give = up ? v[i] : v[4 + i];
          v[i] = keep + dppf<DPP_ROR8>(give);
        }
        *(f32x4*)(smem + L_RED + (hw * 256 + (lane >> 5) * 128 + ((lane >> 4) & 1) * 64 + (lane & 7) * 8 + ((lane >> 3) & 1) * 4) * 4) = (f32x4){v[0], v[1], v[2], v[3]};
      }
      STAMPT(1, 14, 256);
      LBAR();
      WAITV(0);
#pragma unroll
      for (int i = NW4; i < 24; ++i) wpin(i);
      BAR();
      const float* act = (const float*)(smem + L_ACT) + (lane >> 5) * 8;
      float y0[8], y1[8];
#pragma unroll
      for (int c = 0; c < 8; ++c) { y0[c] = 0.f; y1[c] = 0.f; }
#pragma unroll
      for (int i = 0; i < 8; ++i) {
        const float a0 = act[i], a1 = act[16 + i];
#pragma unroll
        for (int c = 0; c < 4; ++c) {
          const float dl = bflo(wdr[i][c]), dh = bfhi(wdr[i][c]);
          y0[2 * c] += a0 * dl; y0[2 * c + 1] += a0 * dh; y1[2 * c] += a1 * dl; y1[2 * c + 1] += a1 * dh;
        }
      }
      u32x4 ya, yb;
#pragma unroll
      for (int c = 0; c < 8; ++c) {
        auto sw = __builtin_amdgcn_permlane32_swap(__float_as_uint(y0[c]), __float_as_uint(y1[c]), false, false);
        const unsigned v = __float_as_uint(__uint_as_float(sw[0]) + __uint_as_float(sw[1])) | (c & 1);
        if (c < 4) ya[c] = v; else yb[c - 4] = v;
      }
      const unsigned yo = WS_YP + fs * 8192 + (lane >> 5) * 4096 + (hw * 32 + (lane & 31)) * 32;
      STW(ya, yo); STW(yb, yo + 16);
      STAMPT(0, 13, 256);
    }
  }
  if (!has_mlp) {
    const int j = bid - 224, tk = j >> 4, c0 = (j & 15) * 64;
    const int pg = tid >> 4, c4 = tid & 15;
    unsigned xn = 0, xh = 0;
    if (tid < 32) {
      const unsigned xo = WS_XS + (idx & (XSR - 1)) * 524288u + (unsigned)misc[5 + tk] * 4096u + c0 * 2 + (tid >> 1) * 8;
#pragma nounroll
      for (unsigned s_ = 0; s_ < SPIN_CAP; ++s_) {
        const u64 a = ld64(ws, xo), b = ld64(ws, xo + 2048);
        xn = (tid & 1) ? (unsigned)(a >> 32) : (unsigned)a; xh = (tid & 1) ? (unsigned)(b >> 32) : (unsigned)b;
        if (__all((unsigned)(a >> 32) != EMPTY && (unsigned)(b >> 32) != EMPTY)) break;
      }
    }
    u32x4 pv[7];
    __builtin_amdgcn_s_sleep(YSL0);
#pragma nounroll
    for (unsigned s_ = 0; s_ < SPIN_CAP; ++s_) {
      unsigned ok = 1u;
#pragma unroll
      for (int p = 0; p < 7; ++p) { pv[p] = LDW(WS_YP + (pg * 7 + p) * 8192 + tk * 4096 + c0 * 4 + c4 * 16); ok &= pv[p][1] & pv[p][3]; }
      if (__all(ok & 1u)) break;
      asm volatile("" ::: "memory");
      __builtin_amdgcn_s_sleep(YSL1);
    }
    STAMPT(0, 13, 0);
    f32x4 s = {0.f, 0.f, 0.f, 0.f};
#pragma unroll
    for (int p = 0; p < 7; ++p) s += __builtin_bit_cast(f32x4, pv[p]);
    float* rd = (float*)smem;
    *(f32x4*)(rd + pg * 64 + c4 * 4) = s;
    LBAR();
    if (tid < 32) {
      f32x2 y = {0.f, 0.f};
#pragma unroll
      for (int p = 0; p < 32; ++p) y += *(const f32x2*)(rd + p * 64 + tid * 2);
      const float sg = rbf(1.0f / (1.0f + __expf(-key_val((unsigned)misc[7 + tk]))));
      const float t0 = rbf(bflo(xn) + rbf(rbf(y[0]) * sg)), t1 = rbf(bfhi(xn) + rbf(rbf(y[1]) * sg));
      *(unsigned*)(out + (size_t)misc[3 + tk] * D_ + c0 + tid * 2) = pack2(bflo(xh) + t0, bfhi(xh) + t1);
    }
    STAMPT(0, 14, 0);
  }
}

extern "C" void kernel_launch(void* const* d_in, const int* in_sizes, int n_in, void* d_out, int out_size,
                              void* d_ws, size_t ws_size, hipStream_t stream) {
  (void)hipFuncSetAttribute((const void*)fused, hipFuncAttributeMaxDynamicSharedMemorySize, LDS_TOTAL);
  fused<<<256, 512, LDS_TOTAL, stream>>>((const u16*)d_in[0], (const u16*)d_in[1], (const u16*)d_in[2], (const u16*)d_in[4],
                                         (const u16*)d_in[5], (const u16*)d_in[6], (const u16*)d_in[7], (u16*)d_out, (char*)d_ws);
}
```

```cpp
#include <hip/hip_runtime.h>
#include <stdint.h>

#define LAS __attribute__((address_space(3)))
#define GAS __attribute__((address_space(1)))
#define DI __device__ __forceinline__

#define XSR 2
#define GPF 5
#define GOW 14
#define NE 4
#define WPOL
#define HRM 3
#define OW0 4
#define SLC0 24
#define SLC1 8
#define SLD1 0
#define HALF 0
#define SGS 15
#define NW2 NW1
#define NR 24
#define THR3 4
#define THR2 6
#define THR 6
#define NW1 8
#define SLA 8
#define SLB 0
constexpr int CODE_PER = 11;
constexpr int XSL1 = 2;
#define YSL0 16
#define YSL1 4
#define SLG0 12
#define SLG1 2
typedef unsigned short u16;
typedef unsigned long long u64;
typedef __attribute__((ext_vector_type(2))) __bf16 bf16x2;
typedef __attribute__((ext_vector_type(8))) __bf16 bf16x8;
typedef __attribute__((ext_vector_type(4))) short s16x4;
typedef __attribute__((ext_vector_type(16))) float f32x16;
typedef __attribute__((ext_vector_type(4))) float f32x4;
typedef __attribute__((ext_vector_type(2))) float f32x2;
typedef __attribute__((ext_vector_type(4))) unsigned u32x4;
typedef __attribute__((ext_vector_type(2))) unsigned u32x2;
typedef GAS unsigned gu32;
typedef GAS unsigned long long gu64;

constexpr int S_ = 2048, D_ = 1024, F_ = 3584;

constexpr unsigned SY_CH = 0, SY_GO = 1024, SY_RE = 2048, SY_FB = 4096, SY_FD = 8192;
constexpr unsigned WS_RS  = 16384;
constexpr unsigned WS_CS  = WS_RS + 256 * 1024;
constexpr unsigned KVB    = 4352;
constexpr unsigned WS_KVP = WS_CS + 256 * 768;
constexpr unsigned WS_ST2 = WS_KVP + 256 * KVB;
constexpr unsigned WS_GU  = WS_ST2 + 256 * 2048;
constexpr unsigned WS_XS  = WS_GU + 256 * 1024;
constexpr unsigned WS_YP  = WS_XS + XSR * 524288;
constexpr unsigned WS_O   = WS_YP + 224 * 8192;
constexpr unsigned WS_END = WS_O + S_ * D_ * 2;
constexpr size_t WS_DIAG  = 64u << 20;
#define STAMPT(kid, i, T) do {} while (0)

DI float bflo(unsigned u) { return __uint_as_float(u << 16); }
DI float bfhi(unsigned u) { return __uint_as_float(u & 0xffff0000u); }
DI float rbf(float x) { return (float)(__bf16)x; }
DI unsigned pack2(float a, float b) { bf16x2 c; c[0] = (__bf16)a; c[1] = (__bf16)b; return __builtin_bit_cast(unsigned, c); }
DI f32x4 mfma16(u32x4 a, u32x4 b, f32x4 c) {
  return __builtin_amdgcn_mfma_f32_16x16x32_bf16(__builtin_bit_cast(bf16x8, a), __builtin_bit_cast(bf16x8, b), c, 0, 0, 0);
}
DI f32x16 mfma32(u32x4 a, u32x4 b, f32x16 c) {
  return __builtin_amdgcn_mfma_f32_32x32x16_bf16(__builtin_bit_cast(bf16x8, a), __builtin_bit_cast(bf16x8, b), c, 0, 0, 0);
}
DI u32x2 trread(const char* p) {
  s16x4 v = __builtin_amdgcn_ds_read_tr16_b64_v4i16((LAS s16x4*)p);
  return __builtin_bit_cast(u32x2, v);
}
DI unsigned opq(unsigned v) { asm volatile("" : "+v"(v)); return v; }
DI u32x2 trread(unsigned base, int off) {
  s16x4 v = __builtin_amdgcn_ds_read_tr16_b64_v4i16((LAS s16x4*)((LAS char*)(size_t)base + off));
  return __builtin_bit_cast(u32x2, v);
}
DI u32x4 ldsr(unsigned base, int off) { return *(const LAS u32x4*)((const LAS char*)(size_t)base + off); }
DI u32x4 cat(u32x2 a, u32x2 b) { return (u32x4){a[0], a[1], b[0], b[1]}; }
DI u32x4 pick(int c, u32x4 a, u32x4 b) { const unsigned m = c ? ~0u : 0u; return (a & m) | (b & ~m); }
DI f32x4 add4(f32x4 a, f32x4 b) {
  f32x4 c;
#pragma unroll
  for (int i = 0; i < 4; ++i) asm("v_add_f32 %0, %1, %2" : "=v"(c[i]) : "v"(a[i]), "v"(b[i]));
  return c;
}
DI float dot2(unsigned a, unsigned b, float c) {
  return __builtin_amdgcn_fdot2_f32_bf16(__builtin_bit_cast(bf16x2, a), __builtin_bit_cast(bf16x2, b), c, false);
}
DI const char* uni(const void* p) {
  const u64 v = (u64)p;
  return (const char*)(((u64)(unsigned)__builtin_amdgcn_readfirstlane((int)(v >> 32)) << 32) | (unsigned)__builtin_amdgcn_readfirstlane((int)v));
}
DI void glds16(unsigned voff, const void* sbase, unsigned lds) {
  asm volatile("s_mov_b32 m0, %2\n\ts_nop 0\n\tglobal_load_lds_dwordx4 %0, %1" ::"v"(voff), "s"(uni(sbase)), "s"(__builtin_amdgcn_readfirstlane(lds)) : "memory");
}
DI float xh_sum(float v) {
  auto r = __builtin_amdgcn_permlane32_swap(__float_as_uint(v), __float_as_uint(v), false, false);
  return __uint_as_float(r[0]) + __uint_as_float(r[1]);
}
template <int CTRL> DI unsigned dppu(unsigned v) { return (unsigned)__builtin_amdgcn_update_dpp(0, (int)v, CTRL, 0xf, 0xf, true); }
template <int CTRL> DI float dppf(float v) { return __uint_as_float(dppu<CTRL>(__float_as_uint(v))); }
constexpr int DPP_X1 = 0xB1, DPP_X2 = 0x4E, DPP_HMIR = 0x141, DPP_MIR = 0x140, DPP_ROR8 = 0x128, DPP_ROR4 = 0x124;
DI float oct_sum(float v) { v += dppf<DPP_X1>(v); v += dppf<DPP_X2>(v); v += dppf<DPP_HMIR>(v); return v; }
DI float x16_sum(float v) {
  auto r = __builtin_amdgcn_permlane16_swap(__float_as_uint(v), __float_as_uint(v), false, false);
  return __uint_as_float(r[0]) + __uint_as_float(r[1]);
}
DI unsigned phi_pair(f32x2 t) {
  const f32x2 u = t + 1.000001f;
  const f32x2 l = t * 1.44269504088896340736f;
  const f32x2 e = (f32x2){__builtin_amdgcn_exp2f(l[0]), __builtin_amdgcn_exp2f(l[1])} + 1e-6f;
  return pack2(__builtin_amdgcn_fmed3f(e[0], u[0], 1.000001f), __builtin_amdgcn_fmed3f(e[1], u[1], 1.000001f));
}
#define RLX_AGENT __ATOMIC_RELAXED, __HIP_MEMORY_SCOPE_AGENT
DI void st32(char* ws, unsigned off, unsigned v) { __hip_atomic_store((gu32*)(ws + off), v, RLX_AGENT); }
DI void st64(char* ws, unsigned off, u64 v) { __hip_atomic_store((gu64*)(ws + off), v, RLX_AGENT); }
DI unsigned ld32(char* ws, unsigned off) { return __hip_atomic_load((gu32*)(ws + off), RLX_AGENT); }
DI u64 ld64(char* ws, unsigned off) { return __hip_atomic_load((gu64*)(ws + off), RLX_AGENT); }
#define WAITV(n) asm volatile("s_waitcnt vmcnt(%0)" :: "n"(n) : "memory")
#define BAR() do { asm volatile("" ::: "memory"); __builtin_amdgcn_s_barrier(); asm volatile("" ::: "memory"); } while (0)
#define LBAR() do { asm volatile("s_waitcnt lgkmcnt(0)" ::: "memory"); __builtin_amdgcn_s_barrier(); asm volatile("" ::: "memory"); } while (0)
#define SB0() __builtin_amdgcn_sched_barrier(0)
constexpr unsigned SPIN_CAP = 1u << 22;
constexpr unsigned EMPTY = 0xFFFFFFFFu;
DI u32x4 seal(u32x4 v) { v[1] = min(v[1], 0xFFFFFFFEu); v[3] = min(v[3], 0xFFFFFFFEu); return v; }
DI unsigned mk_key(float v, unsigned idx) {
  const unsigned u = __float_as_uint(v) >> 16;
  const unsigned o = (u & 0x8000u) ? (~u & 0xffffu) : (u | 0x8000u);
  return (o << 16) | (0xFFFFu - idx);
}
DI unsigned key_idx(unsigned k) { return (0xFFFFu - (k & 0xFFFFu)) & (S_ - 1); }
DI float key_val(unsigned k) { const unsigned o = k >> 16; return __uint_as_float(((o & 0x8000u) ? (o & 0x7fffu) : (~o & 0xffffu)) << 16); }
DI void top2_mix(unsigned& b0, unsigned& b1, unsigned c0, unsigned c1) {
  const unsigned lo = min(b0, c0), mx = max(b1, c1);
  b0 = max(b0, c0); b1 = max(lo, mx);
}
DI void top2_wave(unsigned& b0, unsigned& b1) {
  top2_mix(b0, b1, dppu<DPP_X1>(b0), dppu<DPP_X1>(b1));
  top2_mix(b0, b1, dppu<DPP_X2>(b0), dppu<DPP_X2>(b1));
  top2_mix(b0, b1, dppu<DPP_HMIR>(b0), dppu<DPP_HMIR>(b1));
  top2_mix(b0, b1, dppu<DPP_MIR>(b0), dppu<DPP_MIR>(b1));
  { auto p = __builtin_amdgcn_permlane16_swap(b0, b0, false, false); auto q = __builtin_amdgcn_permlane16_swap(b1, b1, false, false);
    b0 = p[0]; b1 = q[0]; top2_mix(b0, b1, p[1], q[1]); }
  { auto p = __builtin_amdgcn_permlane32_swap(b0, b0, false, false); auto q = __builtin_amdgcn_permlane32_swap(b1, b1, false, false);
    b0 = p[0]; b1 = q[0]; top2_mix(b0, b1, p[1], q[1]); }
}

constexpr int L_A = 0, L_Q = 65536, L_KV = 98304, LDS_TOTAL = 163840;
constexpr int L_M  = L_KV;
constexpr int L_SA = L_M, L_SB = L_M + 512, L_CS = L_M + 1024;
constexpr int L_MISC = L_M + 1792;
constexpr int L_XCH = L_M + 2048;
constexpr int L_KVL = L_M + 11264;
constexpr int L_ZL = L_M + 15360;
constexpr int L_XS = L_M + 3072;
constexpr int L_RED = L_M + 5120;
constexpr int L_ACT = L_M + 9216;
constexpr int L_TB = L_M + 9472;
constexpr int L_WR = L_M + 15872;
constexpr int L_DW = 114688;
constexpr int L_X2 = 81920;
DI constexpr int wslot(int j) { return j < 12 ? 114688 + j * 4096 : (j - 12) * 4096; }

__global__ __launch_bounds__(512, 2) void fused(const u16* __restrict__ x, const u16* __restrict__ wqkv,
                                                const u16* __restrict__ wo, const u16* __restrict__ w_r,
                                                const u16* __restrict__ wg, const u16* __restrict__ wu,
                                                const u16* __restrict__ wd, u16* __restrict__ out, char* __restrict__ ws) {
  extern __shared__ __attribute__((aligned(16))) char smem[];
  u64 pc0; asm volatile("s_getpc_b64 %0" : "=s"(pc0));
  const int tid = threadIdx.x, lane = tid & 63;
  const int wave = __builtin_amdgcn_readfirstlane(tid >> 6);
  STAMPT(0, 0, 0);
  const int bid = blockIdx.x, xcd = bid & 7, idx = bid >> 3;
  const int rb = (xcd & 1) * 8 + (idx & 7);
  const int hp = (xcd >> 1) * 4 + (idx >> 3);
  const int rp = (xcd & 1) * 8 + (xcd & 2) * 2 + (idx >> 3), cb = (xcd >> 2) * 8 + (idx & 7);
  const int hrot = 4 * (cb & HRM);
  const unsigned lds0 = (unsigned)(size_t)smem;
  const __amdgpu_buffer_rsrc_t wsr = __builtin_amdgcn_make_buffer_rsrc(ws, 0, WS_END, 0x00020000);
#define LDW(off) __builtin_amdgcn_raw_buffer_load_b128(wsr, (off), 0, 16)
#define LDWS(off, so) __builtin_amdgcn_raw_buffer_load_b128(wsr, (off), (so), 16)
#define STW(v, off) __builtin_amdgcn_raw_buffer_store_b128((v), wsr, (off), 0, 16)
#define STWS(v, off, so) __builtin_amdgcn_raw_buffer_store_b128((v), wsr, (off), (so), 16)
  const int kph = 4 * (xcd >> 1) + 8 * (xcd & 1), kg_ = (hp + rb) & 3;
  auto korder = [&](int t) { return ((t + kph) & 15) ^ kg_; };
  u32x4 e0;
  {
    const char* eb = wave < 4 ? (const char*)(pc0 & ~255ull) : uni(ws);
    const unsigned eo = wave < 4 ? (idx * CODE_PER + min(tid >> 4, CODE_PER - 1)) * 128 : wave == 6 && bid == 0 ? SY_RE + lane * 16 : SY_GO + bid * 4;
    asm volatile("global_load_dwordx4 %0, %1, %2 sc1" : "=v"(e0) : "v"(eo), "s"(uni(eb)) : "memory");
  }
  {
    const int r0 = 16 * wave + (lane >> 3), r1 = r0 + 8, pb_ = lane >> 4;
    const unsigned a0 = (unsigned)(r0 * D_ + ((lane & 7) ^ ((r0 >> 1) & 7)) * 8) * 2u, a1 = (unsigned)(r1 * D_ + ((lane & 7) ^ ((r1 >> 1) & 7)) * 8) * 2u;
    const unsigned b0 = (unsigned)((8 * wave + 4 * (pb_ >> 1) + ((lane >> 2) & 3)) * (3 * D_) + (pb_ & 1) * 32 + ((lane & 3) ^ ((wave & 1) * 2)) * 8) * 2u;
#pragma unroll
    for (int t = 0; t < 2; ++t) {
      const char* pa = uni(x + (size_t)rb * 128 * D_) + korder(t) * 128;
      const char* pb = uni(wqkv + hp * 64) + (size_t)korder(t) * (64 * 3 * D_ * 2);
      glds16(a0, pa, lds0 + L_A + t * 16384 + wave * 2048); glds16(a1, pa, lds0 + L_A + t * 16384 + wave * 2048 + 1024);
      const bool late = HALF && ((wave ^ rb >> 1) & 2);
      if (t == 0 && late) BAR();
      glds16(b0 + 2048, pb, lds0 + L_KV + t * 16384 + wave * 1024); glds16(b0 + 4096, pb, lds0 + L_KV + t * 16384 + 8192 + wave * 1024);
      glds16(b0, pb, lds0 + L_Q + t * 8192 + wave * 1024);
      if (t == 0 && !late) BAR();
      if (t == 0 && HALF) BAR();
    }
    SB0();
  }
  volatile LAS int* misc = (volatile LAS int*)(size_t)(lds0 + L_MISC);
  const bool has_mlp = bid < 224;
  const int fs = has_mlp ? (bid & 7) * 28 + (bid >> 3) : (bid & 7) * 28;
  const int tcs = (rb & 4) ? 4 : 3;
  const unsigned rs_o = WS_RS + (rb * 16 + korder(0)) * 1024, cs_o = WS_CS + (hp * 16 + korder(tcs)) * 768;
  const u32x4 ONES4 = {0x3F803F80u, 0x3F803F80u, 0x3F803F80u, 0x3F803F80u};

  auto stageW = [&](int j) {
    const int pb_ = lane >> 4;
    const unsigned vo = (unsigned)((8 * (wave & 3) + 4 * (pb_ >> 1) + ((lane >> 2) & 3)) * D_ + (pb_ & 1) * 32 + ((lane & 3) ^ ((pb_ >> 1) * 2)) * 8) * 2u;
    glds16(vo, uni(wo + cb * 64) + (size_t)(j ^ hrot) * (32 * D_ * 2), lds0 + wslot(j) + (wave & 3) * 1024);
  };
  if (wave < 4) {
    const int wr = wave >> 1, wc = wave & 1;
    const int r = lane & 15, kq = lane >> 4;
    f32x4 acc[4][6];
    {
      unsigned aO[2], bQ[2], bK[2];
#pragma unroll
      for (int k = 0; k < 2; ++k) aO[k] = opq(lds0 + L_A + (64 * wr + r) * 128 + (((4 * k + kq) ^ ((r >> 1) & 7)) * 16));
#pragma unroll
      for (int c = 0; c < 2; ++c) {
        const unsigned o_ = lds0 + kq * 1024 + wc * 256 + (r >> 2) * 64 + ((c ^ (kq & 1)) * 32) + (lane & 3) * 8;
        bQ[c] = opq(o_ + L_Q); bK[c] = opq(o_ + L_KV);
      }
#pragma unroll
      for (int i = 0; i < 4; ++i)
#pragma unroll
        for (int j = 0; j < 6; ++j) acc[i][j] = (f32x4){0.f, 0.f, 0.f, 0.f};
      struct Fr { u32x4 a[4]; u32x2 bl[6], bh[6]; };
      Fr F[2];
      auto bread = [&](int f, int b, int k, int hi) {
        return f < 2 ? trread(bQ[f & 1], k * 4096 + b * 8192 + hi * 512) : trread(bK[f & 1], k * 4096 + b * 16384 + ((f >> 1) - 1) * 8192 + hi * 512);
      };
      auto step = [&](Fr& C, Fr& N, int b, int k, int MC, int ML) {
        int ld = 0;
        constexpr int ORD[6] = {2, 3, 4, 5, 0, 1};
        auto load1 = [&]() {
          if (ML == 0) return;
          if (ld < 4) N.a[ld] = ldsr(aO[k], b * 16384 + ld * 2048);
          else {
            const int e = ld - 4, f = ML == 1 ? (e >> 1) : ORD[(e >> 1) % 6], nf = ML == 1 ? 2 : ML == 2 ? 4 : 6;
            if ((e >> 1) < nf) { if (e & 1) N.bh[f] = bread(f, b, k, 1); else N.bl[f] = bread(f, b, k, 0); }
          }
          ++ld;
        };
#pragma unroll
        for (int m = 0; m < 24; ++m) {
          const int j = ORD[m >> 2], i = m & 3;
          if (!((j < 2 ? 1 : 2) & MC)) continue;
          if (j < 2) acc[i][j] = mfma16(cat(C.bl[j], C.bh[j]), C.a[i], acc[i][j]);
          else acc[i][j] = mfma16(C.a[i], cat(C.bl[j], C.bh[j]), acc[i][j]);
          load1();
          SB0();
        }
      };
      WAITV(5); BAR();
      STAMPT(0, 1, 0);
#pragma unroll
      for (int m = 0; m < 4; ++m) F[0].a[m] = ldsr(aO[0], m * 2048);
#pragma unroll
      for (int f = 0; f < 6; ++f) { F[0].bl[f] = bread(f, 0, 0, 0); F[0].bh[f] = bread(f, 0, 0, 1); }
      for (int t4 = 0; t4 < 12; t4 += 4) {
#pragma unroll
        for (int b = 0; b < 4; ++b) {
          step(F[0], F[1], b, 1, 3, 3);
          if (b == 0 && t4 == 0) { asm volatile("s_waitcnt vmcnt(0)" :: "v"(e0) : "memory"); BAR(); }
          step(F[1], F[0], (b + 1) & 3, 0, 3, 3);
          BAR();
        }
      }
#pragma unroll
      for (int b = 0; b < 4; ++b) {
        step(F[0], F[1], b, 1, 2, 2);
        step(F[1], F[0], (b + 1) & 3, 0, 2, b < 3 ? 2 : 0);
        if (b != 2) BAR();
      }
      STAMPT(0, 2, 0);
      {
        const float* sa = (const float*)(smem + L_SA) + 64 * wr + 4 * kq;
        const float* cs = (const float*)(smem + L_CS) + wc * 96 + r;
        const float c4[4] = {cs[32], cs[48], cs[64], cs[80]};
        unsigned kp[4][2][2], vp[4][2][2];
#pragma unroll
        for (int i = 0; i < 4; ++i) {
          const f32x4 a4 = *(const f32x4*)(sa + 16 * i), b4 = *(const f32x4*)(sa + 128 + 16 * i);
          const f32x2 aa[2] = {{a4[0], a4[1]}, {a4[2], a4[3]}}, bb[2] = {{b4[0], b4[1]}, {b4[2], b4[3]}};
#pragma unroll
          for (int j = 0; j < 2; ++j)
#pragma unroll
            for (int e = 0; e < 2; ++e) {
              const f32x2 tk = (f32x2){acc[i][2 + j][2 * e], acc[i][2 + j][2 * e + 1]} * aa[e] + bb[e] * c4[j];
              const f32x2 tv = (f32x2){acc[i][4 + j][2 * e], acc[i][4 + j][2 * e + 1]} * aa[e] + bb[e] * c4[2 + j];
              kp[i][j][e] = phi_pair(tk);
              vp[i][j][e] = pack2(tv[0], tv[1]);
            }
        }
        f32x4 kv[2][2], zv[2];
#pragma unroll
        for (int d = 0; d < 2; ++d) {
          zv[d] = (f32x4){0.f, 0.f, 0.f, 0.f}; kv[d][0] = zv[d]; kv[d][1] = zv[d];
#pragma unroll
          for (int u = 0; u < 2; ++u) {
            const u32x4 ka = {kp[2 * u][d][0], kp[2 * u][d][1], kp[2 * u + 1][d][0], kp[2 * u + 1][d][1]};
#pragma unroll
            for (int e = 0; e < 2; ++e)
              kv[d][e] = mfma16(ka, (u32x4){vp[2 * u][e][0], vp[2 * u][e][1], vp[2 * u + 1][e][0], vp[2 * u + 1][e][1]}, kv[d][e]);
            zv[d] = mfma16(ka, ONES4, zv[d]);
          }
        }
        char* xo = smem + L_XCH + wave * 2048 + lane * 16;
        *(f32x4*)xo = wr ? kv[0][0] : kv[0][1];
        *(f32x4*)(xo + 1024) = wr ? kv[1][0] : kv[1][1];
        if (r < 2) *(f32x4*)(smem + L_XCH + 8192 + wave * 128 + r * 64 + kq * 16) = r ? zv[1] : zv[0];
#pragma unroll
        for (int m = 0; m < 4; ++m) F[0].a[m] = ldsr(aO[0], m * 2048);
#pragma unroll
        for (int f = 0; f < 2; ++f) { F[0].bl[f] = bread(f, 0, 0, 0); F[0].bh[f] = bread(f, 0, 0, 1); }
        STAMPT(1, 0, 0);
        asm volatile("s_waitcnt lgkmcnt(0)" ::: "memory");
        misc[16 + wave] = 1;
#pragma nounroll
        for (unsigned s_ = 0; s_ < SPIN_CAP; ++s_) if (__builtin_amdgcn_readfirstlane(misc[16 + (wave ^ 2)])) break;
        asm volatile("" ::: "memory");
        const char* xi = smem + L_XCH + (wave ^ 2) * 2048 + lane * 16;
        const f32x4 s0 = *(const f32x4*)xi + (wr ? kv[0][1] : kv[0][0]), s1 = *(const f32x4*)(xi + 1024) + (wr ? kv[1][1] : kv[1][0]);
        const unsigned ko = WS_KVP + (hp * 16 + rb) * KVB + wc * 2176;
        STW(seal((u32x4){pack2(s0[0], s0[1]), pack2(s0[2], s0[3]), pack2(s1[0], s1[1]), pack2(s1[2], s1[3])}), ko + wr * 1024 + lane * 16);
        if (wr == 0 && lane < 8) {
          const f32x4 z = *(const f32x4*)(smem + L_XCH + 8192 + wave * 128 + lane * 16) + *(const f32x4*)(smem + L_XCH + 8192 + (wave + 2) * 128 + lane * 16);
          STW(seal(__builtin_bit_cast(u32x4, z)), ko + 2048 + lane * 16);
        }
                STAMPT(0, 3, 0);
      }
#pragma unroll
      for (int b = 0; b < 4; ++b) {
        step(F[0], F[1], b, 1, 1, 1);
        step(F[1], F[0], (b + 1) & 3, 0, 1, b < 3 ? 1 : 0);
      }
    }
    u32x4 qp[4];
    {
      const float* sa = (const float*)(smem + L_SA) + 64 * wr + r;
      const float* cs = (const float*)(smem + L_CS) + wc * 96 + 4 * kq;
      const f32x4 c0 = *(const f32x4*)cs, c1 = *(const f32x4*)(cs + 16);
#pragma unroll
      for (int i = 0; i < 4; ++i) {
        const float a = sa[16 * i], b = sa[128 + 16 * i];
        qp[i][0] = phi_pair((f32x2){acc[i][0][0], acc[i][0][1]} * a + (f32x2){c0[0], c0[1]} * b);
        qp[i][1] = phi_pair((f32x2){acc[i][0][2], acc[i][0][3]} * a + (f32x2){c0[2], c0[3]} * b);
        qp[i][2] = phi_pair((f32x2){acc[i][1][0], acc[i][1][1]} * a + (f32x2){c1[0], c1[1]} * b);
        qp[i][3] = phi_pair((f32x2){acc[i][1][2], acc[i][1][3]} * a + (f32x2){c1[2], c1[3]} * b);
        asm volatile("" : "+v"(qp[i]));
      }
    }
    STAMPT(0, 4, 0);
    LBAR();
    STAMPT(0, 5, 0);
    {
      const u32x4 k0 = *(const u32x4*)(smem + L_KVL + wc * 2048 + lane * 16), k1 = *(const u32x4*)(smem + L_KVL + wc * 2048 + 1024 + lane * 16);
      const float* zl = (const float*)(smem + L_ZL) + wc * 64 + kq * 4;
      const f32x4 za = *(const f32x4*)zl + *(const f32x4*)(zl + 32), zb = *(const f32x4*)(zl + 16) + *(const f32x4*)(zl + 48);
      const u32x4 zf = {pack2(za[0], za[1]), pack2(za[2], za[3]), pack2(zb[0], zb[1]), pack2(zb[2], zb[3])};
      const f32x4 Z4 = {0.f, 0.f, 0.f, 0.f};
      const unsigned oo = WS_O + ((rb * 8 + wr * 4) * 32 + 2 * hp + wc) * 1024 + lane * 16;
#pragma unroll
      for (int i = 0; i < 4; ++i) {
        const f32x4 dn = mfma16(zf, qp[i], Z4), x0 = mfma16(k0, qp[i], Z4), x1 = mfma16(k1, qp[i], Z4);
        const float ri = __builtin_amdgcn_rcpf(dn[0] + 1e-6f);
        STW(seal((u32x4){pack2(x0[0] * ri, x0[1] * ri), pack2(x0[2] * ri, x0[3] * ri), pack2(x1[0] * ri, x1[1] * ri), pack2(x1[2] * ri, x1[3] * ri)}),
            oo + i * 32768);
      }
      if (lane == 0) st32(ws, SY_FB + rb * 256 + hp * 16 + wave * 4, 1u);
      misc[24 + wave] = 1;
    }
    STAMPT(0, 6, 0);
    {
      const int h = lane >> 5, s32 = lane & 31;
      u32x2 wv[8];
      auto wvld = [&]() {
#pragma unroll
        for (int c = 0; c < 8; ++c) wv[c] = *(const volatile LAS u32x2*)(size_t)(lds0 + L_WR + c * 16 + h * 8);
      };
      wvld();
      u32x4 of[32][2];
      const unsigned ol = WS_O + (rp * 8 + wave * 2 + (kq & 1)) * 32768 + (h * 16 + r) * 16;
      auto oload1 = [&](int j, int e) { of[j][e] = LDWS(ol + e * 512 + (j & 3) * 1024, ((j & ~3) ^ hrot) * 1024); };
      auto oload = [&](int j) { oload1(j, 0); oload1(j, 1); };
      auto omark = [&](int j) {
        unsigned mx = 0;
#pragma unroll
        for (int e = 0; e < 4; ++e) mx = max(max(mx, max(of[j + e][0][1], of[j + e][0][3])), max(of[j + e][1][1], of[j + e][1][3]));
        return mx;
      };
      f32x16 a2[4];
      unsigned wO[3];
#pragma unroll
      for (int e = 0; e < 3; ++e) wO[e] = opq(lds0 + e * 65536 + h * 512 + (r >> 2) * 64 + (((kq & 1) ^ h) * 32) + (lane & 3) * 8);
      constexpr int PF = GPF, OW = GOW;
      u32x2 wl[8], wh[8];
      auto wld = [&](int g) {
        const int j = g >> 2;
        const int o_ = (wslot(j) & 65535) + ((g >> 1) & 1) * 1024 + (g & 1) * 256;
        wl[g & 7] = trread(wO[wslot(j) >> 16], o_); wh[g & 7] = trread(wO[wslot(j) >> 16], o_ + 2048);
      };
      const int row = wave * 32 + s32;
      const unsigned xb = opq(lds0 + L_X2 + row * 128 + h * 8);
      const int sw = (row >> 1) & 7;
      float sw_ = 0.f;
#pragma unroll
      for (int c = 0; c < 16; ++c) sw_ = dot2(wv[c >> 1][c & 1], 0x3F803F80u, sw_);
      sw_ = xh_sum(sw_);
      asm volatile("" : "+v"(sw_));
      BAR();
      STAMPT(0, 7, 0);
#pragma unroll
      for (int j = 0; j < OW0; ++j) { oload(j); SB0(); }
#pragma unroll
      for (int c = 0; c < 8; ++c) {
        const u32x2 xx = *(const LAS u32x2*)((const LAS char*)(size_t)(xb + ((c ^ sw) * 16)));
#pragma unroll
        for (int i = 0; i < 2; ++i) {
          a2[c >> 2][(c & 3) * 4 + 2 * i] = bflo(xx[i]); a2[c >> 2][(c & 3) * 4 + 2 * i + 1] = bfhi(xx[i]);
          a2[2 + (c >> 2)][(c & 3) * 4 + 2 * i] = 0.f; a2[2 + (c >> 2)][(c & 3) * 4 + 2 * i + 1] = 0.f;
        }
      }
#pragma unroll
      for (int g = 0; g < PF; ++g) wld(g);
      SB0();
      unsigned mx = omark(0);
#pragma unroll
      for (int j = 0; j < 32; ++j) {
        if (j == 30) wvld();
        if ((j & 3) == 0 && __builtin_expect(__any(mx == EMPTY), 0)) {
#pragma nounroll
          for (unsigned s_ = 0; s_ < SPIN_CAP; ++s_) {
            asm volatile("" ::: "memory");
            STAMPT(3, 12, 0);
            oload(j); oload(j + 1); oload(j + 2); oload(j + 3);
            if (!__any(omark(j) == EMPTY)) break;
          }
        }
#pragma unroll
        for (int n = 0; n < 4; ++n) {
          const int g = j * 4 + n;
          a2[n] = mfma32(cat(wl[g & 7], wh[g & 7]), of[j][n >> 1], a2[n]);
          SB0();
          if (g + PF < 128) wld(g + PF);
          {
            constexpr int RAMP = OW - OW0;
            const int q = j < RAMP ? 2 * OW0 + 4 * j + n : (n & 1) ? -1 : 2 * (j + OW) + (n >> 1);
            if (q >= 0 && q < 64) oload1(q >> 1, q & 1);
          }
          if (n == 0 && j + OW == 32) misc[20 + wave] = 1;
          if ((j & 2) && j < 28) {
            const int f = (j & 1) * 4 + n, jj = (j & ~3) + 4 + (f >> 1);
            if (f == 0) mx = 0;
            mx = max(max(mx, of[jj][f & 1][1]), of[jj][f & 1][3]);
          }
          SB0();
          asm volatile("" :: "v"(wl[g & 7]), "v"(wh[g & 7]), "v"(of[j][n >> 1]));
          SB0();
        }
      }
      STAMPT(0, 8, 0); STAMPT(3, 1, 64); STAMPT(3, 2, 128); STAMPT(3, 3, 192);
      a2[0] += a2[2]; a2[1] += a2[3];
      misc[8 + wave] = 1;
      unsigned hb[16];
      {
        float s[2] = {0.f, 0.f}, sq[2] = {0.f, 0.f}, pw[2] = {0.f, 0.f};
#pragma unroll
        for (int i = 0; i < 16; ++i) {
          const unsigned t = pack2(a2[i >> 3][(i & 7) * 2], a2[i >> 3][(i & 7) * 2 + 1]);
          hb[i] = t;
          s[i & 1] = dot2(t, 0x3F803F80u, s[i & 1]); sq[i & 1] = dot2(t, t, sq[i & 1]); pw[i & 1] = dot2(t, wv[i >> 1][i & 1], pw[i & 1]);
        }
        const u32x4 st = {__float_as_uint(xh_sum(s[0] + s[1])), __float_as_uint(xh_sum(sq[0] + sq[1])), __float_as_uint(xh_sum(pw[0] + pw[1])), __float_as_uint(sw_)};
        if (h == 0) STW(seal(st), WS_ST2 + ((rp * 16 + cb) * 128 + row) * 16);
      }
      STAMPT(0, 9, 0);
      u32x4 pu_[8];
      __builtin_amdgcn_s_sleep(SLC0);
#pragma nounroll
      for (unsigned s_ = 0; s_ < SPIN_CAP; ++s_) {
        unsigned mk = 0u;
#pragma unroll
        for (int j = 0; j < 8; ++j) { pu_[j] = LDW(WS_ST2 + ((rp * 16 + h * 8 + j) * 128 + row) * 16); mk = max(max(mk, pu_[j][1]), pu_[j][3]); }
        if (!__any(mk == EMPTY)) break;
        asm volatile("" ::: "memory");
        __builtin_amdgcn_s_sleep(SLC1);
      }
      STAMPT(0, 10, 0);
      f32x4 tt = {0.f, 0.f, 0.f, 0.f};
#pragma unroll
      for (int j = 0; j < 8; ++j) tt += __builtin_bit_cast(f32x4, pu_[j]);
      const float mean = xh_sum(tt[0]) * (1.0f / 1024.0f);
      const float var = fmaxf(xh_sum(tt[1]) * (1.0f / 1024.0f) - mean * mean, 0.f);
      const float mb = rbf(mean), rs = __builtin_amdgcn_rcpf(rbf(__builtin_amdgcn_sqrtf(rbf(rbf(var) + 1e-5f))));
      float sc = rbf((xh_sum(tt[2]) - mb * xh_sum(tt[3])) * rs);
      if (!(sc == sc)) sc = -INFINITY;
      unsigned b0 = h ? 0u : mk_key(sc, rp * 128 + row), b1 = 0;
      top2_wave(b0, b1);
      const int pan = rp * 4 + wave;
      if (lane == 0) st64(ws, SY_FD + cb * 512 + pan * 8, ((u64)b1 << 32) | b0);
      STAMPT(1, 11, 0);
      unsigned nq[16];
#pragma unroll
      for (int i = 0; i < 16; ++i) {
        const unsigned dq = pack2(bflo(hb[i]) - mb, bfhi(hb[i]) - mb);
        nq[i] = pack2(bflo(dq) * rs, bfhi(dq) * rs);
      }
      {
        const int c0 = key_idx(b0) & 31, c1 = key_idx(b1) & 31;
        char* tb = smem + L_TB + wave * 512;
        if (s32 == c0 || s32 == c1) {
          char* tp = tb + (s32 == c0 ? 0 : 256) + h * 8;
#pragma unroll
          for (int c = 0; c < 8; ++c) { *(u32x2*)(tp + c * 16) = (u32x2){nq[2 * c], nq[2 * c + 1]}; *(u32x2*)(tp + 128 + c * 16) = (u32x2){hb[2 * c], hb[2 * c + 1]}; }
        }
        __builtin_amdgcn_wave_barrier();
        const u32x4 v4 = seal(*(const u32x4*)(tb + s32 * 16));
#pragma unroll
        for (int rr_ = 0; rr_ < XSR; rr_ += 2)
          if (rr_ + h < XSR) STW(v4, WS_XS + (rr_ + h) * 524288 + (pan * 2 + (s32 >> 4)) * 4096 + ((s32 >> 3) & 1) * 2048 + cb * 128 + (s32 & 7) * 16);
      }
      u64 cc = 0;
      if (wave == 0) cc = ld64(ws, SY_FD + cb * 512 + lane * 8);
#pragma unroll
      for (int c = 0; c < 8; ++c) {
        u32x2 ob;
#pragma unroll
        for (int i = 0; i < 2; ++i) ob[i] = pack2(bflo(hb[2 * c + i]) + bflo(nq[2 * c + i]), bfhi(hb[2 * c + i]) + bfhi(nq[2 * c + i]));
        *(LAS u32x2*)((LAS char*)(size_t)(xb + ((c ^ sw) * 16))) = ob;
      }
      if (wave == 0) {
#pragma nounroll
        for (unsigned s_ = 0; s_ < SPIN_CAP; ++s_) {
          if (__all((unsigned)(cc >> 32) != EMPTY)) break;
          __builtin_amdgcn_s_sleep(SLD1);
          cc = ld64(ws, SY_FD + cb * 512 + lane * 8);
        }
        STAMPT(1, 13, 0);
        unsigned c0 = (unsigned)cc, c1 = (unsigned)(cc >> 32);
        top2_wave(c0, c1);
        const unsigned w0 = key_idx(c0), w1 = key_idx(c1);
        const unsigned k0 = __builtin_amdgcn_readlane((unsigned)cc, w0 >> 5), k1 = __builtin_amdgcn_readlane((unsigned)cc, w1 >> 5);
        const unsigned sl0 = (w0 >> 5) * 2 + (k0 == c0 ? 0 : 1), sl1 = (w1 >> 5) * 2 + (k1 == c1 ? 0 : 1);
        if (lane == 0) { misc[1] = w0; misc[2] = w1; misc[3] = sl0; misc[4] = sl1; misc[5] = c0; misc[6] = c1; }
        if (has_mlp) {
          const unsigned xo = WS_XS + (idx & (XSR - 1)) * 524288u + (h ? sl1 : sl0) * 4096u + (fs & 3) * 512 + s32 * 16;
          u32x4 q;
#pragma nounroll
          for (unsigned s_ = 0; s_ < SPIN_CAP; ++s_) {
            q = LDW(xo);
            if (__all(max(q[1], q[3]) != EMPTY)) break;
            asm volatile("" ::: "memory");
            __builtin_amdgcn_s_sleep(XSL1);
          }
          *(u32x4*)(smem + L_XS + h * 528 + s32 * 16) = q;
        }
      }
      LBAR();
      STAMPT(0, 11, 0);
      const int sel0 = misc[1], sel1 = misc[2];
#pragma unroll
      for (int m = 0; m < 4; ++m) {
        const int rw = wave * 32 + m * 8 + (lane >> 3), grow = rp * 128 + rw;
        const u32x4 v = *(const u32x4*)(smem + L_X2 + rw * 128 + (((lane & 7) ^ ((rw >> 1) & 7)) * 16));
        if (grow != sel0 && grow != sel1) *(u32x4*)(out + (size_t)grow * D_ + cb * 64 + (lane & 7) * 8) = v;
      }
      STAMPT(0, 12, 0);
    }
  } else {
    __builtin_amdgcn_s_setprio(2);
    const int hw = wave - 4, mt = tid & 255;
    u32x4 wgr[8], wur[8], wdr[8];
    const int krow = hw * 64 + (lane >> 3);
    const unsigned gof = opq((unsigned)(((fs & 3) * 256 + krow) * F_ + (fs >> 2) * 64 + (lane & 7) * 8) * 2u);
    const unsigned dof = opq((unsigned)((fs * 16 + (lane >> 5) * 8) * D_ + (hw * 32 + (lane & 31)) * 8) * 2u);
    const unsigned wto = opq((unsigned)((idx * 32 + (mt >> 3)) * 2048 + (cb >> 3) * 1024 + (mt & 7) * 128));
#define WLOAD(dst, vo, base) asm volatile("global_load_dwordx4 %0, %1, %2" WPOL : "=v"(dst) : "v"(vo), "s"(uni(base)) : "memory")
    auto wload = [&](int i) {
      if (i < 8) WLOAD(wgr[i], gof, wg + i * (8 * F_));
      else if (i < 16) WLOAD(wur[i - 8], gof, wu + (i - 8) * (8 * F_));
      else WLOAD(wdr[i - 16], dof, wd + (i - 16) * D_);
    };
    auto wpin = [&](int i) {
      if (i < 8) asm volatile("" : "+v"(wgr[i])); else if (i < 16) asm volatile("" : "+v"(wur[i - 8])); else asm volatile("" : "+v"(wdr[i - 16]));
    };
    const int pbi = lane >> 4, pq = (lane >> 2) & 3, pch = lane & 3;
    if (hw < 2) {
      const char* baseA = uni(x + (size_t)rb * 128 * D_);
      const char* baseB = uni(wqkv + hp * 64);
      unsigned voA[2], voB[2];
      {
        const int r0 = 64 * hw + (lane >> 3), r1 = r0 + 8;
        voA[0] = (unsigned)(r0 * D_ + ((lane & 7) ^ ((r0 >> 1) & 7)) * 8) * 2u;
        voA[1] = (unsigned)(r1 * D_ + ((lane & 7) ^ ((r1 >> 1) & 7)) * 8) * 2u;
        voB[0] = (unsigned)((32 * hw + 4 * (pbi >> 1) + pq) * (3 * D_) + (pbi & 1) * 32 + pch * 8) * 2u;
        voB[1] = (unsigned)((32 * hw + 8 + 4 * (pbi >> 1) + pq) * (3 * D_) + (pbi & 1) * 32 + (pch ^ 2) * 8) * 2u;
      }
      auto stage = [&](int t) {
        const int kt = korder(t), b = t & 3;
        const char* pa = baseA + kt * 128;
        const char* pb = baseB + (size_t)kt * (64 * 3 * D_ * 2);
#pragma unroll
        for (int j = 0; j < 8; ++j) glds16(voA[j & 1] + (j >> 1) * (16 * D_ * 2), pa, lds0 + L_A + b * 16384 + (8 * hw + j) * 1024);
#pragma unroll
        for (int j0 = 0; j0 < 4; ++j0) {
          const int j = j0 ^ (HALF ? (rb >> 1) & 2 : 0);
          const unsigned vo = voB[j0 & 1] + (j >> 1) * (16 * 3 * D_ * 2);
          glds16(vo + 2048, pb, lds0 + L_KV + b * 16384 + (4 * hw + j) * 1024);
          glds16(vo + 4096, pb, lds0 + L_KV + b * 16384 + 8192 + (4 * hw + j) * 1024);
          glds16(vo, pb, lds0 + L_Q + b * 8192 + (4 * hw + j) * 1024);
        }
      };
      WAITV(5); BAR();
      stage(2);
      WAITV(20); BAR();
      stage(3);
      for (int t = 0; t < 12; ++t) { WAITV(20); BAR(); stage(t + 4); }
      WAITV(20); BAR();
      asm volatile("s_waitcnt vmcnt(0)" :: "v"(e0) : "memory");
      BAR();
    } else {
      const int aw = hw - 2, at = tid & 127;
      const bool master = bid == 0 && aw == 0;
      unsigned hs_cur = 0, hs_g0 = 0;
      u32x4 ms_exp = {0, 0, 0, 0}, pm = {0, 0, 0, 0};
      const unsigned p_off = opq(master ? SY_RE + lane * 16 : (lane == 1 ? SY_GO : SY_CH) + bid * 4);
      const char* wsu = uni(ws);
#define HS_PROBE() asm volatile("global_load_dwordx4 %0, %1, %2 sc1" : "+v"(pm) : "v"(p_off), "s"(wsu) : "memory")
#define HS_WAIT() asm volatile("s_waitcnt vmcnt(0)" : "+v"(pm) :: "memory")
      bool done = false;
      auto hs_check = [&]() __attribute__((always_inline)) {
        HS_WAIT();
        if (!master) {
          const unsigned c = __builtin_amdgcn_readlane(pm[0], 0), go = __builtin_amdgcn_readlane(pm[0], 1);
          if (go != hs_g0) done = true;
          else if (c != hs_cur) { hs_cur = c; st32(ws, SY_RE + bid * 4, c); }
        } else if (!done && __all(pm[0] == ms_exp[0] && pm[1] == ms_exp[1] && pm[2] == ms_exp[2] && pm[3] == ms_exp[3])) {
          STW(ms_exp, SY_GO + lane * 16);
          done = true;
        }
      };
      u32x4 e4 = {EMPTY, EMPTY, EMPTY, EMPTY};
      asm volatile("" : "+v"(e4));
      const bool prober = master || aw == 1;
      const int kq = lane >> 4, r = lane & 15;
      constexpr unsigned OOB = 0x80000000u;
      const unsigned ko = WS_KVP + (hp * 16 + rb) * KVB + aw * 2176;
      const unsigned l16 = opq(lane * 16);
      const unsigned vm0 = opq(lane < 32 ? rs_o + aw * 512 + lane * 16 : lane < 56 ? cs_o + aw * 384 + (lane - 32) * 16 : ko + 2048 + (lane - 56) * 16);
      const unsigned vm1 = opq(aw ? (lane < 2 ? SY_FD + cb * 512 + rp * 32 + lane * 16 : lane == 2 ? SY_FB + rb * 256 + hp * 16 : OOB)
                                  : (has_mlp ? WS_GU + fs * 1024 + lane * 16 : OOB));
      const unsigned vxs = opq(WS_XS + (rp * 16 + (at >> 3)) * 2048 + cb * 128 + (at & 7) * 16);
      const unsigned vrs = opq((r >> 2) == kq ? rs_o + (64 * aw + r) * 8 : OOB);
      const unsigned vc0 = opq(cs_o + aw * 384 + lane * 4), vc1 = opq(lane < 32 ? cs_o + aw * 384 + 256 + lane * 4 : OOB);
      const unsigned sg_off = opq(aw ? WS_CS + hp * (16 * 768) + min(lane, 47) * 16 : WS_RS + rb * 16384 + lane * 16), sg_st = aw ? 768 : 1024;
      unsigned cb_[2][2];
#pragma unroll
      for (int c = 0; c < 2; ++c) {
        const unsigned o_ = lds0 + kq * 1024 + aw * 256 + (r >> 2) * 64 + ((c ^ (kq & 1)) * 32) + (lane & 3) * 8;
        cb_[0][c] = opq(o_ + L_Q + (tcs & 3) * 8192); cb_[1][c] = opq(o_ + L_KV + (tcs & 3) * 16384);
      }
      const int hq = (3 - (int)((size_t)wqkv >> 7) - (hp & ~3)) & 7;
      const int qln = ((((hp & 3) - hq - 1) & 3) * 8 + (idx & 7)) * 96 + min(at, 95);
      const unsigned vq = opq((unsigned)((((kph + 4) * 64 + qln / 3) & 1023) * 6144 + (qln % 3) * 2048 + ((hp & ~3) + (hq & 3)) * 128));
      auto oclear = [&](int i) { STWS(e4, l16, WS_O + ((rb * 8 + i) * 32 + 2 * hp + aw) * 1024); };
      WAITV(5); BAR();
      STW(e4, vm0);
      u32x4 g0;
      if (master) g0 = LDW(SY_GO + lane * 16);
      u32x2 rst[4];
      {
        u32x4 a[4][2];
#pragma unroll
        for (int i = 0; i < 4; ++i)
#pragma unroll
          for (int k = 0; k < 2; ++k) a[i][k] = *(const u32x4*)(smem + L_A + (64 * aw + 16 * i + r) * 128 + (((4 * k + kq) ^ ((r >> 1) & 7)) * 16));
        const f32x4 Z4 = {0.f, 0.f, 0.f, 0.f};
        const int l3 = lane & 3;
#pragma unroll
        for (int i = 0; i < 4; ++i) {
          const f32x4 s1 = mfma16(ONES4, a[i][1], mfma16(ONES4, a[i][0], Z4)), s2 = mfma16(a[i][1], a[i][1], mfma16(a[i][0], a[i][0], Z4));
          const float q = l3 == 0 ? s2[0] : l3 == 1 ? s2[1] : l3 == 2 ? s2[2] : s2[3];
          rst[i] = (u32x2){min(__float_as_uint(s1[0]), 0xFFFFFFFEu), min(__float_as_uint(q), 0xFFFFFFFEu)};
          asm volatile("" : "+v"(rst[i]));
        }
      }
      u32x4 e1;
      asm volatile("s_waitcnt vmcnt(0)\n\tv_mov_b32 %0, %4\n\tv_mov_b32 %1, %5\n\tv_mov_b32 %2, %6\n\tv_mov_b32 %3, %7"
                   : "=&v"(e1[0]), "=&v"(e1[1]), "=&v"(e1[2]), "=&v"(e1[3]) : "v"(e0[0]), "v"(e0[1]), "v"(e0[2]), "v"(e0[3]) : "memory");
      hs_g0 = __builtin_amdgcn_readfirstlane(e1[0]);
      BAR();
      if (master) {
#pragma unroll
        for (int j = 0; j < 4; ++j) { const unsigned c = e1[j] + 1u; ms_exp[j] = (c == g0[j]) ? c + 1u : c; }
        STW(ms_exp, SY_CH + lane * 16);
      }
      unsigned qt = 0;
      if (hq < 4 && (hp & 3) != hq) asm volatile("global_load_dword %0, %1, %2" : "=v"(qt) : "v"(vq), "s"(uni(wqkv)) : "memory");
#pragma unroll
      for (int i = 0; i < 4; ++i) __builtin_amdgcn_raw_buffer_store_b64(rst[i], wsr, vrs + i * 128, 0, 16);
      BAR();
      STAMPT(2, 0, 384);
      oclear(0); oclear(1); oclear(2); oclear(3); oclear(4); oclear(5);
      BAR();
      STAMPT(2, 1, 384);
      oclear(6); oclear(7);
      STWS(e4, l16, ko); STWS(e4, l16, ko + 1024); STW(e4, vm1);
      if (aw == 1) HS_PROBE();
      BAR();
      STAMPT(2, 2, 384);
      unsigned cv0, cv1;
      {
        f32x4 cacc[6];
#pragma unroll
        for (int f = 0; f < 6; ++f) {
          cacc[f] = (f32x4){0.f, 0.f, 0.f, 0.f};
#pragma unroll
          for (int k = 0; k < 2; ++k) {
            const int o_ = (f >= 4 ? 8192 : 0) + k * 4096;
            cacc[f] = mfma16(ONES4, cat(trread(cb_[f >= 2][f & 1], o_), trread(cb_[f >= 2][f & 1], o_ + 512)), cacc[f]);
          }
        }
        cv0 = min(__float_as_uint(kq == 0 ? cacc[0][0] : kq == 1 ? cacc[1][0] : kq == 2 ? cacc[2][0] : cacc[3][0]), 0xFFFFFFFEu);
        cv1 = min(__float_as_uint(kq == 0 ? cacc[4][0] : cacc[5][0]), 0xFFFFFFFEu);
        asm volatile("" : "+v"(cv0), "+v"(cv1));
      }
      HS_WAIT();
      asm volatile("" :: "v"(qt));
      BAR();
      STAMPT(2, 3, 384);
      if (aw == 1) { hs_cur = __builtin_amdgcn_readlane(pm[0], 0); st32(ws, SY_RE + bid * 4, hs_cur); }
      __builtin_amdgcn_raw_buffer_store_b32(cv0, wsr, vc0, 0, 16); __builtin_amdgcn_raw_buffer_store_b32(cv1, wsr, vc1, 0, 16);
      BAR();
      STAMPT(2, 4, 384);
      if (master) HS_PROBE();
      STWS(e4, l16, WS_ST2 + (rp * 16 + cb) * 2048 + aw * 1024);
#pragma unroll
      for (int rr_ = 0; rr_ < XSR; ++rr_) STWS(e4, vxs, rr_ * 524288);
      BAR();
      STAMPT(2, 5, 384);
      if (master) { hs_check(); HS_PROBE(); }
      if (has_mlp) {
#pragma unroll
        for (int i = 0; i < 4; ++i) STWS(e4, l16, WS_YP + fs * 8192 + aw * 4096 + i * 1024);
      }
      BAR();
      STAMPT(2, 6, 384);
      if (master) hs_check();
      if (prober) HS_PROBE();
      BAR();
      STAMPT(2, 7, 384);
      if (prober) {
        hs_check();
#pragma nounroll
        for (unsigned s_ = 0; s_ < SPIN_CAP && !done; ++s_) { HS_PROBE(); hs_check(); }
      }
      BAR();
      STAMPT(2, 8, 384);
      u32x4 sg[16], wrv;
      asm volatile("global_load_dwordx4 %0, %1, %2" : "=v"(wrv) : "v"(l16 & 127u), "s"(uni(w_r + cb * 64)) : "memory");
#pragma unroll
      for (int i = 0; i < 16; ++i) {
        asm volatile("global_load_dwordx4 %0, %1, %2 sc1" : "=v"(sg[i]) : "v"(sg_off), "s"(wsu + i * sg_st) : "memory");
        if (i == SGS) { BAR(); STAMPT(2, 9, 384); }
      }
      auto tsum = [&](int o) __attribute__((always_inline)) {
        f32x4 t[4];
#pragma unroll
        for (int i = 0; i < 4; ++i) t[i] = add4(__builtin_bit_cast(f32x4, sg[o + i]), __builtin_bit_cast(f32x4, sg[o + 4 + i]));
        return add4(add4(t[0], t[2]), add4(t[1], t[3]));
      };
      WAITV(8);
#pragma unroll
      for (int i = 0; i < 8; ++i) asm volatile("" : "+v"(sg[i]));
      asm volatile("" : "+v"(wrv));
      f32x4 sm = tsum(0), sn;
      asm volatile("" : "+v"(sm));
      BAR();
      STAMPT(2, 10, 384);
      WAITV(0);
#pragma unroll
      for (int i = 8; i < 16; ++i) asm volatile("" : "+v"(sg[i]));
      sn = tsum(8);
      asm volatile("" : "+v"(sn));
      BAR();
      STAMPT(2, 11, 384);
      sm = add4(sm, sn);
      const float chk = (sm[0] + sm[1]) + (sm[2] + sm[3]);
      if (__builtin_expect(__any(chk != chk), 0)) {
#pragma nounroll
        for (unsigned s_ = 0; s_ < SPIN_CAP; ++s_) {
          unsigned mx = 0;
#pragma unroll
          for (int i = 0; i < 16; ++i) { sg[i] = LDWS(sg_off, i * sg_st); mx = max(max(mx, max(sg[i][0], sg[i][1])), max(sg[i][2], sg[i][3])); }
          if (!__any(mx == EMPTY)) break;
        }
        sm = add4(tsum(0), tsum(8));
      }
      STAMPT(1, 1, 384);
      float mean[2], vr[2];
#pragma unroll
      for (int e = 0; e < 2; ++e) {
        mean[e] = sm[2 * e] * (1.0f / 1024.0f);
        vr[e] = fmaxf(sm[2 * e + 1] * (1.0f / 1024.0f) - mean[e] * mean[e], 0.f);
        asm volatile("" : "+v"(mean[e]), "+v"(vr[e]));
      }
      BAR();
      STAMPT(2, 12, 384);
      if (aw == 1) { if (lane < 48) *(f32x4*)(smem + L_CS + lane * 16) = sm; else *(LAS u32x2*)(misc + (lane - 48) * 2) = (u32x2){0u, 0u}; }
#pragma unroll
      for (int e = 0; e < 2; ++e) { vr[e] = __builtin_amdgcn_sqrtf(rbf(rbf(vr[e]) + 1e-5f)); mean[e] = -rbf(mean[e]); asm volatile("" : "+v"(mean[e]), "+v"(vr[e])); }
      BAR();
      STAMPT(2, 13, 384);
      if (aw == 1) *(u32x4*)(smem + L_WR + (l16 & 127u)) = wrv;
      if (aw == 0) {
        const f32x2 ra = {__builtin_amdgcn_rcpf(rbf(vr[0])), __builtin_amdgcn_rcpf(rbf(vr[1]))};
        *(f32x2*)(smem + L_SA + lane * 8) = ra; *(f32x2*)(smem + L_SB + lane * 8) = (f32x2){mean[0] * ra[0], mean[1] * ra[1]};
      }
      STAMPT(1, 2, 384);
    }
    unsigned wt;
    asm volatile("global_load_dword %0, %1, %2" : "=v"(wt) : "v"(wto), "s"(uni(wo)) : "memory");
    wload(0); wload(1); wload(2);
    LBAR();
    STAMPT(2, 15, 384);
#pragma unroll
    for (int i = 3; i < NW1; ++i) { WAITV(THR - 1); wload(i); }
    unsigned vx2[2];
#pragma unroll
    for (int m = 0; m < 2; ++m) { const int rw = m * 8 + (lane >> 3); vx2[m] = (unsigned)(rw * D_ + ((lane & 7) ^ ((rw >> 1) & 7)) * 8) * 2u; }
    u32x4 wr_[24];
    const unsigned voW = (unsigned)((8 * hw + 4 * (pbi >> 1) + pq) * D_ + (pbi & 1) * 32 + (pch ^ ((pbi >> 1) * 2)) * 8) * 2u;
#define RLOAD(dst, vo, base) asm volatile("global_load_dwordx4 %0, %1, %2" : "=v"(dst) : "v"(vo), "s"(uni(base)) : "memory")
#pragma unroll
    for (int i = 0; i < NR; ++i) {
      WAITV(THR - 1);
      if (i < 4) RLOAD(wr_[i], vx2[i & 1], x + (size_t)(rp * 128 + hw * 32 + (i >> 1) * 16) * D_ + cb * 64);
      else RLOAD(wr_[i], voW, uni(wo + cb * 64) + (size_t)((i + 8) ^ hrot) * (32 * D_ * 2));
    }
#pragma unroll
    for (int j = 0; j < NE; ++j) { WAITV(THR - 1); stageW(j); }
    WAITV(0);
#pragma unroll
    for (int i = 0; i < NR; ++i) asm volatile("" : "+v"(wr_[i]));
#pragma unroll
    for (int i = 0; i < NW1; ++i) wpin(i);
    asm volatile("" :: "v"(wt));
    STAMPT(1, 3, 256);
    {
      u32x4 pv[16], zq;
      const unsigned ko = WS_KVP + hp * 16 * KVB + (hw >> 1) * 2176;
      unsigned kl = ko + (hw & 1) * 1024 + lane * 16, zo = ko + ((hw & 1) * 8 + (lane >> 3)) * KVB + 2048 + (lane & 7) * 16;
      u32x4 sel[2];
#pragma unroll
      for (int e = 0; e < 2; ++e)
#pragma unroll
        for (int i = 0; i < 4; ++i)
          sel[e][i] = ((lane & 15) >> 2) != (lane >> 4) || (i >> 1) != e || (i & 1) != ((lane & 3) >> 1) ? 0u : (lane & 1) ? 0x3F800000u : 0x3F80u;
      f32x4 s[2][2];
      auto ksum = [&]() __attribute__((always_inline)) {
#pragma unroll
        for (int p = 0; p < 16; ++p) {
#pragma unroll
          for (int e = 0; e < 2; ++e) s[p >> 3][e] = mfma16(sel[e], pv[p], (p & 7) ? s[p >> 3][e] : (f32x4){0.f, 0.f, 0.f, 0.f});
          SB0();
        }
        s[0][0] += s[1][0]; s[0][1] += s[1][1];
      };
      asm volatile("" : "+v"(sel[0]), "+v"(sel[1]), "+v"(kl), "+v"(zo));
#pragma nounroll
      for (unsigned s_ = 0; s_ < SPIN_CAP; ++s_) { if (__builtin_amdgcn_readfirstlane(misc[16 + hw])) break; __builtin_amdgcn_s_sleep(1); }
      __builtin_amdgcn_s_sleep(SLA);
      zq = LDW(zo);
#pragma unroll
      for (int p = 0; p < 16; ++p) { pv[p] = LDWS(kl + (p & 7) * KVB, ((p >> 3) ^ (idx & 1)) * (8 * KVB)); SB0(); }
      ksum();
      const float chk = s[0][0][0] + __uint_as_float(zq[1]) + __uint_as_float(zq[3]);
      if (__any(chk != chk)) {
        unsigned need = 0x1FFFFu;
        STAMPT(3, 11, 256);
#pragma nounroll
        for (unsigned s_ = 0; s_ < SPIN_CAP; ++s_) {
#pragma unroll
          for (int p = 0; p < 16; ++p) if ((need & (1u << p)) && !__any(max(pv[p][1], pv[p][3]) == EMPTY)) need &= ~(1u << p);
          if (!__any(max(zq[1], zq[3]) == EMPTY)) need &= ~0x10000u;
          if (!need) break;
          asm volatile("" ::: "memory");
#pragma unroll
          for (int p = 0; p < 16; ++p) if (need & (1u << p)) pv[p] = LDWS(kl + (p & 7) * KVB, ((p >> 3) ^ (idx & 1)) * (8 * KVB));
          if (need & 0x10000u) zq = LDW(zo);
        }
        ksum();
      }
      STAMPT(1, 4, 256);
      *(u32x4*)(smem + L_KVL + hw * 1024 + lane * 16) =
          (u32x4){pack2(s[0][0][0], s[0][0][1]), pack2(s[0][0][2], s[0][0][3]), pack2(s[0][1][0], s[0][1][1]), pack2(s[0][1][2], s[0][1][3])};
      f32x4 z = __builtin_bit_cast(f32x4, zq);
#pragma unroll
      for (int i = 0; i < 4; ++i) { float v = z[i]; v += dppf<DPP_ROR8>(v); z[i] = xh_sum(x16_sum(v)); }
      if (lane < 8) *(f32x4*)(smem + L_ZL + hw * 128 + lane * 16) = z;
    }
    LBAR();
#pragma unroll
    for (int j = NE; j < 32; ++j) if (j < 12 || j >= NR + 8) { WAITV(THR3 - 1); stageW(j); }
#pragma nounroll
    for (unsigned s_ = 0; s_ < SPIN_CAP; ++s_) { if (__all(misc[24 + (lane & 3)] != 0)) break; __builtin_amdgcn_s_sleep(1); }
#pragma unroll
    for (int i = NW1; i < NW2; ++i) wload(i);
#pragma unroll
    for (int i = 0; i < NR; ++i) *(u32x4*)(smem + (i < 4 ? L_X2 + (hw * 4 + i) * 1024 : wslot(i + 8) + hw * 1024) + lane * 16) = wr_[i];
    asm volatile("s_waitcnt lgkmcnt(0)" ::: "memory");
    WAITV(0);
    STAMPT(1, 6, 256);
    if (hw == 0) {
      __builtin_amdgcn_s_sleep(SLB);
#pragma nounroll
      for (unsigned s_ = 0; s_ < SPIN_CAP; ++s_) if (__all(ld32(ws, SY_FB + rp * 256 + lane * 4) != EMPTY)) break;
    }
    BAR();
#pragma nounroll
    for (unsigned s_ = 0; s_ < SPIN_CAP; ++s_) { if (__builtin_amdgcn_readfirstlane(misc[20 + hw])) break; __builtin_amdgcn_s_sleep(2); }
#pragma unroll
    for (int i = NW2; i < 24; ++i) { WAITV(THR2 - 1); wload(i); }
    WAITV(0);
#pragma unroll
    for (int i = NW1; i < 24; ++i) wpin(i);
    STAMPT(1, 7, 256);
    const int c16 = lane & 15, kq = lane >> 4;
    u32x4 gf[2][8];
    u32x2 df[16];
    {
#pragma nounroll
      for (unsigned s_ = 0; s_ < SPIN_CAP; ++s_) { if (__all(misc[8 + (lane & 3)] != 0)) break; __builtin_amdgcn_s_sleep(1); }
      STAMPT(3, 5, 256);
#pragma unroll
      for (int i = 0; i < 8; ++i) {
        const int go = hw * 8192 + i * 1024 + ((lane ^ (((lane >> 4) & 1) * 2 + (i & 1) * 4)) * 16);
        *(u32x4*)(smem + go) = wgr[i];
        *(u32x4*)(smem + 32768 + go) = wur[i];
        *(u32x4*)(smem + L_DW + hw * 8704 + ((lane >> 5) * 8 + i) * 544 + (lane & 31) * 16) = wdr[i];
      }
      asm volatile("s_waitcnt lgkmcnt(0)" ::: "memory");
      STAMPT(3, 6, 256);
      misc[12 + hw] = 1;
#pragma nounroll
      for (unsigned s_ = 0; s_ < SPIN_CAP; ++s_) if (__all(misc[12 + (lane & 3)] != 0)) break;
      asm volatile("" ::: "memory");
      STAMPT(3, 7, 256);
      const unsigned gb = opq(lds0 + (kq * 8 + (c16 >> 2)) * 128 + (hw ^ ((c16 >> 3) + (kq & 1) * 2)) * 32 + (lane & 3) * 8);
      const unsigned db = opq(lds0 + L_DW + hw * 8704 + (kq * 4 + (c16 >> 2)) * 544 + (lane & 3) * 8);
#pragma unroll
      for (int m = 0; m < 2; ++m)
#pragma unroll
        for (int k = 0; k < 8; ++k) gf[m][k] = cat(trread(gb, m * 32768 + k * 4096), trread(gb, m * 32768 + k * 4096 + 512));
#pragma unroll
      for (int t = 0; t < 16; ++t) df[t] = trread(db, t * 32);
      SB0();
#pragma unroll
      for (int k = 0; k < 8; ++k) asm volatile("" : "+v"(gf[0][k]), "+v"(gf[1][k]), "+v"(df[2 * k]), "+v"(df[2 * k + 1]));
    }
    const unsigned gu_st = opq(c16 < 4 ? WS_GU + fs * 1024 + hw * 256 + c16 * 64 + kq * 16 : 0x80000000u);
    const unsigned gu_ld = opq(WS_GU + ((fs & ~3) + (c16 >> 2)) * 1024 + (fs & 3) * 256 + (c16 & 3) * 64 + kq * 16);
    const unsigned yp_st = opq(WS_YP + fs * 8192 + (c16 & 1) * 4096 + hw * 1024 + (c16 >> 1) * 64 + kq * 16);
    const unsigned xsb = opq(lds0 + L_XS + (c16 & 1) * 528 + kq * 16);
    STAMPT(1, 8, 256);
    BAR();
    if (has_mlp) {
      f32x4 ga[2] = {{0.f, 0.f, 0.f, 0.f}, {0.f, 0.f, 0.f, 0.f}};
      u32x4 xf[8];
#pragma unroll
      for (int k = 0; k < 8; ++k) xf[k] = ldsr(xsb, k * 64);
#pragma unroll
      for (int k = 0; k < 8; ++k) { ga[0] = mfma16(gf[0][k], xf[k], ga[0]); ga[1] = mfma16(gf[1][k], xf[k], ga[1]); }
      STW(seal(__builtin_bit_cast(u32x4, (c16 & 2) ? ga[1] : ga[0])), gu_st);
      STAMPT(1, 14, 256);
      __builtin_amdgcn_s_sleep(SLG0);
      u32x4 q;
#pragma nounroll
      for (unsigned s_ = 0; s_ < SPIN_CAP; ++s_) {
        q = LDW(gu_ld);
        if (!__any(max(q[1], q[3]) == EMPTY)) break;
        asm volatile("" ::: "memory");
        __builtin_amdgcn_s_sleep(SLG1);
      }
      STAMPT(1, 15, 256);
      float av[4];
#pragma unroll
      for (int i = 0; i < 4; ++i) {
        float v = __uint_as_float(q[i]);
        v += dppf<DPP_ROR8>(v); v += dppf<DPP_ROR4>(v);
        const float o_ = dppf<DPP_X2>(v);
        const float gs = rbf((c16 & 2) ? o_ : v), us = rbf((c16 & 2) ? v : o_);
        const float sg = rbf(gs * __builtin_amdgcn_rcpf(1.0f + __builtin_amdgcn_exp2f(gs * -1.44269504088896340736f)));
        av[i] = sg * us;
      }
      const u32x2 ab = {pack2(av[0], av[1]), pack2(av[2], av[3])};
      f32x4 y[2] = {{0.f, 0.f, 0.f, 0.f}, {0.f, 0.f, 0.f, 0.f}};
#pragma unroll
      for (int t = 0; t < 8; ++t) {
        const u32x2 bt = (c16 >> 1) == t ? ab : (u32x2){0u, 0u};
#pragma unroll
        for (int j = 0; j < 2; ++j)
          y[j] = __builtin_amdgcn_mfma_f32_16x16x16bf16_1k(__builtin_bit_cast(s16x4, df[8 * j + t]), __builtin_bit_cast(s16x4, bt), y[j], 0, 0, 0);
      }
      STW(seal(__builtin_bit_cast(u32x4, y[0])), yp_st); STW(seal(__builtin_bit_cast(u32x4, y[1])), yp_st + 512);
      STAMPT(0, 13, 256);
    }
  }
  if (bid < 64) {
    const int tk = bid >> 5, c0 = (bid & 31) * 32;
    unsigned xn = 0, xh = 0;
    if (tid < 16) {
      const unsigned xo = WS_XS + (idx & (XSR - 1)) * 524288u + (unsigned)misc[3 + tk] * 4096u + c0 * 2 + (tid >> 1) * 8;
#pragma nounroll
      for (unsigned s_ = 0; s_ < SPIN_CAP; ++s_) {
        const u64 a = ld64(ws, xo), b = ld64(ws, xo + 2048);
        xn = (tid & 1) ? (unsigned)(a >> 32) : (unsigned)a; xh = (tid & 1) ? (unsigned)(b >> 32) : (unsigned)b;
        if (__all((unsigned)(a >> 32) != EMPTY && (unsigned)(b >> 32) != EMPTY)) break;
      }
    }
    const float sg = rbf(__builtin_amdgcn_rcpf(1.0f + __builtin_amdgcn_exp2f(key_val((unsigned)misc[5 + tk]) * -1.44269504088896340736f)));
    const size_t oo = (size_t)misc[1 + tk] * D_ + c0 + tid * 2;
    const unsigned yo = opq(WS_YP + (tid >> 3) * 8192 + tk * 4096 + c0 * 4 + (tid & 7) * 16);
    BAR();
    __builtin_amdgcn_s_sleep(YSL0);
    u32x4 pv[4];
#pragma nounroll
    for (unsigned s_ = 0; s_ < SPIN_CAP; ++s_) {
      unsigned mk = 0u;
#pragma unroll
      for (int p = 0; p < 4; ++p) {
        pv[p] = (u32x4){0u, 0u, 0u, 0u};
        if (p < 3 || wave < 4) pv[p] = LDWS(yo, p * (64 * 8192));
      }
#pragma unroll
      for (int p = 0; p < 4; ++p) mk = max(max(mk, pv[p][1]), pv[p][3]);
      if (!__any(mk == EMPTY)) break;
      asm volatile("" ::: "memory");
      __builtin_amdgcn_s_sleep(YSL1);
    }
    STAMPT(0, 15, 0);
    f32x4 s = (__builtin_bit_cast(f32x4, pv[0]) + __builtin_bit_cast(f32x4, pv[1])) + (__builtin_bit_cast(f32x4, pv[2]) + __builtin_bit_cast(f32x4, pv[3]));
#pragma unroll
    for (int i = 0; i < 4; ++i) { float v = s[i]; v += dppf<DPP_ROR8>(v); s[i] = xh_sum(x16_sum(v)); }
    float* rd = (float*)(smem + L_RED);
    if (lane < 8) *(f32x4*)(rd + wave * 32 + lane * 4) = s;
    LBAR();
    if (tid < 16) {
      f32x2 y = {0.f, 0.f};
#pragma unroll
      for (int p = 0; p < 8; ++p) y += *(const f32x2*)(rd + p * 32 + tid * 2);
      const float t0 = rbf(bflo(xn) + rbf(rbf(y[0]) * sg)), t1 = rbf(bfhi(xn) + rbf(rbf(y[1]) * sg));
      *(unsigned*)(out + oo) = pack2(bflo(xh) + t0, bfhi(xh) + t1);
    }
    STAMPT(0, 14, 0);
  }
}

extern "C" void kernel_launch(void* const* d_in, const int* in_sizes, int n_in, void* d_out, int out_size,
                              void* d_ws, size_t ws_size, hipStream_t stream) {
  (void)hipFuncSetAttribute((const void*)fused, hipFuncAttributeMaxDynamicSharedMemorySize, LDS_TOTAL);
  fused<<<256, 512, LDS_TOTAL, stream>>>((const u16*)d_in[0], (const u16*)d_in[1], (const u16*)d_in[2], (const u16*)d_in[4],
                                         (const u16*)d_in[5], (const u16*)d_in[6], (const u16*)d_in[7], (u16*)d_out, (char*)d_ws);
}
```

```cpp
#include <hip/hip_runtime.h>
#include <stdint.h>

#define LAS __attribute__((address_space(3)))
#define GAS __attribute__((address_space(1)))
#define DI __device__ __forceinline__

#define XSR 1
#define GPF 5
#define GOW 14
#define NE 4
#define WPOL
#define HRM 3
#define OW0 4
#define SLC0 24
#define SLC1 8
#define SLD1 0
#define HALF 0
#define SGS 7
#define CPF2 1
#define ODRAIN 1
#define CLATE 1
#define NR 20
#define THR3 4
#define THR2 6
#define THR 6
#define NW1 10
#define SLA 8
#define SLB 0
constexpr int CODE_PER = 11;
constexpr int XSL1 = 2;
#define YSL0 16
#define YSL1 4
#define SLG0 12
#define SLG1 2
typedef unsigned short u16;
typedef unsigned long long u64;
typedef __attribute__((ext_vector_type(2))) __bf16 bf16x2;
typedef __attribute__((ext_vector_type(8))) __bf16 bf16x8;
typedef __attribute__((ext_vector_type(4))) short s16x4;
typedef __attribute__((ext_vector_type(16))) float f32x16;
typedef __attribute__((ext_vector_type(4))) float f32x4;
typedef __attribute__((ext_vector_type(2))) float f32x2;
typedef __attribute__((ext_vector_type(4))) unsigned u32x4;
typedef __attribute__((ext_vector_type(2))) unsigned u32x2;
typedef GAS unsigned gu32;
typedef GAS unsigned long long gu64;

constexpr int S_ = 2048, D_ = 1024, F_ = 3584;

constexpr unsigned SY_CH = 0, SY_GO = 1024, SY_RE = 2048, SY_FB = 4096, SY_FD = 8192;
constexpr unsigned WS_RS  = 16384;
constexpr unsigned WS_CS  = WS_RS + 256 * 1024;
constexpr unsigned KVB    = 4352;
constexpr unsigned WS_KVP = WS_CS + 256 * 768;
constexpr unsigned WS_ST2 = WS_KVP + 256 * KVB;
constexpr unsigned WS_GU  = WS_ST2 + 256 * 2048;
constexpr unsigned WS_XS  = WS_GU + 256 * 1024;
constexpr unsigned WS_YP  = WS_XS + XSR * 524288;
constexpr unsigned WS_O   = WS_YP + 224 * 8192;
constexpr unsigned WS_END = WS_O + S_ * D_ * 2;
constexpr size_t WS_DIAG  = 64u << 20;
#define STAMPT(kid, i, T) do {} while (0)

DI float bflo(unsigned u) { return __uint_as_float(u << 16); }
DI float bfhi(unsigned u) { return __uint_as_float(u & 0xffff0000u); }
DI float rbf(float x) { return (float)(__bf16)x; }
DI unsigned pack2(float a, float b) { bf16x2 c; c[0] = (__bf16)a; c[1] = (__bf16)b; return __builtin_bit_cast(unsigned, c); }
DI f32x4 mfma16(u32x4 a, u32x4 b, f32x4 c) {
  return __builtin_amdgcn_mfma_f32_16x16x32_bf16(__builtin_bit_cast(bf16x8, a), __builtin_bit_cast(bf16x8, b), c, 0, 0, 0);
}
DI f32x16 mfma32(u32x4 a, u32x4 b, f32x16 c) {
  return __builtin_amdgcn_mfma_f32_32x32x16_bf16(__builtin_bit_cast(bf16x8, a), __builtin_bit_cast(bf16x8, b), c, 0, 0, 0);
}
DI u32x2 trread(const char* p) {
  s16x4 v = __builtin_amdgcn_ds_read_tr16_b64_v4i16((LAS s16x4*)p);
  return __builtin_bit_cast(u32x2, v);
}
DI unsigned opq(unsigned v) { asm volatile("" : "+v"(v)); return v; }
DI u32x2 trread(unsigned base, int off) {
  s16x4 v = __builtin_amdgcn_ds_read_tr16_b64_v4i16((LAS s16x4*)((LAS char*)(size_t)base + off));
  return __builtin_bit_cast(u32x2, v);
}
DI u32x4 ldsr(unsigned base, int off) { return *(const LAS u32x4*)((const LAS char*)(size_t)base + off); }
DI u32x4 cat(u32x2 a, u32x2 b) { return (u32x4){a[0], a[1], b[0], b[1]}; }
DI u32x4 pick(int c, u32x4 a, u32x4 b) { const unsigned m = c ? ~0u : 0u; return (a & m) | (b & ~m); }
DI f32x4 add4(f32x4 a, f32x4 b) {
  f32x4 c;
#pragma unroll
  for (int i = 0; i < 4; ++i) asm("v_add_f32 %0, %1, %2" : "=v"(c[i]) : "v"(a[i]), "v"(b[i]));
  return c;
}
DI float dot2(unsigned a, unsigned b, float c) {
  return __builtin_amdgcn_fdot2_f32_bf16(__builtin_bit_cast(bf16x2, a), __builtin_bit_cast(bf16x2, b), c, false);
}
DI const char* uni(const void* p) {
  const u64 v = (u64)p;
  return (const char*)(((u64)(unsigned)__builtin_amdgcn_readfirstlane((int)(v >> 32)) << 32) | (unsigned)__builtin_amdgcn_readfirstlane((int)v));
}
DI void glds16(unsigned voff, const void* sbase, unsigned lds) {
  asm volatile("s_mov_b32 m0, %2\n\ts_nop 0\n\tglobal_load_lds_dwordx4 %0, %1" ::"v"(voff), "s"(uni(sbase)), "s"(__builtin_amdgcn_readfirstlane(lds)) : "memory");
}
DI float xh_sum(float v) {
  auto r = __builtin_amdgcn_permlane32_swap(__float_as_uint(v), __float_as_uint(v), false, false);
  return __uint_as_float(r[0]) + __uint_as_float(r[1]);
}
template <int CTRL> DI unsigned dppu(unsigned v) { return (unsigned)__builtin_amdgcn_update_dpp(0, (int)v, CTRL, 0xf, 0xf, true); }
template <int CTRL> DI float dppf(float v) { return __uint_as_float(dppu<CTRL>(__float_as_uint(v))); }
constexpr int DPP_X1 = 0xB1, DPP_X2 = 0x4E, DPP_HMIR = 0x141, DPP_MIR = 0x140, DPP_ROR8 = 0x128, DPP_ROR4 = 0x124;
DI float oct_sum(float v) { v += dppf<DPP_X1>(v); v += dppf<DPP_X2>(v); v += dppf<DPP_HMIR>(v); return v; }
DI float x16_sum(float v) {
  auto r = __builtin_amdgcn_permlane16_swap(__float_as_uint(v), __float_as_uint(v), false, false);
  return __uint_as_float(r[0]) + __uint_as_float(r[1]);
}
DI unsigned phi_pair(f32x2 t) {
  const f32x2 u = t + 1.000001f;
  const f32x2 l = t * 1.44269504088896340736f;
  const f32x2 e = (f32x2){__builtin_amdgcn_exp2f(l[0]), __builtin_amdgcn_exp2f(l[1])} + 1e-6f;
  return pack2(__builtin_amdgcn_fmed3f(e[0], u[0], 1.000001f), __builtin_amdgcn_fmed3f(e[1], u[1], 1.000001f));
}
#define RLX_AGENT __ATOMIC_RELAXED, __HIP_MEMORY_SCOPE_AGENT
DI void st32(char* ws, unsigned off, unsigned v) { __hip_atomic_store((gu32*)(ws + off), v, RLX_AGENT); }
DI void st64(char* ws, unsigned off, u64 v) { __hip_atomic_store((gu64*)(ws + off), v, RLX_AGENT); }
DI unsigned ld32(char* ws, unsigned off) { return __hip_atomic_load((gu32*)(ws + off), RLX_AGENT); }
DI u64 ld64(char* ws, unsigned off) { return __hip_atomic_load((gu64*)(ws + off), RLX_AGENT); }
#define WAITV(n) asm volatile("s_waitcnt vmcnt(%0)" :: "n"(n) : "memory")
#define BAR() do { asm volatile("" ::: "memory"); __builtin_amdgcn_s_barrier(); asm volatile("" ::: "memory"); } while (0)
#define LBAR() do { asm volatile("s_waitcnt lgkmcnt(0)" ::: "memory"); __builtin_amdgcn_s_barrier(); asm volatile("" ::: "memory"); } while (0)
#define SB0() __builtin_amdgcn_sched_barrier(0)
constexpr unsigned SPIN_CAP = 1u << 22;
constexpr unsigned EMPTY = 0xFFFFFFFFu;
DI u32x4 seal(u32x4 v) { v[1] = min(v[1], 0xFFFFFFFEu); v[3] = min(v[3], 0xFFFFFFFEu); return v; }
DI unsigned mk_key(float v, unsigned idx) {
  const unsigned u = __float_as_uint(v) >> 16;
  const unsigned o = (u & 0x8000u) ? (~u & 0xffffu) : (u | 0x8000u);
  return (o << 16) | (0xFFFFu - idx);
}
DI unsigned key_idx(unsigned k) { return (0xFFFFu - (k & 0xFFFFu)) & (S_ - 1); }
DI float key_val(unsigned k) { const unsigned o = k >> 16; return __uint_as_float(((o & 0x8000u) ? (o & 0x7fffu) : (~o & 0xffffu)) << 16); }
DI void top2_mix(unsigned& b0, unsigned& b1, unsigned c0, unsigned c1) {
  const unsigned lo = min(b0, c0), mx = max(b1, c1);
  b0 = max(b0, c0); b1 = max(lo, mx);
}
DI void top2_wave(unsigned& b0, unsigned& b1) {
  top2_mix(b0, b1, dppu<DPP_X1>(b0), dppu<DPP_X1>(b1));
  top2_mix(b0, b1, dppu<DPP_X2>(b0), dppu<DPP_X2>(b1));
  top2_mix(b0, b1, dppu<DPP_HMIR>(b0), dppu<DPP_HMIR>(b1));
  top2_mix(b0, b1, dppu<DPP_MIR>(b0), dppu<DPP_MIR>(b1));
  { auto p = __builtin_amdgcn_permlane16_swap(b0, b0, false, false); auto q = __builtin_amdgcn_permlane16_swap(b1, b1, false, false);
    b0 = p[0]; b1 = q[0]; top2_mix(b0, b1, p[1], q[1]); }
  { auto p = __builtin_amdgcn_permlane32_swap(b0, b0, false, false); auto q = __builtin_amdgcn_permlane32_swap(b1, b1, false, false);
    b0 = p[0]; b1 = q[0]; top2_mix(b0, b1, p[1], q[1]); }
}

constexpr int L_A = 0, L_Q = 65536, L_KV = 98304, LDS_TOTAL = 163840;
constexpr int L_M  = L_KV;
constexpr int L_SA = L_M, L_SB = L_M + 512, L_CS = L_M + 1024;
constexpr int L_MISC = L_M + 1792;
constexpr int L_XCH = L_M + 2048;
constexpr int L_KVL = L_M + 11264;
constexpr int L_ZL = L_M + 15360;
constexpr int L_XS = L_M + 3072;
constexpr int L_RED = L_M + 5120;
constexpr int L_ACT = L_M + 9216;
constexpr int L_TB = L_M + 9472;
constexpr int L_WR = L_M + 15872;
constexpr int L_DW = 114688;
constexpr int L_X2 = 81920;
DI constexpr int wslot(int j) { return j < 12 ? 114688 + j * 4096 : (j - 12) * 4096; }

__global__ __launch_bounds__(512, 2) void fused(const u16* __restrict__ x, const u16* __restrict__ wqkv,
                                                const u16* __restrict__ wo, const u16* __restrict__ w_r,
                                                const u16* __restrict__ wg, const u16* __restrict__ wu,
                                                const u16* __restrict__ wd, u16* __restrict__ out, char* __restrict__ ws) {
  extern __shared__ __attribute__((aligned(16))) char smem[];
  u64 pc0; unsigned clen;
  asm volatile("s_getpc_b64 %0\n.Lfused_pc:\n\ts_mov_b32 %1, .Lfused_end-.Lfused_pc-16" : "=s"(pc0), "=s"(clen));
  const int tid = threadIdx.x, lane = tid & 63;
  const int wave = __builtin_amdgcn_readfirstlane(tid >> 6);
  STAMPT(0, 0, 0);
  const int bid = blockIdx.x, xcd = bid & 7, idx = bid >> 3;
  const int rb = (xcd & 1) * 8 + (idx & 7);
  const int hp = (xcd >> 1) * 4 + (idx >> 3);
  const int rp = rb, cb = ((3 + (xcd >> 1) + 2 * (xcd & 1)) & 3) * 4 + (hp & 3);
  const int hrot = 4 * (cb & HRM);
  const unsigned lds0 = (unsigned)(size_t)smem;
  const __amdgpu_buffer_rsrc_t wsr = __builtin_amdgcn_make_buffer_rsrc(ws, 0, WS_END, 0x00020000);
#define LDW(off) __builtin_amdgcn_raw_buffer_load_b128(wsr, (off), 0, 16)
#define LDWS(off, so) __builtin_amdgcn_raw_buffer_load_b128(wsr, (off), (so), 16)
#define STW(v, off) __builtin_amdgcn_raw_buffer_store_b128((v), wsr, (off), 0, 16)
#define STWS(v, off, so) __builtin_amdgcn_raw_buffer_store_b128((v), wsr, (off), (so), 16)
  const int kph = 4 * (xcd >> 1) + 8 * (xcd & 1), kg_ = (hp + rb) & 3;
  auto korder = [&](int t) { return ((t + kph) & 15) ^ kg_; };
  u32x4 e0;
  {
    const char* eb = wave < 4 ? (const char*)pc0 : uni(ws);
    const unsigned eo = wave < 4 ? min((unsigned)(idx * CODE_PER + min(tid >> 4, CODE_PER - 1)) * 128u, clen) : wave == 6 && bid == 0 ? SY_RE + lane * 16 : SY_GO + bid * 4;
    asm volatile("global_load_dwordx4 %0, %1, %2 sc1" : "=v"(e0) : "v"(eo), "s"(uni(eb)) : "memory");
  }
  {
    const int r0 = 16 * wave + (lane >> 3), r1 = r0 + 8, pb_ = lane >> 4;
    const unsigned a0 = (unsigned)(r0 * D_ + ((lane & 7) ^ ((r0 >> 1) & 7)) * 8) * 2u, a1 = (unsigned)(r1 * D_ + ((lane & 7) ^ ((r1 >> 1) & 7)) * 8) * 2u;
    const unsigned b0 = (unsigned)((8 * wave + 4 * (pb_ >> 1) + ((lane >> 2) & 3)) * (3 * D_) + (pb_ & 1) * 32 + ((lane & 3) ^ ((wave & 1) * 2)) * 8) * 2u;
#pragma unroll
    for (int t = 0; t < 2; ++t) {
      const char* pa = uni(x + (size_t)rb * 128 * D_) + korder(t) * 128;
      const char* pb = uni(wqkv + hp * 64) + (size_t)korder(t) * (64 * 3 * D_ * 2);
      glds16(a0, pa, lds0 + L_A + t * 16384 + wave * 2048); glds16(a1, pa, lds0 + L_A + t * 16384 + wave * 2048 + 1024);
      const bool late = HALF && ((wave ^ rb >> 1) & 2);
      if (t == 0 && late) BAR();
      glds16(b0 + 2048, pb, lds0 + L_KV + t * 16384 + wave * 1024); glds16(b0 + 4096, pb, lds0 + L_KV + t * 16384 + 8192 + wave * 1024);
      glds16(b0, pb, lds0 + L_Q + t * 8192 + wave * 1024);
      if (t == 0 && !late) BAR();
      if (t == 0 && HALF) BAR();
    }
    SB0();
  }
  volatile LAS int* misc = (volatile LAS int*)(size_t)(lds0 + L_MISC);
  const bool has_mlp = bid < 224;
  const int fs = has_mlp ? (bid & 7) * 28 + (bid >> 3) : (bid & 7) * 28;
  const int tcs = (rb & 4) ? 4 : 3;
  const unsigned rs_o = WS_RS + (rb * 16 + korder(0)) * 1024, cs_o = WS_CS + (hp * 16 + korder(tcs)) * 768;
  const u32x4 ONES4 = {0x3F803F80u, 0x3F803F80u, 0x3F803F80u, 0x3F803F80u};

  if (wave < 4) {
    const int wr = wave >> 1, wc = wave & 1;
    const int r = lane & 15, kq = lane >> 4;
    f32x4 acc[4][6];
    {
      unsigned aO[2], bQ[2], bK[2];
#pragma unroll
      for (int k = 0; k < 2; ++k) aO[k] = opq(lds0 + L_A + (64 * wr + r) * 128 + (((4 * k + kq) ^ ((r >> 1) & 7)) * 16));
#pragma unroll
      for (int c = 0; c < 2; ++c) {
        const unsigned o_ = lds0 + kq * 1024 + wc * 256 + (r >> 2) * 64 + ((c ^ (kq & 1)) * 32) + (lane & 3) * 8;
        bQ[c] = opq(o_ + L_Q); bK[c] = opq(o_ + L_KV);
      }
#pragma unroll
      for (int i = 0; i < 4; ++i)
#pragma unroll
        for (int j = 0; j < 6; ++j) acc[i][j] = (f32x4){0.f, 0.f, 0.f, 0.f};
      struct Fr { u32x4 a[4]; u32x2 bl[6], bh[6]; };
      Fr F[2];
      auto bread = [&](int f, int b, int k, int hi) {
        return f < 2 ? trread(bQ[f & 1], k * 4096 + b * 8192 + hi * 512) : trread(bK[f & 1], k * 4096 + b * 16384 + ((f >> 1) - 1) * 8192 + hi * 512);
      };
      auto step = [&](Fr& C, Fr& N, int b, int k, int MC, int ML) {
        int ld = 0;
        constexpr int ORD[6] = {2, 3, 4, 5, 0, 1};
        auto load1 = [&]() {
          if (ML == 0) return;
          if (ld < 4) N.a[ld] = ldsr(aO[k], b * 16384 + ld * 2048);
          else {
            const int e = ld - 4, f = ML == 1 ? (e >> 1) : ORD[(e >> 1) % 6], nf = ML == 1 ? 2 : ML == 2 ? 4 : 6;
            if ((e >> 1) < nf) { if (e & 1) N.bh[f] = bread(f, b, k, 1); else N.bl[f] = bread(f, b, k, 0); }
          }
          ++ld;
        };
#pragma unroll
        for (int m = 0; m < 24; ++m) {
          const int j = ORD[m >> 2], i = m & 3;
          if (!((j < 2 ? 1 : 2) & MC)) continue;
          if (j < 2) acc[i][j] = mfma16(cat(C.bl[j], C.bh[j]), C.a[i], acc[i][j]);
          else acc[i][j] = mfma16(C.a[i], cat(C.bl[j], C.bh[j]), acc[i][j]);
          load1();
          SB0();
        }
      };
      WAITV(5); BAR();
      STAMPT(0, 1, 0);
#pragma unroll
      for (int m = 0; m < 4; ++m) F[0].a[m] = ldsr(aO[0], m * 2048);
#pragma unroll
      for (int f = 0; f < 6; ++f) { F[0].bl[f] = bread(f, 0, 0, 0); F[0].bh[f] = bread(f, 0, 0, 1); }
      for (int t4 = 0; t4 < 12; t4 += 4) {
#pragma unroll
        for (int b = 0; b < 4; ++b) {
          step(F[0], F[1], b, 1, 3, 3);
          if (b == 0 && t4 == 0) { asm volatile("s_waitcnt vmcnt(0)" :: "v"(e0) : "memory"); STAMPT(3, 13, 0); BAR(); STAMPT(3, 14, 0); }
          step(F[1], F[0], (b + 1) & 3, 0, 3, 3);
          BAR();
        }
      }
#pragma unroll
      for (int b = 0; b < 4; ++b) {
        step(F[0], F[1], b, 1, 2, 2);
        step(F[1], F[0], (b + 1) & 3, 0, 2, b < 3 ? 2 : 0);
        if (b < 2) BAR();
      }
      misc[28 + wave] = 1;
#pragma nounroll
      for (unsigned s_ = 0; s_ < SPIN_CAP; ++s_) if (__builtin_amdgcn_readfirstlane(misc[7])) break;
      asm volatile("" ::: "memory");
      STAMPT(0, 2, 0);
      {
        const float* sa = (const float*)(smem + L_SA) + 64 * wr + 4 * kq;
        const float* cs = (const float*)(smem + L_CS) + wc * 96 + r;
        const float c4[4] = {cs[32], cs[48], cs[64], cs[80]};
        unsigned kp[4][2][2], vp[4][2][2];
#pragma unroll
        for (int i = 0; i < 4; ++i) {
          const f32x4 a4 = *(const f32x4*)(sa + 16 * i), b4 = *(const f32x4*)(sa + 128 + 16 * i);
          const f32x2 aa[2] = {{a4[0], a4[1]}, {a4[2], a4[3]}}, bb[2] = {{b4[0], b4[1]}, {b4[2], b4[3]}};
#pragma unroll
          for (int j = 0; j < 2; ++j)
#pragma unroll
            for (int e = 0; e < 2; ++e) {
              const f32x2 tk = (f32x2){acc[i][2 + j][2 * e], acc[i][2 + j][2 * e + 1]} * aa[e] + bb[e] * c4[j];
              const f32x2 tv = (f32x2){acc[i][4 + j][2 * e], acc[i][4 + j][2 * e + 1]} * aa[e] + bb[e] * c4[2 + j];
              kp[i][j][e] = phi_pair(tk);
              vp[i][j][e] = pack2(tv[0], tv[1]);
            }
        }
        f32x4 kv[2][2], zv[2];
#pragma unroll
        for (int d = 0; d < 2; ++d) {
          zv[d] = (f32x4){0.f, 0.f, 0.f, 0.f}; kv[d][0] = zv[d]; kv[d][1] = zv[d];
#pragma unroll
          for (int u = 0; u < 2; ++u) {
            const u32x4 ka = {kp[2 * u][d][0], kp[2 * u][d][1], kp[2 * u + 1][d][0], kp[2 * u + 1][d][1]};
#pragma unroll
            for (int e = 0; e < 2; ++e)
              kv[d][e] = mfma16(ka, (u32x4){vp[2 * u][e][0], vp[2 * u][e][1], vp[2 * u + 1][e][0], vp[2 * u + 1][e][1]}, kv[d][e]);
            zv[d] = mfma16(ka, ONES4, zv[d]);
          }
        }
        char* xo = smem + L_XCH + wave * 2048 + lane * 16;
        *(f32x4*)xo = wr ? kv[0][0] : kv[0][1];
        *(f32x4*)(xo + 1024) = wr ? kv[1][0] : kv[1][1];
        if (r < 2) *(f32x4*)(smem + L_XCH + 8192 + wave * 128 + r * 64 + kq * 16) = r ? zv[1] : zv[0];
#pragma unroll
        for (int m = 0; m < 4; ++m) F[0].a[m] = ldsr(aO[0], m * 2048);
#pragma unroll
        for (int f = 0; f < 2; ++f) { F[0].bl[f] = bread(f, 0, 0, 0); F[0].bh[f] = bread(f, 0, 0, 1); }
        STAMPT(1, 0, 0);
        asm volatile("s_waitcnt lgkmcnt(0)" ::: "memory");
        misc[16 + wave] = 1;
#pragma nounroll
        for (unsigned s_ = 0; s_ < SPIN_CAP; ++s_) if (__builtin_amdgcn_readfirstlane(misc[16 + (wave ^ 2)])) break;
        asm volatile("" ::: "memory");
        const char* xi = smem + L_XCH + (wave ^ 2) * 2048 + lane * 16;
        const f32x4 s0 = *(const f32x4*)xi + (wr ? kv[0][1] : kv[0][0]), s1 = *(const f32x4*)(xi + 1024) + (wr ? kv[1][1] : kv[1][0]);
        const unsigned ko = WS_KVP + (hp * 16 + rb) * KVB + wc * 2176;
        STW(seal((u32x4){pack2(s0[0], s0[1]), pack2(s0[2], s0[3]), pack2(s1[0], s1[1]), pack2(s1[2], s1[3])}), ko + wr * 1024 + lane * 16);
        if (wr == 0 && lane < 8) {
          const f32x4 z = *(const f32x4*)(smem + L_XCH + 8192 + wave * 128 + lane * 16) + *(const f32x4*)(smem + L_XCH + 8192 + (wave + 2) * 128 + lane * 16);
          STW(seal(__builtin_bit_cast(u32x4, z)), ko + 2048 + lane * 16);
        }
                STAMPT(0, 3, 0);
      }
#pragma unroll
      for (int b = 0; b < 4; ++b) {
        step(F[0], F[1], b, 1, 1, 1);
        step(F[1], F[0], (b + 1) & 3, 0, 1, b < 3 ? 1 : 0);
      }
    }
    u32x4 qp[4];
    {
      const float* sa = (const float*)(smem + L_SA) + 64 * wr + r;
      const float* cs = (const float*)(smem + L_CS) + wc * 96 + 4 * kq;
      const f32x4 c0 = *(const f32x4*)cs, c1 = *(const f32x4*)(cs + 16);
#pragma unroll
      for (int i = 0; i < 4; ++i) {
        const float a = sa[16 * i], b = sa[128 + 16 * i];
        qp[i][0] = phi_pair((f32x2){acc[i][0][0], acc[i][0][1]} * a + (f32x2){c0[0], c0[1]} * b);
        qp[i][1] = phi_pair((f32x2){acc[i][0][2], acc[i][0][3]} * a + (f32x2){c0[2], c0[3]} * b);
        qp[i][2] = phi_pair((f32x2){acc[i][1][0], acc[i][1][1]} * a + (f32x2){c1[0], c1[1]} * b);
        qp[i][3] = phi_pair((f32x2){acc[i][1][2], acc[i][1][3]} * a + (f32x2){c1[2], c1[3]} * b);
        asm volatile("" : "+v"(qp[i]));
      }
    }
    u32x2 xr[8];
    {
      const int row = wave * 32 + (lane & 31);
#pragma unroll
      for (int c = 0; c < 8; ++c) xr[c] = *(const u32x2*)(smem + L_A + ((cb & 3) ^ kg_) * 16384 + row * 128 + ((c ^ ((row >> 1) & 7)) * 16) + (lane >> 5) * 8);
    }
    STAMPT(0, 4, 0);
    LBAR();
    STAMPT(0, 5, 0);
    {
      const u32x4 k0 = *(const u32x4*)(smem + L_KVL + wc * 2048 + lane * 16), k1 = *(const u32x4*)(smem + L_KVL + wc * 2048 + 1024 + lane * 16);
      const float* zl = (const float*)(smem + L_ZL) + wc * 64 + kq * 4;
      const f32x4 za = *(const f32x4*)zl + *(const f32x4*)(zl + 32), zb = *(const f32x4*)(zl + 16) + *(const f32x4*)(zl + 48);
      const u32x4 zf = {pack2(za[0], za[1]), pack2(za[2], za[3]), pack2(zb[0], zb[1]), pack2(zb[2], zb[3])};
      const f32x4 Z4 = {0.f, 0.f, 0.f, 0.f};
      const unsigned oo = WS_O + ((rb * 8 + wr * 4) * 32 + 2 * hp + wc) * 1024 + lane * 16;
#pragma unroll
      for (int i = 0; i < 4; ++i) {
        const f32x4 dn = mfma16(zf, qp[i], Z4), x0 = mfma16(k0, qp[i], Z4), x1 = mfma16(k1, qp[i], Z4);
        const float ri = __builtin_amdgcn_rcpf(dn[0] + 1e-6f);
        STW(seal((u32x4){pack2(x0[0] * ri, x0[1] * ri), pack2(x0[2] * ri, x0[3] * ri), pack2(x1[0] * ri, x1[1] * ri), pack2(x1[2] * ri, x1[3] * ri)}),
            oo + i * 32768);
      }
      if (ODRAIN) { misc[24 + wave] = 1; WAITV(0); }
      if (lane == 0) st32(ws, SY_FB + rb * 256 + hp * 16 + wave * 4, 1u);
      misc[24 + wave] = 1;
    }
    STAMPT(0, 6, 0);
    {
      const int h = lane >> 5, s32 = lane & 31;
      u32x2 wv[8];
      auto wvld = [&]() {
#pragma unroll
        for (int c = 0; c < 8; ++c) wv[c] = *(const volatile LAS u32x2*)(size_t)(lds0 + L_WR + c * 16 + h * 8);
      };
      wvld();
      u32x4 of[32][2];
      const unsigned ol = WS_O + (rp * 8 + wave * 2 + (kq & 1)) * 32768 + (h * 16 + r) * 16;
      auto oload1 = [&](int j, int e) { of[j][e] = LDWS(ol + e * 512 + (j & 3) * 1024, ((j & ~3) ^ hrot) * 1024); };
      auto oload = [&](int j) { oload1(j, 0); oload1(j, 1); };
      auto omark = [&](int j) {
        unsigned mx = 0;
#pragma unroll
        for (int e = 0; e < 4; ++e) mx = max(max(mx, max(of[j + e][0][1], of[j + e][0][3])), max(of[j + e][1][1], of[j + e][1][3]));
        return mx;
      };
      f32x16 a2[4];
      unsigned wO[3];
#pragma unroll
      for (int e = 0; e < 3; ++e) wO[e] = opq(lds0 + e * 65536 + h * 512 + (r >> 2) * 64 + (((kq & 1) ^ h) * 32) + (lane & 3) * 8);
      constexpr int PF = GPF, OW = GOW;
      u32x2 wl[8], wh[8];
      auto wld = [&](int g) {
        const int j = g >> 2;
        const int o_ = (wslot(j) & 65535) + ((g >> 1) & 1) * 1024 + (g & 1) * 256;
        wl[g & 7] = trread(wO[wslot(j) >> 16], o_); wh[g & 7] = trread(wO[wslot(j) >> 16], o_ + 2048);
      };
      const int row = wave * 32 + s32;
      const unsigned xb = opq(lds0 + L_X2 + row * 128 + h * 8);
      const int sw = (row >> 1) & 7;
      float sw_ = 0.f;
#pragma unroll
      for (int c = 0; c < 16; ++c) sw_ = dot2(wv[c >> 1][c & 1], 0x3F803F80u, sw_);
      sw_ = xh_sum(sw_);
      asm volatile("" : "+v"(sw_));
      BAR();
      STAMPT(0, 7, 0);
#pragma unroll
      for (int j = 0; j < OW0; ++j) { oload(j); SB0(); }
#pragma unroll
      for (int c = 0; c < 8; ++c) {
        const u32x2 xx = xr[c];
#pragma unroll
        for (int i = 0; i < 2; ++i) {
          a2[c >> 2][(c & 3) * 4 + 2 * i] = bflo(xx[i]); a2[c >> 2][(c & 3) * 4 + 2 * i + 1] = bfhi(xx[i]);
          a2[2 + (c >> 2)][(c & 3) * 4 + 2 * i] = 0.f; a2[2 + (c >> 2)][(c & 3) * 4 + 2 * i + 1] = 0.f;
        }
      }
#pragma unroll
      for (int g = 0; g < PF; ++g) wld(g);
      SB0();
      unsigned mx = ODRAIN ? 0u : omark(0);
#pragma unroll
      for (int j = 0; j < 32; ++j) {
        if (j == 30) wvld();
        if (!ODRAIN && (j & 3) == 0 && __builtin_expect(__any(mx == EMPTY), 0)) {
#pragma nounroll
          for (unsigned s_ = 0; s_ < SPIN_CAP; ++s_) {
            asm volatile("" ::: "memory");
            STAMPT(3, 12, 0);
            oload(j); oload(j + 1); oload(j + 2); oload(j + 3);
            if (!__any(omark(j) == EMPTY)) break;
          }
        }
#pragma unroll
        for (int n = 0; n < 4; ++n) {
          const int g = j * 4 + n;
          a2[n] = mfma32(cat(wl[g & 7], wh[g & 7]), of[j][n >> 1], a2[n]);
          SB0();
          if (g + PF < 128) wld(g + PF);
          {
            constexpr int RAMP = OW - OW0;
            const int q = j < RAMP ? 2 * OW0 + 4 * j + n : (n & 1) ? -1 : 2 * (j + OW) + (n >> 1);
            if (q >= 0 && q < 64) oload1(q >> 1, q & 1);
          }
          if (n == 0 && j + OW == 32) misc[20 + wave] = 1;
          if (!ODRAIN && (j & 2) && j < 28) {
            const int f = (j & 1) * 4 + n, jj = (j & ~3) + 4 + (f >> 1);
            if (f == 0) mx = 0;
            mx = max(max(mx, of[jj][f & 1][1]), of[jj][f & 1][3]);
          }
          SB0();
          asm volatile("" :: "v"(wl[g & 7]), "v"(wh[g & 7]), "v"(of[j][n >> 1]));
          SB0();
        }
      }
      STAMPT(0, 8, 0); STAMPT(3, 1, 64); STAMPT(3, 2, 128); STAMPT(3, 3, 192);
      a2[0] += a2[2]; a2[1] += a2[3];
      misc[8 + wave] = 1;
      unsigned hb[16];
      {
        float s[2] = {0.f, 0.f}, sq[2] = {0.f, 0.f}, pw[2] = {0.f, 0.f};
#pragma unroll
        for (int i = 0; i < 16; ++i) {
          const unsigned t = pack2(a2[i >> 3][(i & 7) * 2], a2[i >> 3][(i & 7) * 2 + 1]);
          hb[i] = t;
          s[i & 1] = dot2(t, 0x3F803F80u, s[i & 1]); sq[i & 1] = dot2(t, t, sq[i & 1]); pw[i & 1] = dot2(t, wv[i >> 1][i & 1], pw[i & 1]);
        }
        const u32x4 st = {__float_as_uint(xh_sum(s[0] + s[1])), __float_as_uint(xh_sum(sq[0] + sq[1])), __float_as_uint(xh_sum(pw[0] + pw[1])), __float_as_uint(sw_)};
        if (h == 0) STW(seal(st), WS_ST2 + ((rp * 16 + cb) * 128 + row) * 16);
      }
      STAMPT(0, 9, 0);
      u32x4 pu_[8];
      __builtin_amdgcn_s_sleep(SLC0);
#pragma nounroll
      for (unsigned s_ = 0; s_ < SPIN_CAP; ++s_) {
        unsigned mk = 0u;
#pragma unroll
        for (int j = 0; j < 8; ++j) { pu_[j] = LDW(WS_ST2 + ((rp * 16 + h * 8 + j) * 128 + row) * 16); mk = max(max(mk, pu_[j][1]), pu_[j][3]); }
        if (!__any(mk == EMPTY)) break;
        asm volatile("" ::: "memory");
        __builtin_amdgcn_s_sleep(SLC1);
      }
      STAMPT(0, 10, 0);
      f32x4 tt = {0.f, 0.f, 0.f, 0.f};
#pragma unroll
      for (int j = 0; j < 8; ++j) tt += __builtin_bit_cast(f32x4, pu_[j]);
      const float mean = xh_sum(tt[0]) * (1.0f / 1024.0f);
      const float var = fmaxf(xh_sum(tt[1]) * (1.0f / 1024.0f) - mean * mean, 0.f);
      const float mb = rbf(mean), rs = __builtin_amdgcn_rcpf(rbf(__builtin_amdgcn_sqrtf(rbf(rbf(var) + 1e-5f))));
      float sc = rbf((xh_sum(tt[2]) - mb * xh_sum(tt[3])) * rs);
      if (!(sc == sc)) sc = -INFINITY;
      unsigned b0 = h ? 0u : mk_key(sc, rp * 128 + row), b1 = 0;
      top2_wave(b0, b1);
      const int pan = rp * 4 + wave;
      if (lane == 0) st64(ws, SY_FD + cb * 512 + pan * 8, ((u64)b1 << 32) | b0);
      STAMPT(1, 11, 0);
      unsigned nq[16];
#pragma unroll
      for (int i = 0; i < 16; ++i) {
        const unsigned dq = pack2(bflo(hb[i]) - mb, bfhi(hb[i]) - mb);
        nq[i] = pack2(bflo(dq) * rs, bfhi(dq) * rs);
      }
      {
        const int c0 = key_idx(b0) & 31, c1 = key_idx(b1) & 31;
        char* tb = smem + L_TB + wave * 512;
        if (s32 == c0 || s32 == c1) {
          char* tp = tb + (s32 == c0 ? 0 : 256) + h * 8;
#pragma unroll
          for (int c = 0; c < 8; ++c) { *(u32x2*)(tp + c * 16) = (u32x2){nq[2 * c], nq[2 * c + 1]}; *(u32x2*)(tp + 128 + c * 16) = (u32x2){hb[2 * c], hb[2 * c + 1]}; }
        }
        __builtin_amdgcn_wave_barrier();
        const u32x4 v4 = seal(*(const u32x4*)(tb + s32 * 16));
#pragma unroll
        for (int rr_ = 0; rr_ < XSR; rr_ += 2)
          if (rr_ + h < XSR) STW(v4, WS_XS + (rr_ + h) * 524288 + (pan * 2 + (s32 >> 4)) * 4096 + ((s32 >> 3) & 1) * 2048 + cb * 128 + (s32 & 7) * 16);
      }
      u64 cc = 0;
      if (wave == 0) cc = ld64(ws, SY_FD + cb * 512 + lane * 8);
#pragma unroll
      for (int c = 0; c < 8; ++c) {
        u32x2 ob;
#pragma unroll
        for (int i = 0; i < 2; ++i) ob[i] = pack2(bflo(hb[2 * c + i]) + bflo(nq[2 * c + i]), bfhi(hb[2 * c + i]) + bfhi(nq[2 * c + i]));
        *(LAS u32x2*)((LAS char*)(size_t)(xb + ((c ^ sw) * 16))) = ob;
      }
      if (wave == 0) {
#pragma nounroll
        for (unsigned s_ = 0; s_ < SPIN_CAP; ++s_) {
          if (__all((unsigned)(cc >> 32) != EMPTY)) break;
          __builtin_amdgcn_s_sleep(SLD1);
          cc = ld64(ws, SY_FD + cb * 512 + lane * 8);
        }
        STAMPT(1, 13, 0);
        unsigned c0 = (unsigned)cc, c1 = (unsigned)(cc >> 32);
        top2_wave(c0, c1);
        const unsigned w0 = key_idx(c0), w1 = key_idx(c1);
        const unsigned k0 = __builtin_amdgcn_readlane((unsigned)cc, w0 >> 5), k1 = __builtin_amdgcn_readlane((unsigned)cc, w1 >> 5);
        const unsigned sl0 = (w0 >> 5) * 2 + (k0 == c0 ? 0 : 1), sl1 = (w1 >> 5) * 2 + (k1 == c1 ? 0 : 1);
        if (lane == 0) { misc[1] = w0; misc[2] = w1; misc[3] = sl0; misc[4] = sl1; misc[5] = c0; misc[6] = c1; }
        if (has_mlp) {
          const unsigned xo = WS_XS + (idx & (XSR - 1)) * 524288u + (h ? sl1 : sl0) * 4096u + (fs & 3) * 512 + s32 * 16;
          u32x4 q;
#pragma nounroll
          for (unsigned s_ = 0; s_ < SPIN_CAP; ++s_) {
            q = LDW(xo);
            if (__all(max(q[1], q[3]) != EMPTY)) break;
            asm volatile("" ::: "memory");
            __builtin_amdgcn_s_sleep(XSL1);
          }
          *(u32x4*)(smem + L_XS + h * 528 + s32 * 16) = q;
        }
      }
      LBAR();
      STAMPT(0, 11, 0);
      const int sel0 = misc[1], sel1 = misc[2];
#pragma unroll
      for (int m = 0; m < 4; ++m) {
        const int rw = wave * 32 + m * 8 + (lane >> 3), grow = rp * 128 + rw;
        const u32x4 v = *(const u32x4*)(smem + L_X2 + rw * 128 + (((lane & 7) ^ ((rw >> 1) & 7)) * 16));
        if (grow != sel0 && grow != sel1) *(u32x4*)(out + (size_t)grow * D_ + cb * 64 + (lane & 7) * 8) = v;
      }
      STAMPT(0, 12, 0);
    }
  } else {
    __builtin_amdgcn_s_setprio(2);
    const int hw = wave - 4, mt = tid & 255;
    u32x4 wgr[8], wur[8], wdr[8];
    const int krow = hw * 64 + (lane >> 3);
    const unsigned gof = opq((unsigned)(((fs & 3) * 256 + krow) * F_ + (fs >> 2) * 64 + (lane & 7) * 8) * 2u);
    const unsigned dof = opq((unsigned)((fs * 16 + (lane >> 5) * 8) * D_ + (hw * 32 + (lane & 31)) * 8) * 2u);
    const unsigned wto = opq((unsigned)((idx * 32 + (mt >> 3)) * 2048 + (cb >> 2) * 512 + ((mt >> 1) & 3) * 128));
#define WLOAD(dst, vo, base) asm volatile("global_load_dwordx4 %0, %1, %2" WPOL : "=v"(dst) : "v"(vo), "s"(uni(base)) : "memory")
    auto wload = [&](int i) {
      if (i < 8) WLOAD(wgr[i], gof, wg + i * (8 * F_));
      else if (i < 16) WLOAD(wur[i - 8], gof, wu + (i - 8) * (8 * F_));
      else WLOAD(wdr[i - 16], dof, wd + (i - 16) * D_);
    };
    auto wpin = [&](int i) {
      if (i < 8) asm volatile("" : "+v"(wgr[i])); else if (i < 16) asm volatile("" : "+v"(wur[i - 8])); else asm volatile("" : "+v"(wdr[i - 16]));
    };
    const int pbi = lane >> 4, pq = (lane >> 2) & 3, pch = lane & 3;
    const unsigned voW = opq((unsigned)((8 * hw + 4 * (pbi >> 1) + pq) * D_ + (pbi & 1) * 32 + (pch ^ ((pbi >> 1) * 2)) * 8) * 2u);
    auto stageW = [&](int j) { glds16(voW, uni(wo + cb * 64) + (size_t)(j ^ hrot) * (32 * D_ * 2), lds0 + wslot(j) + hw * 1024); };
    u32x4 e4 = {EMPTY, EMPTY, EMPTY, EMPTY};
    asm volatile("" : "+v"(e4));
    const unsigned l16 = opq(lane * 16);
    if (hw < 2) {
      const char* baseA = uni(x + (size_t)rb * 128 * D_);
      const char* baseB = uni(wqkv + hp * 64);
      unsigned voA[2], voB[2];
      {
        const int r0 = 64 * hw + (lane >> 3), r1 = r0 + 8;
        voA[0] = (unsigned)(r0 * D_ + ((lane & 7) ^ ((r0 >> 1) & 7)) * 8) * 2u;
        voA[1] = (unsigned)(r1 * D_ + ((lane & 7) ^ ((r1 >> 1) & 7)) * 8) * 2u;
        voB[0] = (unsigned)((32 * hw + 4 * (pbi >> 1) + pq) * (3 * D_) + (pbi & 1) * 32 + pch * 8) * 2u;
        voB[1] = (unsigned)((32 * hw + 8 + 4 * (pbi >> 1) + pq) * (3 * D_) + (pbi & 1) * 32 + (pch ^ 2) * 8) * 2u;
      }
      auto stage = [&](int t) {
        const int kt = korder(t), b = t & 3;
        const char* pa = baseA + kt * 128;
        const char* pb = baseB + (size_t)kt * (64 * 3 * D_ * 2);
#pragma unroll
        for (int j = 0; j < 8; ++j) glds16(voA[j & 1] + (j >> 1) * (16 * D_ * 2), pa, lds0 + L_A + b * 16384 + (8 * hw + j) * 1024);
#pragma unroll
        for (int j0 = 0; j0 < 4; ++j0) {
          const int j = j0 ^ (HALF ? (rb >> 1) & 2 : 0);
          const unsigned vo = voB[j0 & 1] + (j >> 1) * (16 * 3 * D_ * 2);
          glds16(vo + 2048, pb, lds0 + L_KV + b * 16384 + (4 * hw + j) * 1024);
          glds16(vo + 4096, pb, lds0 + L_KV + b * 16384 + 8192 + (4 * hw + j) * 1024);
          glds16(vo, pb, lds0 + L_Q + b * 8192 + (4 * hw + j) * 1024);
        }
      };
      WAITV(5); BAR();
      stage(2); stage(3);
      WAITV(40); BAR();
      for (int t = 0; t < 12; ++t) { WAITV(20); BAR(); stage(t + 4); }
      WAITV(20); BAR();
      asm volatile("s_waitcnt vmcnt(0)" :: "v"(e0) : "memory");
      BAR();
    } else {
      const int aw = hw - 2, at = tid & 127;
      const bool master = bid == 0 && aw == 0;
      unsigned hs_cur = 0, hs_g0 = 0;
      u32x4 ms_exp = {0, 0, 0, 0}, pm = {0, 0, 0, 0};
      const unsigned p_off = opq(master ? SY_RE + lane * 16 : (lane == 1 ? SY_GO : SY_CH) + bid * 4);
      const char* wsu = uni(ws);
#define HS_PROBE() asm volatile("global_load_dwordx4 %0, %1, %2 sc1" : "+v"(pm) : "v"(p_off), "s"(wsu) : "memory")
#define HS_WAIT() asm volatile("s_waitcnt vmcnt(0)" : "+v"(pm) :: "memory")
      bool done = false;
      auto hs_check = [&]() __attribute__((always_inline)) {
        HS_WAIT();
        if (!master) {
          const unsigned c = __builtin_amdgcn_readlane(pm[0], 0), go = __builtin_amdgcn_readlane(pm[0], 1);
          if (go != hs_g0) done = true;
          else if (c != hs_cur) { hs_cur = c; st32(ws, SY_RE + bid * 4, c); }
        } else if (!done && __all(pm[0] == ms_exp[0] && pm[1] == ms_exp[1] && pm[2] == ms_exp[2] && pm[3] == ms_exp[3])) {
          STW(ms_exp, SY_GO + lane * 16);
          done = true;
        }
      };
      const bool prober = master || aw == 1;
      const int kq = lane >> 4, r = lane & 15;
      constexpr unsigned OOB = 0x80000000u;
      const unsigned ko = WS_KVP + (hp * 16 + rb) * KVB + aw * 2176;
      const unsigned vm0 = opq(lane < 32 ? rs_o + aw * 512 + lane * 16 : lane < 56 ? cs_o + aw * 384 + (lane - 32) * 16 : ko + 2048 + (lane - 56) * 16);
      WAITV(5); BAR();
      u32x4 g0;
      if (master) g0 = LDW(SY_GO + lane * 16);
      u32x2 rst[4];
      {
        u32x4 a[4][2];
#pragma unroll
        for (int i = 0; i < 4; ++i)
#pragma unroll
          for (int k = 0; k < 2; ++k) a[i][k] = *(const u32x4*)(smem + L_A + (64 * aw + 16 * i + r) * 128 + (((4 * k + kq) ^ ((r >> 1) & 7)) * 16));
        const f32x4 Z4 = {0.f, 0.f, 0.f, 0.f};
        const int l3 = lane & 3;
#pragma unroll
        for (int i = 0; i < 4; ++i) {
          const f32x4 s1 = mfma16(ONES4, a[i][1], mfma16(ONES4, a[i][0], Z4)), s2 = mfma16(a[i][1], a[i][1], mfma16(a[i][0], a[i][0], Z4));
          const float q = l3 == 0 ? s2[0] : l3 == 1 ? s2[1] : l3 == 2 ? s2[2] : s2[3];
          rst[i] = (u32x2){min(__float_as_uint(s1[0]), 0xFFFFFFFEu), min(__float_as_uint(q), 0xFFFFFFFEu)};
          asm volatile("" : "+v"(rst[i]));
        }
      }
      const unsigned vm1 = opq(aw ? (lane < 2 ? SY_FD + cb * 512 + rp * 32 + lane * 16 : lane == 2 ? SY_FB + rb * 256 + hp * 16 : OOB)
                                  : (has_mlp ? WS_GU + fs * 1024 + lane * 16 : OOB));
      const unsigned vrs = opq((r >> 2) == kq ? rs_o + (64 * aw + r) * 8 : OOB);
      const unsigned vc0 = opq(cs_o + aw * 384 + lane * 4), vc1 = opq(lane < 32 ? cs_o + aw * 384 + 256 + lane * 4 : OOB);
      const unsigned sg_off = opq(aw ? WS_CS + hp * (16 * 768) + min(lane, 47) * 16 : WS_RS + rb * 16384 + lane * 16), sg_st = aw ? 768 : 1024;
      unsigned cb_[2][2];
#pragma unroll
      for (int c = 0; c < 2; ++c) {
        const unsigned o_ = lds0 + kq * 1024 + aw * 256 + (r >> 2) * 64 + ((c ^ (kq & 1)) * 32) + (lane & 3) * 8;
        cb_[0][c] = opq(o_ + L_Q + (tcs & 3) * 8192); cb_[1][c] = opq(o_ + L_KV + (tcs & 3) * 16384);
      }
      const int hq = (3 - (int)((size_t)wqkv >> 7) - (hp & ~3)) & 7;
      const int qln = ((((hp & 3) - hq - 1) & 3) * 8 + (idx & 7)) * 96 + min(at, 95);
      const unsigned vq = opq((unsigned)((((kph + 4) * 64 + qln / 3) & 1023) * 6144 + (qln % 3) * 2048 + ((hp & ~3) + (hq & 3)) * 128));
      const unsigned vcp = opq(min((unsigned)(idx * CODE_PER + min(at >> 3, CODE_PER - 1)) * 128u, clen));
      u32x4 e1;
      asm volatile("s_waitcnt vmcnt(0)\n\tv_mov_b32 %0, %4\n\tv_mov_b32 %1, %5\n\tv_mov_b32 %2, %6\n\tv_mov_b32 %3, %7"
                   : "=&v"(e1[0]), "=&v"(e1[1]), "=&v"(e1[2]), "=&v"(e1[3]) : "v"(e0[0]), "v"(e0[1]), "v"(e0[2]), "v"(e0[3]) : "memory");
      hs_g0 = __builtin_amdgcn_readfirstlane(e1[0]);
      BAR();
      if (master) {
#pragma unroll
        for (int j = 0; j < 4; ++j) { const unsigned c = e1[j] + 1u; ms_exp[j] = (c == g0[j]) ? c + 1u : c; }
        STW(ms_exp, SY_CH + lane * 16);
      }
      STW(e4, vm0); STW(e4, vm1); STWS(e4, l16, ko); STWS(e4, l16, ko + 1024);
      unsigned qt = 0;
      if (hq < 4 && (hp & 3) != hq) asm volatile("global_load_dword %0, %1, %2" : "=v"(qt) : "v"(vq), "s"(uni(wqkv)) : "memory");
      BAR();
      STAMPT(2, 0, 384);
      BAR();
      STAMPT(2, 1, 384);
      if (aw == 1) HS_PROBE();
      HS_WAIT();
      asm volatile("" :: "v"(qt));
      BAR();
      STAMPT(2, 2, 384);
      if (aw == 1) { hs_cur = __builtin_amdgcn_readlane(pm[0], 0); st32(ws, SY_RE + bid * 4, hs_cur); }
#pragma unroll
      for (int i = 0; i < 4; ++i) __builtin_amdgcn_raw_buffer_store_b64(rst[i], wsr, vrs + i * 128, 0, 16);
      unsigned cv0, cv1;
      {
        f32x4 cacc[6];
#pragma unroll
        for (int f = 0; f < 6; ++f) {
          cacc[f] = (f32x4){0.f, 0.f, 0.f, 0.f};
#pragma unroll
          for (int k = 0; k < 2; ++k) {
            const int o_ = (f >= 4 ? 8192 : 0) + k * 4096;
            cacc[f] = mfma16(ONES4, cat(trread(cb_[f >= 2][f & 1], o_), trread(cb_[f >= 2][f & 1], o_ + 512)), cacc[f]);
          }
        }
        cv0 = min(__float_as_uint(kq == 0 ? cacc[0][0] : kq == 1 ? cacc[1][0] : kq == 2 ? cacc[2][0] : cacc[3][0]), 0xFFFFFFFEu);
        cv1 = min(__float_as_uint(kq == 0 ? cacc[4][0] : cacc[5][0]), 0xFFFFFFFEu);
        asm volatile("" : "+v"(cv0), "+v"(cv1));
      }
      BAR();
      STAMPT(2, 3, 384);
      if (master) HS_PROBE();
      __builtin_amdgcn_raw_buffer_store_b32(cv0, wsr, vc0, 0, 16); __builtin_amdgcn_raw_buffer_store_b32(cv1, wsr, vc1, 0, 16);
      STWS(e4, l16, WS_ST2 + (rp * 16 + cb) * 2048 + aw * 1024);
      BAR();
      STAMPT(2, 4, 384);
      if (master) { hs_check(); HS_PROBE(); }
      BAR();
      STAMPT(2, 5, 384);
      if (master) { hs_check(); HS_PROBE(); }
      BAR();
      STAMPT(2, 6, 384);
      if (master) hs_check();
      if (prober) HS_PROBE();
      BAR();
      STAMPT(2, 7, 384);
      if (prober) {
        hs_check();
#pragma nounroll
        for (unsigned s_ = 0; s_ < SPIN_CAP && !done; ++s_) { HS_PROBE(); hs_check(); }
      }
      BAR();
      STAMPT(2, 8, 384);
      unsigned cpf = 0;
      if (CPF2) asm volatile("global_load_dword %0, %1, %2" : "=v"(cpf) : "v"(vcp), "s"(pc0) : "memory");
      u32x4 sg[16], wrv;
      asm volatile("global_load_dwordx4 %0, %1, %2" : "=v"(wrv) : "v"(l16 & 127u), "s"(uni(w_r + cb * 64)) : "memory");
#pragma unroll
      for (int i = 0; i < 16; ++i) {
        asm volatile("global_load_dwordx4 %0, %1, %2 sc1" : "=v"(sg[i]) : "v"(sg_off), "s"(wsu + i * sg_st) : "memory");
        if (i == SGS) { BAR(); STAMPT(2, 9, 384); }
      }
      auto tsum = [&](int o) __attribute__((always_inline)) {
        f32x4 t[4];
#pragma unroll
        for (int i = 0; i < 4; ++i) t[i] = add4(__builtin_bit_cast(f32x4, sg[o + i]), __builtin_bit_cast(f32x4, sg[o + 4 + i]));
        return add4(add4(t[0], t[2]), add4(t[1], t[3]));
      };
      WAITV(8);
#pragma unroll
      for (int i = 0; i < 8; ++i) asm volatile("" : "+v"(sg[i]));
      asm volatile("" : "+v"(wrv) : "v"(cpf));
      f32x4 sm = tsum(0), sn;
      asm volatile("" : "+v"(sm));
      BAR();
      STAMPT(2, 10, 384);
      WAITV(0);
#pragma unroll
      for (int i = 8; i < 16; ++i) asm volatile("" : "+v"(sg[i]));
      sn = tsum(8);
      asm volatile("" : "+v"(sn));
      BAR();
      STAMPT(2, 11, 384);
      sm = add4(sm, sn);
      const float chk = (sm[0] + sm[1]) + (sm[2] + sm[3]);
      if (__builtin_expect(__any(chk != chk), 0)) {
#pragma nounroll
        for (unsigned s_ = 0; s_ < SPIN_CAP; ++s_) {
          unsigned mx = 0;
#pragma unroll
          for (int i = 0; i < 16; ++i) { sg[i] = LDWS(sg_off, i * sg_st); mx = max(max(mx, max(sg[i][0], sg[i][1])), max(sg[i][2], sg[i][3])); }
          if (!__any(mx == EMPTY)) break;
        }
        sm = add4(tsum(0), tsum(8));
      }
      STAMPT(1, 1, 384);
      float mean[2], vr[2];
#pragma unroll
      for (int e = 0; e < 2; ++e) {
        mean[e] = sm[2 * e] * (1.0f / 1024.0f);
        vr[e] = fmaxf(sm[2 * e + 1] * (1.0f / 1024.0f) - mean[e] * mean[e], 0.f);
        asm volatile("" : "+v"(mean[e]), "+v"(vr[e]));
      }
      BAR();
      STAMPT(2, 12, 384);
      if (aw == 1) { if (lane < 48) *(f32x4*)(smem + L_CS + lane * 16) = sm; else *(LAS u32x2*)(misc + (lane - 48) * 2) = (u32x2){0u, 0u}; }
#pragma unroll
      for (int e = 0; e < 2; ++e) { vr[e] = __builtin_amdgcn_sqrtf(rbf(rbf(vr[e]) + 1e-5f)); mean[e] = -rbf(mean[e]); asm volatile("" : "+v"(mean[e]), "+v"(vr[e])); }
      if (aw == 1) *(u32x4*)(smem + L_WR + (l16 & 127u)) = wrv;
      LBAR();
      STAMPT(2, 13, 384);
      if (aw == 0) {
        const f32x2 ra = {__builtin_amdgcn_rcpf(rbf(vr[0])), __builtin_amdgcn_rcpf(rbf(vr[1]))};
        *(f32x2*)(smem + L_SA + lane * 8) = ra; *(f32x2*)(smem + L_SB + lane * 8) = (f32x2){mean[0] * ra[0], mean[1] * ra[1]};
        asm volatile("s_waitcnt lgkmcnt(0)" ::: "memory");
        misc[7] = 1;
      }
      STAMPT(1, 2, 384);
    }
#pragma unroll
    for (int i = 0; i < 4; ++i) if (!ODRAIN) STWS(e4, l16, WS_O + ((rb * 8 + hw * 2 + (i >> 1)) * 32 + 2 * hp + (i & 1)) * 1024);
    unsigned wt;
    asm volatile("global_load_dword %0, %1, %2" : "=v"(wt) : "v"(wto), "s"(uni(wo)) : "memory");
#pragma unroll
    for (int i = 0; i < NW1; ++i) { WAITV(THR - 1); wload(i); }
    u32x4 wr_[20];
#define RLOAD(dst, vo, base) asm volatile("global_load_dwordx4 %0, %1, %2" : "=v"(dst) : "v"(vo), "s"(uni(base)) : "memory")
#pragma unroll
    for (int i = 0; i < NR; ++i) {
      WAITV(THR - 1);
      RLOAD(wr_[i], voW, uni(wo + cb * 64) + (size_t)((i + 12) ^ hrot) * (32 * D_ * 2));
      if (i % 6 == 0) STAMPT(3, 8 + i / 6, 256);
    }
#pragma nounroll
    for (unsigned s_ = 0; s_ < SPIN_CAP; ++s_) if (__all(misc[28 + (lane & 3)] != 0)) break;
#pragma unroll
    for (int j = 0; j < NE; ++j) { WAITV(THR - 1); stageW(j); }
    WAITV(0);
#pragma unroll
    for (int i = 0; i < NR; ++i) asm volatile("" : "+v"(wr_[i]));
#pragma unroll
    for (int i = 0; i < NW1; ++i) wpin(i);
    asm volatile("" :: "v"(wt));
    STAMPT(1, 3, 256);
    {
      u32x4 pv[16], zq;
      const unsigned ko = WS_KVP + hp * 16 * KVB + (hw >> 1) * 2176;
      unsigned kl = ko + (hw & 1) * 1024 + lane * 16, zo = ko + ((hw & 1) * 8 + (lane >> 3)) * KVB + 2048 + (lane & 7) * 16;
      u32x4 sel[2];
#pragma unroll
      for (int e = 0; e < 2; ++e)
#pragma unroll
        for (int i = 0; i < 4; ++i)
          sel[e][i] = ((lane & 15) >> 2) != (lane >> 4) || (i >> 1) != e || (i & 1) != ((lane & 3) >> 1) ? 0u : (lane & 1) ? 0x3F800000u : 0x3F80u;
      f32x4 s[2][2];
      auto ksum = [&]() __attribute__((always_inline)) {
#pragma unroll
        for (int p = 0; p < 16; ++p) {
#pragma unroll
          for (int e = 0; e < 2; ++e) s[p >> 3][e] = mfma16(sel[e], pv[p], (p & 7) ? s[p >> 3][e] : (f32x4){0.f, 0.f, 0.f, 0.f});
          SB0();
        }
        s[0][0] += s[1][0]; s[0][1] += s[1][1];
      };
      asm volatile("" : "+v"(sel[0]), "+v"(sel[1]), "+v"(kl), "+v"(zo));
#pragma nounroll
      for (unsigned s_ = 0; s_ < SPIN_CAP; ++s_) { if (__builtin_amdgcn_readfirstlane(misc[16 + hw])) break; __builtin_amdgcn_s_sleep(1); }
      __builtin_amdgcn_s_sleep(SLA);
      zq = LDW(zo);
#pragma unroll
      for (int p = 0; p < 16; ++p) { pv[p] = LDWS(kl + (p & 7) * KVB, ((p >> 3) ^ (idx & 1)) * (8 * KVB)); SB0(); }
      ksum();
      const float chk = s[0][0][0] + __uint_as_float(zq[1]) + __uint_as_float(zq[3]);
      if (__any(chk != chk)) {
        unsigned need = 0x1FFFFu;
#pragma nounroll
        for (unsigned s_ = 0; s_ < SPIN_CAP; ++s_) {
#pragma unroll
          for (int p = 0; p < 16; ++p) if ((need & (1u << p)) && !__any(max(pv[p][1], pv[p][3]) == EMPTY)) need &= ~(1u << p);
          if (!__any(max(zq[1], zq[3]) == EMPTY)) need &= ~0x10000u;
          if (!need) break;
          asm volatile("" ::: "memory");
#pragma unroll
          for (int p = 0; p < 16; ++p) if (need & (1u << p)) pv[p] = LDWS(kl + (p & 7) * KVB, ((p >> 3) ^ (idx & 1)) * (8 * KVB));
          if (need & 0x10000u) zq = LDW(zo);
        }
        ksum();
      }
      STAMPT(1, 4, 256);
      *(u32x4*)(smem + L_KVL + hw * 1024 + lane * 16) =
          (u32x4){pack2(s[0][0][0], s[0][0][1]), pack2(s[0][0][2], s[0][0][3]), pack2(s[0][1][0], s[0][1][1]), pack2(s[0][1][2], s[0][1][3])};
      f32x4 z = __builtin_bit_cast(f32x4, zq);
#pragma unroll
      for (int i = 0; i < 4; ++i) { float v = z[i]; v += dppf<DPP_ROR8>(v); z[i] = xh_sum(x16_sum(v)); }
      if (lane < 8) *(f32x4*)(smem + L_ZL + hw * 128 + lane * 16) = z;
    }
    LBAR();
#pragma unroll
    for (int j = NE; j < 32; ++j) if (j < 12 || j >= NR + 12) { WAITV(THR3 - 1); stageW(j); }
#pragma nounroll
    for (unsigned s_ = 0; s_ < SPIN_CAP; ++s_) { if (__all(misc[24 + (lane & 3)] != 0)) break; __builtin_amdgcn_s_sleep(1); }
    if (!CLATE)
    {
      const u32x4 e5 = {EMPTY, EMPTY, EMPTY, EMPTY};
      if (has_mlp) { STW(e5, WS_YP + fs * 8192 + mt * 16); STW(e5, WS_YP + fs * 8192 + 4096 + mt * 16); }
#pragma unroll
      for (int rr_ = 0; rr_ < XSR; rr_ += 2)
        if (rr_ + (hw >> 1) < XSR) STW(e5, WS_XS + (rr_ + (hw >> 1)) * 524288 + (rp * 16 + ((mt >> 3) & 15)) * 2048 + cb * 128 + (mt & 7) * 16);
    }
#pragma unroll
    for (int i = 0; i < NR; ++i) *(u32x4*)(smem + wslot(i + 12) + hw * 1024 + lane * 16) = wr_[i];
    asm volatile("s_waitcnt lgkmcnt(0)" ::: "memory");
    if (CLATE)
    {
      const u32x4 e5 = {EMPTY, EMPTY, EMPTY, EMPTY};
      if (has_mlp) { STW(e5, WS_YP + fs * 8192 + mt * 16); STW(e5, WS_YP + fs * 8192 + 4096 + mt * 16); }
#pragma unroll
      for (int rr_ = 0; rr_ < XSR; rr_ += 2)
        if (rr_ + (hw >> 1) < XSR) STW(e5, WS_XS + (rr_ + (hw >> 1)) * 524288 + (rp * 16 + ((mt >> 3) & 15)) * 2048 + cb * 128 + (mt & 7) * 16);
    }
    WAITV(0);
    STAMPT(1, 6, 256);
    if (hw == 0) {
      __builtin_amdgcn_s_sleep(SLB);
#pragma nounroll
      for (unsigned s_ = 0; s_ < SPIN_CAP; ++s_) if (__all(ld32(ws, SY_FB + rp * 256 + lane * 4) != EMPTY)) break;
    }
    BAR();
#pragma nounroll
    for (unsigned s_ = 0; s_ < SPIN_CAP; ++s_) { if (__builtin_amdgcn_readfirstlane(misc[20 + hw])) break; __builtin_amdgcn_s_sleep(2); }
#pragma unroll
    for (int i = NW1; i < 24; ++i) { WAITV(THR2 - 1); wload(i); }
    WAITV(0);
#pragma unroll
    for (int i = NW1; i < 24; ++i) wpin(i);
    STAMPT(1, 7, 256);
    const int c16 = lane & 15, kq = lane >> 4;
    u32x4 gf[2][8];
    u32x2 df[16];
    {
#pragma nounroll
      for (unsigned s_ = 0; s_ < SPIN_CAP; ++s_) { if (__all(misc[8 + (lane & 3)] != 0)) break; __builtin_amdgcn_s_sleep(1); }
      STAMPT(3, 5, 256);
#pragma unroll
      for (int i = 0; i < 8; ++i) {
        const int go = hw * 8192 + i * 1024 + ((lane ^ (((lane >> 4) & 1) * 2 + (i & 1) * 4)) * 16);
        *(u32x4*)(smem + go) = wgr[i];
        *(u32x4*)(smem + 32768 + go) = wur[i];
        *(u32x4*)(smem + L_DW + hw * 8704 + ((lane >> 5) * 8 + i) * 544 + (lane & 31) * 16) = wdr[i];
      }
      asm volatile("s_waitcnt lgkmcnt(0)" ::: "memory");
      STAMPT(3, 6, 256);
      misc[12 + hw] = 1;
#pragma nounroll
      for (unsigned s_ = 0; s_ < SPIN_CAP; ++s_) if (__all(misc[12 + (lane & 3)] != 0)) break;
      asm volatile("" ::: "memory");
      STAMPT(3, 7, 256);
      const unsigned gb = opq(lds0 + (kq * 8 + (c16 >> 2)) * 128 + (hw ^ ((c16 >> 3) + (kq & 1) * 2)) * 32 + (lane & 3) * 8);
      const unsigned db = opq(lds0 + L_DW + hw * 8704 + (kq * 4 + (c16 >> 2)) * 544 + (lane & 3) * 8);
#pragma unroll
      for (int m = 0; m < 2; ++m)
#pragma unroll
        for (int k = 0; k < 8; ++k) gf[m][k] = cat(trread(gb, m * 32768 + k * 4096), trread(gb, m * 32768 + k * 4096 + 512));
#pragma unroll
      for (int t = 0; t < 16; ++t) df[t] = trread(db, t * 32);
      SB0();
#pragma unroll
      for (int k = 0; k < 8; ++k) asm volatile("" : "+v"(gf[0][k]), "+v"(gf[1][k]), "+v"(df[2 * k]), "+v"(df[2 * k + 1]));
    }
    const unsigned gu_st = opq(c16 < 4 ? WS_GU + fs * 1024 + hw * 256 + c16 * 64 + kq * 16 : 0x80000000u);
    const unsigned gu_ld = opq(WS_GU + ((fs & ~3) + (c16 >> 2)) * 1024 + (fs & 3) * 256 + (c16 & 3) * 64 + kq * 16);
    const unsigned yp_st = opq(WS_YP + fs * 8192 + (c16 & 1) * 4096 + hw * 1024 + (c16 >> 1) * 64 + kq * 16);
    const unsigned xsb = opq(lds0 + L_XS + (c16 & 1) * 528 + kq * 16);
    STAMPT(1, 8, 256);
    BAR();
    if (has_mlp) {
      f32x4 ga[2] = {{0.f, 0.f, 0.f, 0.f}, {0.f, 0.f, 0.f, 0.f}};
      u32x4 xf[8];
#pragma unroll
      for (int k = 0; k < 8; ++k) xf[k] = ldsr(xsb, k * 64);
#pragma unroll
      for (int k = 0; k < 8; ++k) { ga[0] = mfma16(gf[0][k], xf[k], ga[0]); ga[1] = mfma16(gf[1][k], xf[k], ga[1]); }
      STW(seal(__builtin_bit_cast(u32x4, (c16 & 2) ? ga[1] : ga[0])), gu_st);
      STAMPT(1, 14, 256);
      __builtin_amdgcn_s_sleep(SLG0);
      u32x4 q;
#pragma nounroll
      for (unsigned s_ = 0; s_ < SPIN_CAP; ++s_) {
        q = LDW(gu_ld);
        if (!__any(max(q[1], q[3]) == EMPTY)) break;
        asm volatile("" ::: "memory");
        __builtin_amdgcn_s_sleep(SLG1);
      }
      STAMPT(1, 15, 256);
      float av[4];
#pragma unroll
      for (int i = 0; i < 4; ++i) {
        float v = __uint_as_float(q[i]);
        v += dppf<DPP_ROR8>(v); v += dppf<DPP_ROR4>(v);
        const float o_ = dppf<DPP_X2>(v);
        const float gs = rbf((c16 & 2) ? o_ : v), us = rbf((c16 & 2) ? v : o_);
        const float sg = rbf(gs * __builtin_amdgcn_rcpf(1.0f + __builtin_amdgcn_exp2f(gs * -1.44269504088896340736f)));
        av[i] = sg * us;
      }
      const u32x2 ab = {pack2(av[0], av[1]), pack2(av[2], av[3])};
      f32x4 y[2] = {{0.f, 0.f, 0.f, 0.f}, {0.f, 0.f, 0.f, 0.f}};
#pragma unroll
      for (int t = 0; t < 8; ++t) {
        const u32x2 bt = (c16 >> 1) == t ? ab : (u32x2){0u, 0u};
#pragma unroll
        for (int j = 0; j < 2; ++j)
          y[j] = __builtin_amdgcn_mfma_f32_16x16x16bf16_1k(__builtin_bit_cast(s16x4, df[8 * j + t]), __builtin_bit_cast(s16x4, bt), y[j], 0, 0, 0);
      }
      STW(seal(__builtin_bit_cast(u32x4, y[0])), yp_st); STW(seal(__builtin_bit_cast(u32x4, y[1])), yp_st + 512);
      STAMPT(0, 13, 256);
    }
  }
  if (bid < 64) {
    const int tk = bid >> 5, c0 = (bid & 31) * 32;
    unsigned xn = 0, xh = 0;
    if (tid < 16) {
      const unsigned xo = WS_XS + (idx & (XSR - 1)) * 524288u + (unsigned)misc[3 + tk] * 4096u + c0 * 2 + (tid >> 1) * 8;
#pragma nounroll
      for (unsigned s_ = 0; s_ < SPIN_CAP; ++s_) {
        const u64 a = ld64(ws, xo), b = ld64(ws, xo + 2048);
        xn = (tid & 1) ? (unsigned)(a >> 32) : (unsigned)a; xh = (tid & 1) ? (unsigned)(b >> 32) : (unsigned)b;
        if (__all((unsigned)(a >> 32) != EMPTY && (unsigned)(b >> 32) != EMPTY)) break;
      }
    }
    const float sg = rbf(__builtin_amdgcn_rcpf(1.0f + __builtin_amdgcn_exp2f(key_val((unsigned)misc[5 + tk]) * -1.44269504088896340736f)));
    const size_t oo = (size_t)misc[1 + tk] * D_ + c0 + tid * 2;
    const unsigned yo = opq(WS_YP + (tid >> 3) * 8192 + tk * 4096 + c0 * 4 + (tid & 7) * 16);
    BAR();
    __builtin_amdgcn_s_sleep(YSL0);
    u32x4 pv[4];
#pragma nounroll
    for (unsigned s_ = 0; s_ < SPIN_CAP; ++s_) {
      unsigned mk = 0u;
#pragma unroll
      for (int p = 0; p < 4; ++p) {
        pv[p] = (u32x4){0u, 0u, 0u, 0u};
        if (p < 3 || wave < 4) pv[p] = LDWS(yo, p * (64 * 8192));
      }
#pragma unroll
      for (int p = 0; p < 4; ++p) mk = max(max(mk, pv[p][1]), pv[p][3]);
      if (!__any(mk == EMPTY)) break;
      asm volatile("" ::: "memory");
      __builtin_amdgcn_s_sleep(YSL1);
    }
    STAMPT(0, 15, 0);
    f32x4 s = (__builtin_bit_cast(f32x4, pv[0]) + __builtin_bit_cast(f32x4, pv[1])) + (__builtin_bit_cast(f32x4, pv[2]) + __builtin_bit_cast(f32x4, pv[3]));
#pragma unroll
    for (int i = 0; i < 4; ++i) { float v = s[i]; v += dppf<DPP_ROR8>(v); s[i] = xh_sum(x16_sum(v)); }
    float* rd = (float*)(smem + L_RED);
    if (lane < 8) *(f32x4*)(rd + wave * 32 + lane * 4) = s;
    LBAR();
    if (tid < 16) {
      f32x2 y = {0.f, 0.f};
#pragma unroll
      for (int p = 0; p < 8; ++p) y += *(const f32x2*)(rd + p * 32 + tid * 2);
      const float t0 = rbf(bflo(xn) + rbf(rbf(y[0]) * sg)), t1 = rbf(bfhi(xn) + rbf(rbf(y[1]) * sg));
      *(unsigned*)(out + oo) = pack2(bflo(xh) + t0, bfhi(xh) + t1);
      asm volatile(".Lfused_end:" ::: "memory");
    }
    STAMPT(0, 14, 0);
  }
}

extern "C" void kernel_launch(void* const* d_in, const int* in_sizes, int n_in, void* d_out, int out_size,
                              void* d_ws, size_t ws_size, hipStream_t stream) {
  (void)hipFuncSetAttribute((const void*)fused, hipFuncAttributeMaxDynamicSharedMemorySize, LDS_TOTAL);
  fused<<<256, 512, LDS_TOTAL, stream>>>((const u16*)d_in[0], (const u16*)d_in[1], (const u16*)d_in[2], (const u16*)d_in[4],
                                         (const u16*)d_in[5], (const u16*)d_in[6], (const u16*)d_in[7], (u16*)d_out, (char*)d_ws);
}
```

```cpp
#include <hip/hip_runtime.h>
#include <stdint.h>

#define LAS __attribute__((address_space(3)))
#define GAS __attribute__((address_space(1)))
#define DI __device__ __forceinline__

#define XSR 1
#define GPF 5
#define GOW 14
#define NE 4
#define WPOL
#define HRM 3
#define OW0 4
#define SLC0 24
#define SLC1 8
#define SLD1 0
#define SGS 7
#define CPF2 1
#define RSG 3
#define EOUT 0
#define GB 1
#define NR 20
#define THR3 4
#define THR2 6
#define THR 6
#define NW1 10
#define SLA 8
#define SLB 0
constexpr int CODE_PER = 11;
constexpr int XSL1 = 2;
#define YSL0 12
#define YSL1 4
#define SLG0 14
#define SLG1 2
typedef unsigned short u16;
typedef unsigned long long u64;
typedef __attribute__((ext_vector_type(2))) __bf16 bf16x2;
typedef __attribute__((ext_vector_type(8))) __bf16 bf16x8;
typedef __attribute__((ext_vector_type(4))) short s16x4;
typedef __attribute__((ext_vector_type(16))) float f32x16;
typedef __attribute__((ext_vector_type(4))) float f32x4;
typedef __attribute__((ext_vector_type(2))) float f32x2;
typedef __attribute__((ext_vector_type(4))) unsigned u32x4;
typedef __attribute__((ext_vector_type(2))) unsigned u32x2;
typedef GAS unsigned gu32;
typedef GAS unsigned long long gu64;

constexpr int S_ = 2048, D_ = 1024, F_ = 3584;

constexpr unsigned SY_CH = 0, SY_GO = 1024, SY_RE = 2048, SY_FB = 4096, SY_FD = 8192;
constexpr unsigned WS_RS  = 16384;
constexpr unsigned WS_CS  = WS_RS + 256 * 1024;
constexpr unsigned KVB    = 4352;
constexpr unsigned WS_KVP = WS_CS + 256 * 768;
constexpr unsigned WS_ST2 = WS_KVP + 256 * KVB;
constexpr unsigned WS_GU  = WS_ST2 + 256 * 2048;
constexpr unsigned WS_XS  = WS_GU + 256 * 1024;
constexpr unsigned WS_YP  = WS_XS + XSR * 524288;
constexpr unsigned WS_O   = WS_YP + 56 * 8192;
constexpr unsigned WS_END = WS_O + S_ * D_ * 2;
constexpr size_t WS_DIAG  = 64u << 20;
#define STAMPT(kid, i, T) do {} while (0)

DI float bflo(unsigned u) { return __uint_as_float(u << 16); }
DI float bfhi(unsigned u) { return __uint_as_float(u & 0xffff0000u); }
DI float rbf(float x) { return (float)(__bf16)x; }
DI unsigned pack2(float a, float b) { bf16x2 c; c[0] = (__bf16)a; c[1] = (__bf16)b; return __builtin_bit_cast(unsigned, c); }
DI f32x4 mfma16(u32x4 a, u32x4 b, f32x4 c) {
  return __builtin_amdgcn_mfma_f32_16x16x32_bf16(__builtin_bit_cast(bf16x8, a), __builtin_bit_cast(bf16x8, b), c, 0, 0, 0);
}
DI f32x16 mfma32(u32x4 a, u32x4 b, f32x16 c) {
  return __builtin_amdgcn_mfma_f32_32x32x16_bf16(__builtin_bit_cast(bf16x8, a), __builtin_bit_cast(bf16x8, b), c, 0, 0, 0);
}
DI u32x2 trread(const char* p) {
  s16x4 v = __builtin_amdgcn_ds_read_tr16_b64_v4i16((LAS s16x4*)p);
  return __builtin_bit_cast(u32x2, v);
}
DI unsigned opq(unsigned v) { asm volatile("" : "+v"(v)); return v; }
DI u32x2 trread(unsigned base, int off) {
  s16x4 v = __builtin_amdgcn_ds_read_tr16_b64_v4i16((LAS s16x4*)((LAS char*)(size_t)base + off));
  return __builtin_bit_cast(u32x2, v);
}
DI u32x4 ldsr(unsigned base, int off) { return *(const LAS u32x4*)((const LAS char*)(size_t)base + off); }
DI u32x4 cat(u32x2 a, u32x2 b) { return (u32x4){a[0], a[1], b[0], b[1]}; }
DI u32x4 pick(int c, u32x4 a, u32x4 b) { const unsigned m = c ? ~0u : 0u; return (a & m) | (b & ~m); }
DI f32x4 add4(f32x4 a, f32x4 b) {
  f32x4 c;
#pragma unroll
  for (int i = 0; i < 4; ++i) asm("v_add_f32 %0, %1, %2" : "=v"(c[i]) : "v"(a[i]), "v"(b[i]));
  return c;
}
DI float dot2(unsigned a, unsigned b, float c) {
  return __builtin_amdgcn_fdot2_f32_bf16(__builtin_bit_cast(bf16x2, a), __builtin_bit_cast(bf16x2, b), c, false);
}
DI const char* uni(const void* p) {
  const u64 v = (u64)p;
  return (const char*)(((u64)(unsigned)__builtin_amdgcn_readfirstlane((int)(v >> 32)) << 32) | (unsigned)__builtin_amdgcn_readfirstlane((int)v));
}
DI void glds16(unsigned voff, const void* sbase, unsigned lds) {
  asm volatile("s_mov_b32 m0, %2\n\ts_nop 0\n\tglobal_load_lds_dwordx4 %0, %1" ::"v"(voff), "s"(uni(sbase)), "s"(__builtin_amdgcn_readfirstlane(lds)) : "memory");
}
DI float xh_sum(float v) {
  auto r = __builtin_amdgcn_permlane32_swap(__float_as_uint(v), __float_as_uint(v), false, false);
  return __uint_as_float(r[0]) + __uint_as_float(r[1]);
}
template <int CTRL> DI unsigned dppu(unsigned v) { return (unsigned)__builtin_amdgcn_update_dpp(0, (int)v, CTRL, 0xf, 0xf, true); }
template <int CTRL> DI float dppf(float v) { return __uint_as_float(dppu<CTRL>(__float_as_uint(v))); }
constexpr int DPP_X1 = 0xB1, DPP_X2 = 0x4E, DPP_HMIR = 0x141, DPP_MIR = 0x140, DPP_ROR8 = 0x128, DPP_ROR4 = 0x124, DPP_ROR12 = 0x12C;
DI float oct_sum(float v) { v += dppf<DPP_X1>(v); v += dppf<DPP_X2>(v); v += dppf<DPP_HMIR>(v); return v; }
DI float x16_sum(float v) {
  auto r = __builtin_amdgcn_permlane16_swap(__float_as_uint(v), __float_as_uint(v), false, false);
  return __uint_as_float(r[0]) + __uint_as_float(r[1]);
}
DI unsigned phi_pair(f32x2 t) {
  const f32x2 u = t + 1.000001f;
  const f32x2 l = t * 1.44269504088896340736f;
  const f32x2 e = (f32x2){__builtin_amdgcn_exp2f(l[0]), __builtin_amdgcn_exp2f(l[1])} + 1e-6f;
  return pack2(__builtin_amdgcn_fmed3f(e[0], u[0], 1.000001f), __builtin_amdgcn_fmed3f(e[1], u[1], 1.000001f));
}
#define RLX_AGENT __ATOMIC_RELAXED, __HIP_MEMORY_SCOPE_AGENT
DI void st32(char* ws, unsigned off, unsigned v) { __hip_atomic_store((gu32*)(ws + off), v, RLX_AGENT); }
DI void st64(char* ws, unsigned off, u64 v) { __hip_atomic_store((gu64*)(ws + off), v, RLX_AGENT); }
DI unsigned ld32(char* ws, unsigned off) { return __hip_atomic_load((gu32*)(ws + off), RLX_AGENT); }
DI u64 ld64(char* ws, unsigned off) { return __hip_atomic_load((gu64*)(ws + off), RLX_AGENT); }
#define WAITV(n) asm volatile("s_waitcnt vmcnt(%0)" :: "n"(n) : "memory")
#define BAR() do { asm volatile("" ::: "memory"); __builtin_amdgcn_s_barrier(); asm volatile("" ::: "memory"); } while (0)
#define LBAR() do { asm volatile("s_waitcnt lgkmcnt(0)" ::: "memory"); __builtin_amdgcn_s_barrier(); asm volatile("" ::: "memory"); } while (0)
#define SB0() __builtin_amdgcn_sched_barrier(0)
constexpr unsigned SPIN_CAP = 1u << 22;
constexpr unsigned EMPTY = 0xFFFFFFFFu;
DI u32x4 seal(u32x4 v) { v[1] = min(v[1], 0xFFFFFFFEu); v[3] = min(v[3], 0xFFFFFFFEu); return v; }
DI unsigned mk_key(float v, unsigned idx) {
  const unsigned u = __float_as_uint(v) >> 16;
  const unsigned o = (u & 0x8000u) ? (~u & 0xffffu) : (u | 0x8000u);
  return (o << 16) | (0xFFFFu - idx);
}
DI unsigned key_idx(unsigned k) { return (0xFFFFu - (k & 0xFFFFu)) & (S_ - 1); }
DI float key_val(unsigned k) { const unsigned o = k >> 16; return __uint_as_float(((o & 0x8000u) ? (o & 0x7fffu) : (~o & 0xffffu)) << 16); }
DI void top2_mix(unsigned& b0, unsigned& b1, unsigned c0, unsigned c1) {
  const unsigned lo = min(b0, c0), mx = max(b1, c1);
  b0 = max(b0, c0); b1 = max(lo, mx);
}
DI void top2_wave(unsigned& b0, unsigned& b1) {
  top2_mix(b0, b1, dppu<DPP_X1>(b0), dppu<DPP_X1>(b1));
  top2_mix(b0, b1, dppu<DPP_X2>(b0), dppu<DPP_X2>(b1));
  top2_mix(b0, b1, dppu<DPP_HMIR>(b0), dppu<DPP_HMIR>(b1));
  top2_mix(b0, b1, dppu<DPP_MIR>(b0), dppu<DPP_MIR>(b1));
  { auto p = __builtin_amdgcn_permlane16_swap(b0, b0, false, false); auto q = __builtin_amdgcn_permlane16_swap(b1, b1, false, false);
    b0 = p[0]; b1 = q[0]; top2_mix(b0, b1, p[1], q[1]); }
  { auto p = __builtin_amdgcn_permlane32_swap(b0, b0, false, false); auto q = __builtin_amdgcn_permlane32_swap(b1, b1, false, false);
    b0 = p[0]; b1 = q[0]; top2_mix(b0, b1, p[1], q[1]); }
}

constexpr int L_A = 0, L_Q = 65536, L_KV = 98304, LDS_TOTAL = 163840;
constexpr int L_M  = L_KV;
constexpr int L_SA = L_M, L_SB = L_M + 512, L_CS = L_M + 1024;
constexpr int L_MISC = L_M + 1792;
constexpr int L_XCH = L_M + 2048;
constexpr int L_KVL = L_M + 11264;
constexpr int L_ZL = L_M + 15360;
constexpr int L_XS = L_M + 3072;
constexpr int L_RED = L_M + 5120;
constexpr int L_ACT = L_M + 9216;
constexpr int L_TB = L_M + 9472;
constexpr int L_WR = L_M + 15872;
constexpr int L_DW = 114688;
constexpr int L_X2 = 81920;
DI constexpr int wslot(int j) { return j < 12 ? 114688 + j * 4096 : (j - 12) * 4096; }

__global__ __launch_bounds__(512, 2) void fused(const u16* __restrict__ x, const u16* __restrict__ wqkv,
                                                const u16* __restrict__ wo, const u16* __restrict__ w_r,
                                                const u16* __restrict__ wg, const u16* __restrict__ wu,
                                                const u16* __restrict__ wd, u16* __restrict__ out, char* __restrict__ ws) {
  extern __shared__ __attribute__((aligned(16))) char smem[];
  u64 pc0; unsigned clen;
  asm volatile("s_getpc_b64 %0\n.Lfused_pc:\n\ts_mov_b32 %1, .Lfused_end-.Lfused_pc-16" : "=s"(pc0), "=s"(clen));
  const int tid = threadIdx.x, lane = tid & 63;
  const int wave = __builtin_amdgcn_readfirstlane(tid >> 6);
  STAMPT(0, 0, 0);
  const int bid = blockIdx.x, xcd = bid & 7, idx = bid >> 3;
  const int rb = (xcd & 1) * 8 + (idx & 7);
  const int hp = (xcd >> 1) * 4 + (idx >> 3);
  const int rp = rb, cb = ((3 + (xcd >> 1) + 2 * (xcd & 1)) & 3) * 4 + (hp & 3);
  const int hrot = 4 * (cb & HRM);
  const unsigned lds0 = (unsigned)(size_t)smem;
  const __amdgpu_buffer_rsrc_t wsr = __builtin_amdgcn_make_buffer_rsrc(ws, 0, WS_END, 0x00020000);
#define LDW(off) __builtin_amdgcn_raw_buffer_load_b128(wsr, (off), 0, 16)
#define LDWS(off, so) __builtin_amdgcn_raw_buffer_load_b128(wsr, (off), (so), 16)
#define STW(v, off) __builtin_amdgcn_raw_buffer_store_b128((v), wsr, (off), 0, 16)
#define STWS(v, off, so) __builtin_amdgcn_raw_buffer_store_b128((v), wsr, (off), (so), 16)
  const int kph = 4 * (xcd >> 1) + 8 * (xcd & 1), kg_ = (hp + rb) & 3;
  auto korder = [&](int t) { return ((t + kph) & 15) ^ kg_; };
  u32x4 e0;
  {
    const char* eb = wave < 4 ? (const char*)pc0 : uni(ws);
    const unsigned eo = wave < 4 ? min((unsigned)(idx * CODE_PER + min(tid >> 4, CODE_PER - 1)) * 128u, clen) : wave == 6 && bid == 0 ? SY_RE + lane * 16 : SY_GO + bid * 4;
    asm volatile("global_load_dwordx4 %0, %1, %2 sc1" : "=v"(e0) : "v"(eo), "s"(uni(eb)) : "memory");
  }
  {
    const int r0 = 16 * wave + (lane >> 3), r1 = r0 + 8, pb_ = lane >> 4;
    const unsigned a0 = (unsigned)(r0 * D_ + ((lane & 7) ^ ((r0 >> 1) & 7)) * 8) * 2u, a1 = (unsigned)(r1 * D_ + ((lane & 7) ^ ((r1 >> 1) & 7)) * 8) * 2u;
    const unsigned b0 = (unsigned)((8 * wave + 4 * (pb_ >> 1) + ((lane >> 2) & 3)) * (3 * D_) + (pb_ & 1) * 32 + ((lane & 3) ^ ((wave & 1) * 2)) * 8) * 2u;
#pragma unroll
    for (int t = 0; t < 2; ++t) {
      const char* pa = uni(x + (size_t)rb * 128 * D_) + korder(t) * 128;
      const char* pb = uni(wqkv + hp * 64) + (size_t)korder(t) * (64 * 3 * D_ * 2);
      glds16(a0, pa, lds0 + L_A + t * 16384 + wave * 2048); glds16(a1, pa, lds0 + L_A + t * 16384 + wave * 2048 + 1024);
      glds16(b0 + 2048, pb, lds0 + L_KV + t * 16384 + wave * 1024); glds16(b0 + 4096, pb, lds0 + L_KV + t * 16384 + 8192 + wave * 1024);
      glds16(b0, pb, lds0 + L_Q + t * 8192 + wave * 1024);
      if (t == 0) BAR();
    }
    SB0();
  }
  volatile LAS int* misc = (volatile LAS int*)(size_t)(lds0 + L_MISC);
  const bool has_mlp = bid < 224;
  const int fs = has_mlp ? (bid & 7) * 28 + (bid >> 3) : (bid & 7) * 28;
  const int tcs = (rb & 4) ? 4 : 3;
  const unsigned rs_o = WS_RS + (rb * 16 + korder(0)) * 1024, cs_o = WS_CS + (hp * 16 + korder(tcs)) * 768;
  const u32x4 ONES4 = {0x3F803F80u, 0x3F803F80u, 0x3F803F80u, 0x3F803F80u};

  if (wave < 4) {
    const int wr = wave >> 1, wc = wave & 1;
    const int r = lane & 15, kq = lane >> 4;
    f32x4 acc[4][6];
    {
      unsigned aO[2], bQ[2], bK[2];
#pragma unroll
      for (int k = 0; k < 2; ++k) aO[k] = opq(lds0 + L_A + (64 * wr + r) * 128 + (((4 * k + kq) ^ ((r >> 1) & 7)) * 16));
#pragma unroll
      for (int c = 0; c < 2; ++c) {
        const unsigned o_ = lds0 + kq * 1024 + wc * 256 + (r >> 2) * 64 + ((c ^ (kq & 1)) * 32) + (lane & 3) * 8;
        bQ[c] = opq(o_ + L_Q); bK[c] = opq(o_ + L_KV);
      }
#pragma unroll
      for (int i = 0; i < 4; ++i)
#pragma unroll
        for (int j = 0; j < 6; ++j) acc[i][j] = (f32x4){0.f, 0.f, 0.f, 0.f};
      struct Fr { u32x4 a[4]; u32x2 bl[6], bh[6]; };
      Fr F[2];
      auto bread = [&](int f, int b, int k, int hi) {
        return f < 2 ? trread(bQ[f & 1], k * 4096 + b * 8192 + hi * 512) : trread(bK[f & 1], k * 4096 + b * 16384 + ((f >> 1) - 1) * 8192 + hi * 512);
      };
      auto step = [&](Fr& C, Fr& N, int b, int k, int MC, int ML) {
        int ld = 0;
        constexpr int ORD[6] = {2, 3, 4, 5, 0, 1};
        auto load1 = [&]() {
          if (ML == 0) return;
          if (ld < 4) N.a[ld] = ldsr(aO[k], b * 16384 + ld * 2048);
          else {
            const int e = ld - 4, f = ML == 1 ? (e >> 1) : ORD[(e >> 1) % 6], nf = ML == 1 ? 2 : ML == 2 ? 4 : 6;
            if ((e >> 1) < nf) { if (e & 1) N.bh[f] = bread(f, b, k, 1); else N.bl[f] = bread(f, b, k, 0); }
          }
          ++ld;
        };
#pragma unroll
        for (int m = 0; m < 24; ++m) {
          const int j = ORD[m >> 2], i = m & 3;
          if (!((j < 2 ? 1 : 2) & MC)) continue;
          if (j < 2) acc[i][j] = mfma16(cat(C.bl[j], C.bh[j]), C.a[i], acc[i][j]);
          else acc[i][j] = mfma16(C.a[i], cat(C.bl[j], C.bh[j]), acc[i][j]);
          load1();
          SB0();
        }
      };
      WAITV(5); BAR();
      STAMPT(0, 1, 0);
#pragma unroll
      for (int m = 0; m < 4; ++m) F[0].a[m] = ldsr(aO[0], m * 2048);
#pragma unroll
      for (int f = 0; f < 6; ++f) { F[0].bl[f] = bread(f, 0, 0, 0); F[0].bh[f] = bread(f, 0, 0, 1); }
      for (int t4 = 0; t4 < 12; t4 += 4) {
#pragma unroll
        for (int b = 0; b < 4; ++b) {
          step(F[0], F[1], b, 1, 3, 3);
          if (b == 0 && t4 == 0) { asm volatile("s_waitcnt vmcnt(0)" :: "v"(e0) : "memory"); STAMPT(3, 13, 0); BAR(); STAMPT(3, 14, 0); }
          step(F[1], F[0], (b + 1) & 3, 0, 3, 3);
          BAR();
        }
      }
#pragma unroll
      for (int b = 0; b < 4; ++b) {
        step(F[0], F[1], b, 1, 2, 2);
        step(F[1], F[0], (b + 1) & 3, 0, 2, b < 3 ? 2 : 0);
        if (b < 2) BAR();
      }
      misc[28 + wave] = 1;
#pragma nounroll
      for (unsigned s_ = 0; s_ < SPIN_CAP; ++s_) if (__builtin_amdgcn_readfirstlane(misc[7])) break;
      asm volatile("" ::: "memory");
      STAMPT(0, 2, 0);
      {
        const float* sa = (const float*)(smem + L_SA) + 64 * wr + 4 * kq;
        const float* cs = (const float*)(smem + L_CS) + wc * 96 + r;
        const float c4[4] = {cs[32], cs[48], cs[64], cs[80]};
        unsigned kp[4][2][2], vp[4][2][2];
#pragma unroll
        for (int i = 0; i < 4; ++i) {
          const f32x4 a4 = *(const f32x4*)(sa + 16 * i), b4 = *(const f32x4*)(sa + 128 + 16 * i);
          const f32x2 aa[2] = {{a4[0], a4[1]}, {a4[2], a4[3]}}, bb[2] = {{b4[0], b4[1]}, {b4[2], b4[3]}};
#pragma unroll
          for (int j = 0; j < 2; ++j)
#pragma unroll
            for (int e = 0; e < 2; ++e) {
              const f32x2 tk = (f32x2){acc[i][2 + j][2 * e], acc[i][2 + j][2 * e + 1]} * aa[e] + bb[e] * c4[j];
              const f32x2 tv = (f32x2){acc[i][4 + j][2 * e], acc[i][4 + j][2 * e + 1]} * aa[e] + bb[e] * c4[2 + j];
              kp[i][j][e] = phi_pair(tk);
              vp[i][j][e] = pack2(tv[0], tv[1]);
            }
        }
        f32x4 kv[2][2], zv[2];
#pragma unroll
        for (int d = 0; d < 2; ++d) {
          zv[d] = (f32x4){0.f, 0.f, 0.f, 0.f}; kv[d][0] = zv[d]; kv[d][1] = zv[d];
#pragma unroll
          for (int u = 0; u < 2; ++u) {
            const u32x4 ka = {kp[2 * u][d][0], kp[2 * u][d][1], kp[2 * u + 1][d][0], kp[2 * u + 1][d][1]};
#pragma unroll
            for (int e = 0; e < 2; ++e)
              kv[d][e] = mfma16(ka, (u32x4){vp[2 * u][e][0], vp[2 * u][e][1], vp[2 * u + 1][e][0], vp[2 * u + 1][e][1]}, kv[d][e]);
            zv[d] = mfma16(ka, ONES4, zv[d]);
          }
        }
        char* xo = smem + L_XCH + wave * 2048 + lane * 16;
        *(f32x4*)xo = wr ? kv[0][0] : kv[0][1];
        *(f32x4*)(xo + 1024) = wr ? kv[1][0] : kv[1][1];
        if (r < 2) *(f32x4*)(smem + L_XCH + 8192 + wave * 128 + r * 64 + kq * 16) = r ? zv[1] : zv[0];
#pragma unroll
        for (int m = 0; m < 4; ++m) F[0].a[m] = ldsr(aO[0], m * 2048);
#pragma unroll
        for (int f = 0; f < 2; ++f) { F[0].bl[f] = bread(f, 0, 0, 0); F[0].bh[f] = bread(f, 0, 0, 1); }
        STAMPT(1, 0, 0);
        asm volatile("s_waitcnt lgkmcnt(0)" ::: "memory");
        misc[16 + wave] = 1;
#pragma nounroll
        for (unsigned s_ = 0; s_ < SPIN_CAP; ++s_) if (__builtin_amdgcn_readfirstlane(misc[16 + (wave ^ 2)])) break;
        asm volatile("" ::: "memory");
        const char* xi = smem + L_XCH + (wave ^ 2) * 2048 + lane * 16;
        const f32x4 s0 = *(const f32x4*)xi + (wr ? kv[0][1] : kv[0][0]), s1 = *(const f32x4*)(xi + 1024) + (wr ? kv[1][1] : kv[1][0]);
        const unsigned ko = WS_KVP + (hp * 16 + rb) * KVB + wc * 2176;
        STW(seal((u32x4){pack2(s0[0], s0[1]), pack2(s0[2], s0[3]), pack2(s1[0], s1[1]), pack2(s1[2], s1[3])}), ko + wr * 1024 + lane * 16);
        if (wr == 0 && lane < 8) {
          const f32x4 z = *(const f32x4*)(smem + L_XCH + 8192 + wave * 128 + lane * 16) + *(const f32x4*)(smem + L_XCH + 8192 + (wave + 2) * 128 + lane * 16);
          STW(seal(__builtin_bit_cast(u32x4, z)), ko + 2048 + lane * 16);
        }
                STAMPT(0, 3, 0);
      }
#pragma unroll
      for (int b = 0; b < 4; ++b) {
        step(F[0], F[1], b, 1, 1, 1);
        step(F[1], F[0], (b + 1) & 3, 0, 1, b < 3 ? 1 : 0);
        if (b == GB) misc[16 + wave] = 2;
      }
    }
    u32x4 qp[4];
    {
      const float* sa = (const float*)(smem + L_SA) + 64 * wr + r;
      const float* cs = (const float*)(smem + L_CS) + wc * 96 + 4 * kq;
      const f32x4 c0 = *(const f32x4*)cs, c1 = *(const f32x4*)(cs + 16);
#pragma unroll
      for (int i = 0; i < 4; ++i) {
        const float a = sa[16 * i], b = sa[128 + 16 * i];
        qp[i][0] = phi_pair((f32x2){acc[i][0][0], acc[i][0][1]} * a + (f32x2){c0[0], c0[1]} * b);
        qp[i][1] = phi_pair((f32x2){acc[i][0][2], acc[i][0][3]} * a + (f32x2){c0[2], c0[3]} * b);
        qp[i][2] = phi_pair((f32x2){acc[i][1][0], acc[i][1][1]} * a + (f32x2){c1[0], c1[1]} * b);
        qp[i][3] = phi_pair((f32x2){acc[i][1][2], acc[i][1][3]} * a + (f32x2){c1[2], c1[3]} * b);
        asm volatile("" : "+v"(qp[i]));
      }
    }
    u32x2 xr[8];
    {
      const int row = wave * 32 + (lane & 31);
#pragma unroll
      for (int c = 0; c < 8; ++c) xr[c] = *(const u32x2*)(smem + L_A + ((cb & 3) ^ kg_) * 16384 + row * 128 + ((c ^ ((row >> 1) & 7)) * 16) + (lane >> 5) * 8);
    }
    STAMPT(0, 4, 0);
    LBAR();
    STAMPT(0, 5, 0);
    {
      const u32x4 k0 = *(const u32x4*)(smem + L_KVL + wc * 2048 + lane * 16), k1 = *(const u32x4*)(smem + L_KVL + wc * 2048 + 1024 + lane * 16);
      const float* zl = (const float*)(smem + L_ZL) + wc * 64 + kq * 4;
      const f32x4 za = *(const f32x4*)zl + *(const f32x4*)(zl + 32), zb = *(const f32x4*)(zl + 16) + *(const f32x4*)(zl + 48);
      const u32x4 zf = {pack2(za[0], za[1]), pack2(za[2], za[3]), pack2(zb[0], zb[1]), pack2(zb[2], zb[3])};
      const f32x4 Z4 = {0.f, 0.f, 0.f, 0.f};
      const unsigned oo = WS_O + ((rb * 8 + wr * 4) * 32 + 2 * hp + wc) * 1024 + lane * 16;
#pragma unroll
      for (int i = 0; i < 4; ++i) {
        const f32x4 dn = mfma16(zf, qp[i], Z4), x0 = mfma16(k0, qp[i], Z4), x1 = mfma16(k1, qp[i], Z4);
        const float ri = __builtin_amdgcn_rcpf(dn[0] + 1e-6f);
        STW(((u32x4){pack2(x0[0] * ri, x0[1] * ri), pack2(x0[2] * ri, x0[3] * ri), pack2(x1[0] * ri, x1[1] * ri), pack2(x1[2] * ri, x1[3] * ri)}),
            oo + i * 32768);
      }
      misc[24 + wave] = 1;
      WAITV(0);
      if (lane == 0) st32(ws, SY_FB + rb * 256 + hp * 16 + wave * 4, 1u);
    }
    STAMPT(0, 6, 0);
    {
      const int h = lane >> 5, s32 = lane & 31;
      u32x2 wv[8];
      auto wvld = [&]() {
#pragma unroll
        for (int c = 0; c < 8; ++c) wv[c] = *(const volatile LAS u32x2*)(size_t)(lds0 + L_WR + c * 16 + h * 8);
      };
      wvld();
      u32x4 of[32][2];
      const unsigned ol = WS_O + (rp * 8 + wave * 2 + (kq & 1)) * 32768 + (h * 16 + r) * 16;
      auto oload1 = [&](int j, int e) { of[j][e] = LDWS(ol + e * 512 + (j & 3) * 1024, ((j & ~3) ^ hrot) * 1024); };
      auto oload = [&](int j) { oload1(j, 0); oload1(j, 1); };
      f32x16 a2[4];
      unsigned wO[3];
#pragma unroll
      for (int e = 0; e < 3; ++e) wO[e] = opq(lds0 + e * 65536 + h * 512 + (r >> 2) * 64 + (((kq & 1) ^ h) * 32) + (lane & 3) * 8);
      constexpr int PF = GPF, OW = GOW;
      u32x2 wl[8], wh[8];
      auto wld = [&](int g) {
        const int j = g >> 2;
        const int o_ = (wslot(j) & 65535) + ((g >> 1) & 1) * 1024 + (g & 1) * 256;
        wl[g & 7] = trread(wO[wslot(j) >> 16], o_); wh[g & 7] = trread(wO[wslot(j) >> 16], o_ + 2048);
      };
      const int row = wave * 32 + s32;
      const unsigned xb = opq(lds0 + L_X2 + row * 128 + h * 8);
      const int sw = (row >> 1) & 7;
      float sw_ = 0.f;
#pragma unroll
      for (int c = 0; c < 16; ++c) sw_ = dot2(wv[c >> 1][c & 1], 0x3F803F80u, sw_);
      sw_ = xh_sum(sw_);
      asm volatile("" : "+v"(sw_));
      BAR();
      STAMPT(0, 7, 0);
#pragma unroll
      for (int j = 0; j < OW0; ++j) { oload(j); SB0(); }
#pragma unroll
      for (int c = 0; c < 8; ++c) {
        const u32x2 xx = xr[c];
#pragma unroll
        for (int i = 0; i < 2; ++i) {
          a2[c >> 2][(c & 3) * 4 + 2 * i] = bflo(xx[i]); a2[c >> 2][(c & 3) * 4 + 2 * i + 1] = bfhi(xx[i]);
          a2[2 + (c >> 2)][(c & 3) * 4 + 2 * i] = 0.f; a2[2 + (c >> 2)][(c & 3) * 4 + 2 * i + 1] = 0.f;
        }
      }
#pragma unroll
      for (int g = 0; g < PF; ++g) wld(g);
      SB0();
#pragma unroll
      for (int j = 0; j < 32; ++j) {
        if (j == 30) wvld();
#pragma unroll
        for (int n = 0; n < 4; ++n) {
          const int g = j * 4 + n;
          a2[n] = mfma32(cat(wl[g & 7], wh[g & 7]), of[j][n >> 1], a2[n]);
          SB0();
          if (g + PF < 128) wld(g + PF);
          {
            constexpr int RAMP = OW - OW0;
            const int q = j < RAMP ? 2 * OW0 + 4 * j + n : (n & 1) ? -1 : 2 * (j + OW) + (n >> 1);
            if (q >= 0 && q < 64) oload1(q >> 1, q & 1);
          }
          if (n == 0 && j + OW == 32) misc[20 + wave] = 1;
          SB0();
          asm volatile("" :: "v"(wl[g & 7]), "v"(wh[g & 7]), "v"(of[j][n >> 1]));
          SB0();
        }
      }
      STAMPT(0, 8, 0); STAMPT(3, 1, 64); STAMPT(3, 2, 128); STAMPT(3, 3, 192);
      a2[0] += a2[2]; a2[1] += a2[3];
      misc[8 + wave] = 1;
      unsigned hb[16];
      {
        float s[2] = {0.f, 0.f}, sq[2] = {0.f, 0.f}, pw[2] = {0.f, 0.f};
#pragma unroll
        for (int i = 0; i < 16; ++i) {
          const unsigned t = pack2(a2[i >> 3][(i & 7) * 2], a2[i >> 3][(i & 7) * 2 + 1]);
          hb[i] = t;
          s[i & 1] = dot2(t, 0x3F803F80u, s[i & 1]); sq[i & 1] = dot2(t, t, sq[i & 1]); pw[i & 1] = dot2(t, wv[i >> 1][i & 1], pw[i & 1]);
        }
        const u32x4 st = {__float_as_uint(xh_sum(s[0] + s[1])), __float_as_uint(xh_sum(sq[0] + sq[1])), __float_as_uint(xh_sum(pw[0] + pw[1])), __float_as_uint(sw_)};
        if (h == 0) STW(seal(st), WS_ST2 + ((rp * 16 + cb) * 128 + row) * 16);
      }
      STAMPT(0, 9, 0);
      u32x4 pu_[8];
      __builtin_amdgcn_s_sleep(SLC0);
#pragma nounroll
      for (unsigned s_ = 0; s_ < SPIN_CAP; ++s_) {
        unsigned mk = 0u;
#pragma unroll
        for (int j = 0; j < 8; ++j) { pu_[j] = LDW(WS_ST2 + ((rp * 16 + h * 8 + j) * 128 + row) * 16); mk = max(max(mk, pu_[j][1]), pu_[j][3]); }
        if (!__any(mk == EMPTY)) break;
        asm volatile("" ::: "memory");
        __builtin_amdgcn_s_sleep(SLC1);
      }
      STAMPT(0, 10, 0);
      f32x4 tt = {0.f, 0.f, 0.f, 0.f};
#pragma unroll
      for (int j = 0; j < 8; ++j) tt += __builtin_bit_cast(f32x4, pu_[j]);
      const float mean = xh_sum(tt[0]) * (1.0f / 1024.0f);
      const float var = fmaxf(xh_sum(tt[1]) * (1.0f / 1024.0f) - mean * mean, 0.f);
      const float mb = rbf(mean), rs = __builtin_amdgcn_rcpf(rbf(__builtin_amdgcn_sqrtf(rbf(rbf(var) + 1e-5f))));
      float sc = rbf((xh_sum(tt[2]) - mb * xh_sum(tt[3])) * rs);
      if (!(sc == sc)) sc = -INFINITY;
      unsigned b0 = h ? 0u : mk_key(sc, rp * 128 + row), b1 = 0;
      top2_wave(b0, b1);
      const int pan = rp * 4 + wave;
      if (lane == 0) st64(ws, SY_FD + cb * 512 + pan * 8, ((u64)b1 << 32) | b0);
      STAMPT(1, 11, 0);
      unsigned nq[16];
#pragma unroll
      for (int i = 0; i < 16; ++i) {
        const unsigned dq = pack2(bflo(hb[i]) - mb, bfhi(hb[i]) - mb);
        nq[i] = pack2(bflo(dq) * rs, bfhi(dq) * rs);
      }
      const int lc0 = key_idx(b0) & 31, lc1 = key_idx(b1) & 31;
      {
        const int c0 = lc0, c1 = lc1;
        char* tb = smem + L_TB + wave * 512;
        if (s32 == c0 || s32 == c1) {
          char* tp = tb + (s32 == c0 ? 0 : 256) + h * 8;
#pragma unroll
          for (int c = 0; c < 8; ++c) { *(u32x2*)(tp + c * 16) = (u32x2){nq[2 * c], nq[2 * c + 1]}; *(u32x2*)(tp + 128 + c * 16) = (u32x2){hb[2 * c], hb[2 * c + 1]}; }
        }
        __builtin_amdgcn_wave_barrier();
        const u32x4 v4 = seal(*(const u32x4*)(tb + s32 * 16));
#pragma unroll
        for (int rr_ = 0; rr_ < XSR; rr_ += 2)
          if (rr_ + h < XSR) STW(v4, WS_XS + (rr_ + h) * 524288 + (pan * 2 + (s32 >> 4)) * 4096 + ((s32 >> 3) & 1) * 2048 + cb * 128 + (s32 & 7) * 16);
      }
      u64 cc = 0;
      if (wave == 0) cc = ld64(ws, SY_FD + cb * 512 + lane * 8);
#pragma unroll
      for (int c = 0; c < 8; ++c) {
        u32x2 ob;
#pragma unroll
        for (int i = 0; i < 2; ++i) ob[i] = pack2(bflo(hb[2 * c + i]) + bflo(nq[2 * c + i]), bfhi(hb[2 * c + i]) + bfhi(nq[2 * c + i]));
        *(LAS u32x2*)((LAS char*)(size_t)(xb + ((c ^ sw) * 16))) = ob;
      }
      auto ost = [&](int rl, bool on) {
        const int rw = wave * 32 + rl;
        const u32x4 v = *(const u32x4*)(smem + L_X2 + rw * 128 + (((lane & 7) ^ ((rw >> 1) & 7)) * 16));
        if (on) *(u32x4*)(out + (size_t)(rp * 128 + rw) * D_ + cb * 64 + (lane & 7) * 8) = v;
      };
      __builtin_amdgcn_wave_barrier();
      if (EOUT == 2 || (EOUT == 1 && wave != 0)) {
#pragma unroll
        for (int m = 0; m < 4; ++m) { const int rl = m * 8 + (lane >> 3); ost(rl, rl != lc0 && rl != lc1); }
      }
      if (wave == 0) {
#pragma nounroll
        for (unsigned s_ = 0; s_ < SPIN_CAP; ++s_) {
          if (__all((unsigned)(cc >> 32) != EMPTY)) break;
          __builtin_amdgcn_s_sleep(SLD1);
          cc = ld64(ws, SY_FD + cb * 512 + lane * 8);
        }
        STAMPT(1, 13, 0);
        unsigned c0 = (unsigned)cc, c1 = (unsigned)(cc >> 32);
        top2_wave(c0, c1);
        const unsigned w0 = key_idx(c0), w1 = key_idx(c1);
        const unsigned k0 = __builtin_amdgcn_readlane((unsigned)cc, w0 >> 5), k1 = __builtin_amdgcn_readlane((unsigned)cc, w1 >> 5);
        const unsigned sl0 = (w0 >> 5) * 2 + (k0 == c0 ? 0 : 1), sl1 = (w1 >> 5) * 2 + (k1 == c1 ? 0 : 1);
        if (lane == 0) { misc[1] = w0; misc[2] = w1; misc[3] = sl0; misc[4] = sl1; misc[5] = c0; misc[6] = c1; }
        if (has_mlp) {
          const unsigned xo = WS_XS + (idx & (XSR - 1)) * 524288u + (h ? sl1 : sl0) * 4096u + (fs & 3) * 512 + s32 * 16;
          u32x4 q;
#pragma nounroll
          for (unsigned s_ = 0; s_ < SPIN_CAP; ++s_) {
            q = LDW(xo);
            if (__all(max(q[1], q[3]) != EMPTY)) break;
            asm volatile("" ::: "memory");
            __builtin_amdgcn_s_sleep(XSL1);
          }
          *(u32x4*)(smem + L_XS + h * 528 + s32 * 16) = q;
        }
      }
      LBAR();
      STAMPT(0, 11, 0);
      const int sel0 = misc[1], sel1 = misc[2];
      if (EOUT == 2 || (EOUT == 1 && wave != 0)) {
        const int rl = (lane & 8) ? lc1 : lc0, grow = rp * 128 + wave * 32 + rl;
        ost(rl, lane < 16 && grow != sel0 && grow != sel1);
      } else {
#pragma unroll
        for (int m = 0; m < 4; ++m) { const int rl = m * 8 + (lane >> 3), grow = rp * 128 + wave * 32 + rl; ost(rl, grow != sel0 && grow != sel1); }
      }
      STAMPT(0, 12, 0);
      if (bid < 64 && wave == 0) {
        const int tk = bid >> 5, c0 = (bid & 31) * 32;
        const unsigned xo = WS_XS + (idx & (XSR - 1)) * 524288u + (unsigned)misc[3 + tk] * 4096u + c0 * 2 + (lane & 7) * 8;
        u64 xn, xh;
#pragma nounroll
        for (unsigned s_ = 0; s_ < SPIN_CAP; ++s_) {
          xn = ld64(ws, xo); xh = ld64(ws, xo + 2048);
          if (__all((unsigned)(xn >> 32) != EMPTY && (unsigned)(xh >> 32) != EMPTY)) break;
        }
        const float sg = rbf(__builtin_amdgcn_rcpf(1.0f + __builtin_amdgcn_exp2f(key_val((unsigned)misc[5 + tk]) * -1.44269504088896340736f)));
        u16* op = out + (size_t)(tk ? sel1 : sel0) * D_ + c0 + (lane & 7) * 4;
        const unsigned yo = opq(WS_YP + (lane >> 3) * 8192 + tk * 4096 + c0 * 4 + (lane & 7) * 16);
#pragma nounroll
        for (unsigned s_ = 0; s_ < SPIN_CAP; ++s_) { if (__builtin_amdgcn_readfirstlane(misc[0])) break; __builtin_amdgcn_s_sleep(1); }
        __builtin_amdgcn_s_sleep(YSL0);
        u32x4 pv[7];
#pragma nounroll
        for (unsigned s_ = 0; s_ < SPIN_CAP; ++s_) {
          unsigned mk = 0u;
#pragma unroll
          for (int p = 0; p < 7; ++p) pv[p] = LDWS(yo, p * 65536);
#pragma unroll
          for (int p = 0; p < 7; ++p) mk = max(max(mk, pv[p][1]), pv[p][3]);
          if (!__any(mk == EMPTY)) break;
          asm volatile("" ::: "memory");
          __builtin_amdgcn_s_sleep(YSL1);
        }
        STAMPT(0, 15, 0);
        f32x4 y = __builtin_bit_cast(f32x4, pv[0]);
#pragma unroll
        for (int p = 1; p < 7; ++p) y += __builtin_bit_cast(f32x4, pv[p]);
        float t[4];
#pragma unroll
        for (int i = 0; i < 4; ++i) {
          float v = y[i]; v += dppf<DPP_ROR8>(v); v = xh_sum(x16_sum(v));
          const unsigned nn = (unsigned)(xn >> (32 * (i >> 1))), hh = (unsigned)(xh >> (32 * (i >> 1)));
          t[i] = ((i & 1) ? bfhi(hh) : bflo(hh)) + rbf(((i & 1) ? bfhi(nn) : bflo(nn)) + rbf(rbf(v) * sg));
        }
        if (lane < 8) *(u32x2*)op = (u32x2){pack2(t[0], t[1]), pack2(t[2], t[3])};
        asm volatile(".Lfused_end:" ::: "memory");
        STAMPT(0, 14, 0);
      }
    }
  } else {
    __builtin_amdgcn_s_setprio(2);
    const int hw = wave - 4, mt = tid & 255;
    u32x4 wgr[8], wur[8], wdr[8];
    const int krow = hw * 64 + (lane >> 3);
    const unsigned gof = opq((unsigned)(((fs & 3) * 256 + krow) * F_ + (fs >> 2) * 64 + (lane & 7) * 8) * 2u);
    const unsigned dof = opq((unsigned)(((fs >> 2) * 64 + (lane >> 3)) * D_ + (fs & 3) * 256 + hw * 64 + (lane & 7) * 8) * 2u);
    const unsigned wto = opq((unsigned)((idx * 32 + (mt >> 3)) * 2048 + (cb >> 2) * 512 + ((mt >> 1) & 3) * 128));
#define WLOAD(dst, vo, base) asm volatile("global_load_dwordx4 %0, %1, %2" WPOL : "=v"(dst) : "v"(vo), "s"(uni(base)) : "memory")
    auto wload = [&](int i) {
      if (i < 8) WLOAD(wgr[i], gof, wg + i * (8 * F_));
      else if (i < 16) WLOAD(wur[i - 8], gof, wu + (i - 8) * (8 * F_));
      else WLOAD(wdr[i - 16], dof, wd + (i - 16) * (8 * D_));
    };
    auto wpin = [&](int i) {
      if (i < 8) asm volatile("" : "+v"(wgr[i])); else if (i < 16) asm volatile("" : "+v"(wur[i - 8])); else asm volatile("" : "+v"(wdr[i - 16]));
    };
    const int pbi = lane >> 4, pq = (lane >> 2) & 3, pch = lane & 3;
    const unsigned voW = opq((unsigned)((8 * hw + 4 * (pbi >> 1) + pq) * D_ + (pbi & 1) * 32 + (pch ^ ((pbi >> 1) * 2)) * 8) * 2u);
    auto stageW = [&](int j) { glds16(voW, uni(wo + cb * 64) + (size_t)(j ^ hrot) * (32 * D_ * 2), lds0 + wslot(j) + hw * 1024); };
    u32x4 e4 = {EMPTY, EMPTY, EMPTY, EMPTY};
    asm volatile("" : "+v"(e4));
    const unsigned l16 = opq(lane * 16);
    if (hw < 2) {
      const char* baseA = uni(x + (size_t)rb * 128 * D_);
      const char* baseB = uni(wqkv + hp * 64);
      unsigned voA[2], voB[2];
      {
        const int r0 = 64 * hw + (lane >> 3), r1 = r0 + 8;
        voA[0] = (unsigned)(r0 * D_ + ((lane & 7) ^ ((r0 >> 1) & 7)) * 8) * 2u;
        voA[1] = (unsigned)(r1 * D_ + ((lane & 7) ^ ((r1 >> 1) & 7)) * 8) * 2u;
        voB[0] = (unsigned)((32 * hw + 4 * (pbi >> 1) + pq) * (3 * D_) + (pbi & 1) * 32 + pch * 8) * 2u;
        voB[1] = (unsigned)((32 * hw + 8 + 4 * (pbi >> 1) + pq) * (3 * D_) + (pbi & 1) * 32 + (pch ^ 2) * 8) * 2u;
      }
      auto stage = [&](int t) {
        const int kt = korder(t), b = t & 3;
        const char* pa = baseA + kt * 128;
        const char* pb = baseB + (size_t)kt * (64 * 3 * D_ * 2);
#pragma unroll
        for (int j = 0; j < 8; ++j) glds16(voA[j & 1] + (j >> 1) * (16 * D_ * 2), pa, lds0 + L_A + b * 16384 + (8 * hw + j) * 1024);
#pragma unroll
        for (int j = 0; j < 4; ++j) {
          const unsigned vo = voB[j & 1] + (j >> 1) * (16 * 3 * D_ * 2);
          glds16(vo + 2048, pb, lds0 + L_KV + b * 16384 + (4 * hw + j) * 1024);
          glds16(vo + 4096, pb, lds0 + L_KV + b * 16384 + 8192 + (4 * hw + j) * 1024);
          glds16(vo, pb, lds0 + L_Q + b * 8192 + (4 * hw + j) * 1024);
        }
      };
      WAITV(5); BAR();
      stage(2); stage(3);
      WAITV(40); BAR();
      for (int t = 0; t < 12; ++t) { WAITV(20); BAR(); stage(t + 4); }
      WAITV(20); BAR();
      asm volatile("s_waitcnt vmcnt(0)" :: "v"(e0) : "memory");
      BAR();
    } else {
      const int aw = hw - 2, at = tid & 127;
      const bool master = bid == 0 && aw == 0;
      unsigned hs_cur = 0, hs_g0 = 0;
      u32x4 ms_exp = {0, 0, 0, 0}, pm = {0, 0, 0, 0};
      const unsigned p_off = opq(master ? SY_RE + lane * 16 : (lane == 1 ? SY_GO : SY_CH) + bid * 4);
      const char* wsu = uni(ws);
#define HS_PROBE() asm volatile("global_load_dwordx4 %0, %1, %2 sc1" : "+v"(pm) : "v"(p_off), "s"(wsu) : "memory")
#define HS_WAIT() asm volatile("s_waitcnt vmcnt(0)" : "+v"(pm) :: "memory")
      bool done = false;
      auto hs_check = [&]() __attribute__((always_inline)) {
        HS_WAIT();
        if (!master) {
          const unsigned c = __builtin_amdgcn_readlane(pm[0], 0), go = __builtin_amdgcn_readlane(pm[0], 1);
          if (go != hs_g0) done = true;
          else if (c != hs_cur) { hs_cur = c; st32(ws, SY_RE + bid * 4, c); }
        } else if (!done && __all(pm[0] == ms_exp[0] && pm[1] == ms_exp[1] && pm[2] == ms_exp[2] && pm[3] == ms_exp[3])) {
          STW(ms_exp, SY_GO + lane * 16);
          done = true;
        }
      };
      const bool prober = master || aw == 1;
      const int kq = lane >> 4, r = lane & 15;
      constexpr unsigned OOB = 0x80000000u;
      const unsigned ko = WS_KVP + (hp * 16 + rb) * KVB + aw * 2176;
      const unsigned vm0 = opq(lane < 32 ? rs_o + aw * 512 + lane * 16 : lane < 56 ? cs_o + aw * 384 + (lane - 32) * 16 : ko + 2048 + (lane - 56) * 16);
      WAITV(5); BAR();
      u32x4 g0;
      if (master) g0 = LDW(SY_GO + lane * 16);
      u32x2 rst[4];
      {
        u32x4 a[4][2];
#pragma unroll
        for (int i = 0; i < 4; ++i)
#pragma unroll
          for (int k = 0; k < 2; ++k) a[i][k] = *(const u32x4*)(smem + L_A + (64 * aw + 16 * i + r) * 128 + (((4 * k + kq) ^ ((r >> 1) & 7)) * 16));
        const f32x4 Z4 = {0.f, 0.f, 0.f, 0.f};
        const int l3 = lane & 3;
#pragma unroll
        for (int i = 0; i < 4; ++i) {
          const f32x4 s1 = mfma16(ONES4, a[i][1], mfma16(ONES4, a[i][0], Z4)), s2 = mfma16(a[i][1], a[i][1], mfma16(a[i][0], a[i][0], Z4));
          const float q = l3 == 0 ? s2[0] : l3 == 1 ? s2[1] : l3 == 2 ? s2[2] : s2[3];
          rst[i] = (u32x2){min(__float_as_uint(s1[0]), 0xFFFFFFFEu), min(__float_as_uint(q), 0xFFFFFFFEu)};
          asm volatile("" : "+v"(rst[i]));
        }
      }
      const unsigned vm1 = opq(aw ? (lane < 2 ? SY_FD + cb * 512 + rp * 32 + lane * 16 : lane == 2 ? SY_FB + rb * 256 + hp * 16 : OOB)
                                  : (has_mlp ? WS_GU + fs * 1024 + lane * 16 : OOB));
      const unsigned vrs = opq((r >> 2) == kq ? rs_o + (64 * aw + r) * 8 : OOB);
      const unsigned vc0 = opq(cs_o + aw * 384 + lane * 4), vc1 = opq(lane < 32 ? cs_o + aw * 384 + 256 + lane * 4 : OOB);
      const unsigned sg_off = opq(aw ? WS_CS + hp * (16 * 768) + min(lane, 47) * 16 : WS_RS + rb * 16384 + lane * 16), sg_st = aw ? 768 : 1024;
      unsigned cb_[2][2];
#pragma unroll
      for (int c = 0; c < 2; ++c) {
        const unsigned o_ = lds0 + kq * 1024 + aw * 256 + (r >> 2) * 64 + ((c ^ (kq & 1)) * 32) + (lane & 3) * 8;
        cb_[0][c] = opq(o_ + L_Q + (tcs & 3) * 8192); cb_[1][c] = opq(o_ + L_KV + (tcs & 3) * 16384);
      }
      const int hq = (3 - (int)((size_t)wqkv >> 7) - (hp & ~3)) & 7;
      const int qln = ((((hp & 3) - hq - 1) & 3) * 8 + (idx & 7)) * 96 + min(at, 95);
      const unsigned vq = opq((unsigned)((((kph + 4) * 64 + qln / 3) & 1023) * 6144 + (qln % 3) * 2048 + ((hp & ~3) + (hq & 3)) * 128));
      const unsigned vcp = opq(min((unsigned)(idx * CODE_PER + min(at >> 3, CODE_PER - 1)) * 128u, clen));
      u32x4 e1;
      asm volatile("s_waitcnt vmcnt(0)\n\tv_mov_b32 %0, %4\n\tv_mov_b32 %1, %5\n\tv_mov_b32 %2, %6\n\tv_mov_b32 %3, %7"
                   : "=&v"(e1[0]), "=&v"(e1[1]), "=&v"(e1[2]), "=&v"(e1[3]) : "v"(e0[0]), "v"(e0[1]), "v"(e0[2]), "v"(e0[3]) : "memory");
      hs_g0 = __builtin_amdgcn_readfirstlane(e1[0]);
      BAR();
      if (master) {
#pragma unroll
        for (int j = 0; j < 4; ++j) { const unsigned c = e1[j] + 1u; ms_exp[j] = (c == g0[j]) ? c + 1u : c; }
        STW(ms_exp, SY_CH + lane * 16);
      }
      STW(e4, vm0); STW(e4, vm1); STWS(e4, l16, ko); STWS(e4, l16, ko + 1024);
      unsigned qt = 0;
      if (hq < 4 && (hp & 3) != hq) asm volatile("global_load_dword %0, %1, %2" : "=v"(qt) : "v"(vq), "s"(uni(wqkv)) : "memory");
      BAR();
      STAMPT(2, 0, 384);
      BAR();
      STAMPT(2, 1, 384);
      if (aw == 1) HS_PROBE();
      HS_WAIT();
      asm volatile("" :: "v"(qt));
      BAR();
      STAMPT(2, 2, 384);
      if (aw == 1) { hs_cur = __builtin_amdgcn_readlane(pm[0], 0); st32(ws, SY_RE + bid * 4, hs_cur); }
#pragma unroll
      for (int i = 0; i < 4; ++i) __builtin_amdgcn_raw_buffer_store_b64(rst[i], wsr, vrs + i * 128, 0, 16);
      unsigned cv0, cv1;
      {
        f32x4 cacc[6];
#pragma unroll
        for (int f = 0; f < 6; ++f) {
          cacc[f] = (f32x4){0.f, 0.f, 0.f, 0.f};
#pragma unroll
          for (int k = 0; k < 2; ++k) {
            const int o_ = (f >= 4 ? 8192 : 0) + k * 4096;
            cacc[f] = mfma16(ONES4, cat(trread(cb_[f >= 2][f & 1], o_), trread(cb_[f >= 2][f & 1], o_ + 512)), cacc[f]);
          }
        }
        cv0 = min(__float_as_uint(kq == 0 ? cacc[0][0] : kq == 1 ? cacc[1][0] : kq == 2 ? cacc[2][0] : cacc[3][0]), 0xFFFFFFFEu);
        cv1 = min(__float_as_uint(kq == 0 ? cacc[4][0] : cacc[5][0]), 0xFFFFFFFEu);
        asm volatile("" : "+v"(cv0), "+v"(cv1));
      }
      BAR();
      STAMPT(2, 3, 384);
      if (master) HS_PROBE();
      __builtin_amdgcn_raw_buffer_store_b32(cv0, wsr, vc0, 0, 16); __builtin_amdgcn_raw_buffer_store_b32(cv1, wsr, vc1, 0, 16);
      STWS(e4, l16, WS_ST2 + (rp * 16 + cb) * 2048 + aw * 1024);
      BAR();
      STAMPT(2, 4, 384);
      if (master) { hs_check(); HS_PROBE(); }
      BAR();
      STAMPT(2, 5, 384);
      if (master) { hs_check(); HS_PROBE(); }
      BAR();
      STAMPT(2, 6, 384);
      if (master) hs_check();
      if (prober) HS_PROBE();
      BAR();
      STAMPT(2, 7, 384);
      if (prober) {
        hs_check();
#pragma nounroll
        for (unsigned s_ = 0; s_ < SPIN_CAP && !done; ++s_) { HS_PROBE(); hs_check(); }
      }
      BAR();
      STAMPT(2, 8, 384);
      unsigned cpf = 0;
      if (CPF2) asm volatile("global_load_dword %0, %1, %2" : "=v"(cpf) : "v"(vcp), "s"(pc0) : "memory");
      u32x4 sg[16], wrv;
      asm volatile("global_load_dwordx4 %0, %1, %2" : "=v"(wrv) : "v"(l16 & 127u), "s"(uni(w_r + cb * 64)) : "memory");
#pragma unroll
      for (int i = 0; i < 16; ++i) {
        asm volatile("global_load_dwordx4 %0, %1, %2 sc1" : "=v"(sg[i]) : "v"(sg_off), "s"(wsu + i * sg_st) : "memory");
        if (i == SGS) { BAR(); STAMPT(2, 9, 384); }
      }
      auto tsum = [&](int o) __attribute__((always_inline)) {
        f32x4 t[4];
#pragma unroll
        for (int i = 0; i < 4; ++i) t[i] = add4(__builtin_bit_cast(f32x4, sg[o + i]), __builtin_bit_cast(f32x4, sg[o + 4 + i]));
        return add4(add4(t[0], t[2]), add4(t[1], t[3]));
      };
      WAITV(8);
#pragma unroll
      for (int i = 0; i < 8; ++i) asm volatile("" : "+v"(sg[i]));
      asm volatile("" : "+v"(wrv) : "v"(cpf));
      f32x4 sm = tsum(0), sn;
      asm volatile("" : "+v"(sm));
      BAR();
      STAMPT(2, 10, 384);
      WAITV(0);
#pragma unroll
      for (int i = 8; i < 16; ++i) asm volatile("" : "+v"(sg[i]));
      sn = tsum(8);
      asm volatile("" : "+v"(sn));
      BAR();
      STAMPT(2, 11, 384);
      sm = add4(sm, sn);

      const float chk = (sm[0] + sm[1]) + (sm[2] + sm[3]);
      if (__builtin_expect(__any(chk != chk), 0)) {
#pragma nounroll
        for (unsigned s_ = 0; s_ < SPIN_CAP; ++s_) {
          unsigned mx = 0;
#pragma unroll
          for (int i = 0; i < 16; ++i) { sg[i] = LDWS(sg_off, i * sg_st); mx = max(max(mx, max(sg[i][0], sg[i][1])), max(sg[i][2], sg[i][3])); }
          if (!__any(mx == EMPTY)) break;
        }
        sm = add4(tsum(0), tsum(8));
      }
      STAMPT(1, 1, 384);
      float mean[2], vr[2];
#pragma unroll
      for (int e = 0; e < 2; ++e) {
        mean[e] = sm[2 * e] * (1.0f / 1024.0f);
        vr[e] = fmaxf(sm[2 * e + 1] * (1.0f / 1024.0f) - mean[e] * mean[e], 0.f);
        asm volatile("" : "+v"(mean[e]), "+v"(vr[e]));
      }
      BAR();
      STAMPT(2, 12, 384);
      if (aw == 1) { if (lane < 48) *(f32x4*)(smem + L_CS + lane * 16) = sm; else *(LAS u32x2*)(misc + (lane - 48) * 2) = (u32x2){0u, 0u}; }
#pragma unroll
      for (int e = 0; e < 2; ++e) { vr[e] = __builtin_amdgcn_sqrtf(rbf(rbf(vr[e]) + 1e-5f)); mean[e] = -rbf(mean[e]); asm volatile("" : "+v"(mean[e]), "+v"(vr[e])); }
      if (aw == 1) *(u32x4*)(smem + L_WR + (l16 & 127u)) = wrv;
      LBAR();
      STAMPT(2, 13, 384);
      if (aw == 0) {
        const f32x2 ra = {__builtin_amdgcn_rcpf(rbf(vr[0])), __builtin_amdgcn_rcpf(rbf(vr[1]))};
        *(f32x2*)(smem + L_SA + lane * 8) = ra; *(f32x2*)(smem + L_SB + lane * 8) = (f32x2){mean[0] * ra[0], mean[1] * ra[1]};
        asm volatile("s_waitcnt lgkmcnt(0)" ::: "memory");
        misc[7] = 1;
      }
      STAMPT(1, 2, 384);
    }
    unsigned wt;
    asm volatile("global_load_dword %0, %1, %2" : "=v"(wt) : "v"(wto), "s"(uni(wo)) : "memory");
#pragma unroll
    for (int i = 0; i < NW1; ++i) { WAITV(THR - 1); wload(i); }
    u32x4 wr_[20];
#define RLOAD(dst, vo, base) asm volatile("global_load_dwordx4 %0, %1, %2" : "=v"(dst) : "v"(vo), "s"(uni(base)) : "memory")
#pragma unroll
    for (int i = 0; i < NR; ++i) {
      WAITV(THR - 1);
      RLOAD(wr_[i], voW, uni(wo + cb * 64) + (size_t)((i + 12) ^ hrot) * (32 * D_ * 2));
      if (i % 6 == 0) STAMPT(3, 8 + i / 6, 256);
    }
#pragma nounroll
    for (unsigned s_ = 0; s_ < SPIN_CAP; ++s_) if (__all(misc[28 + (lane & 3)] != 0)) break;
#pragma unroll
    for (int j = 0; j < NE; ++j) { WAITV(THR - 1); stageW(j); }
    WAITV(0);
#pragma unroll
    for (int i = 0; i < NR; ++i) asm volatile("" : "+v"(wr_[i]));
#pragma unroll
    for (int i = 0; i < NW1; ++i) wpin(i);
    asm volatile("" :: "v"(wt));
    STAMPT(1, 3, 256);
    {
      u32x4 pv[16], zq;
      const unsigned ko = WS_KVP + hp * 16 * KVB + (hw >> 1) * 2176;
      unsigned kl = ko + (hw & 1) * 1024 + lane * 16, zo = ko + ((hw & 1) * 8 + (lane >> 3)) * KVB + 2048 + (lane & 7) * 16;
      u32x4 sel[2];
#pragma unroll
      for (int e = 0; e < 2; ++e)
#pragma unroll
        for (int i = 0; i < 4; ++i)
          sel[e][i] = ((lane & 15) >> 2) != (lane >> 4) || (i >> 1) != e || (i & 1) != ((lane & 3) >> 1) ? 0u : (lane & 1) ? 0x3F800000u : 0x3F80u;
      f32x4 s[2][2];
      auto ksum = [&]() __attribute__((always_inline)) {
#pragma unroll
        for (int p = 0; p < 16; ++p) {
#pragma unroll
          for (int e = 0; e < 2; ++e) s[p >> 3][e] = mfma16(sel[e], pv[p], (p & 7) ? s[p >> 3][e] : (f32x4){0.f, 0.f, 0.f, 0.f});
          SB0();
        }
        s[0][0] += s[1][0]; s[0][1] += s[1][1];
      };
      asm volatile("" : "+v"(sel[0]), "+v"(sel[1]), "+v"(kl), "+v"(zo));
#pragma nounroll
      for (unsigned s_ = 0; s_ < SPIN_CAP; ++s_) { if (__builtin_amdgcn_readfirstlane(misc[16 + hw]) == 2) break; __builtin_amdgcn_s_sleep(1); }
      zq = LDW(zo);
#pragma unroll
      for (int p = 0; p < 16; ++p) { pv[p] = LDWS(kl + (p & 7) * KVB, ((p >> 3) ^ (idx & 1)) * (8 * KVB)); SB0(); }
      ksum();
      const float chk = s[0][0][0] + __uint_as_float(zq[1]) + __uint_as_float(zq[3]);
      if (__any(chk != chk)) {
        unsigned need = 0x1FFFFu;
#pragma nounroll
        for (unsigned s_ = 0; s_ < SPIN_CAP; ++s_) {
#pragma unroll
          for (int p = 0; p < 16; ++p) if ((need & (1u << p)) && !__any(max(pv[p][1], pv[p][3]) == EMPTY)) need &= ~(1u << p);
          if (!__any(max(zq[1], zq[3]) == EMPTY)) need &= ~0x10000u;
          if (!need) break;
          asm volatile("" ::: "memory");
#pragma unroll
          for (int p = 0; p < 16; ++p) if (need & (1u << p)) pv[p] = LDWS(kl + (p & 7) * KVB, ((p >> 3) ^ (idx & 1)) * (8 * KVB));
          if (need & 0x10000u) zq = LDW(zo);
        }
        ksum();
      }
      STAMPT(1, 4, 256);
      *(u32x4*)(smem + L_KVL + hw * 1024 + lane * 16) =
          (u32x4){pack2(s[0][0][0], s[0][0][1]), pack2(s[0][0][2], s[0][0][3]), pack2(s[0][1][0], s[0][1][1]), pack2(s[0][1][2], s[0][1][3])};
      f32x4 z = __builtin_bit_cast(f32x4, zq);
#pragma unroll
      for (int i = 0; i < 4; ++i) { float v = z[i]; v += dppf<DPP_ROR8>(v); z[i] = xh_sum(x16_sum(v)); }
      if (lane < 8) *(f32x4*)(smem + L_ZL + hw * 128 + lane * 16) = z;
    }
    LBAR();
#pragma unroll
    for (int j = NE; j < 32; ++j) if (j < 12 || j >= NR + 12) { WAITV(THR3 - 1); stageW(j); }
#pragma nounroll
    for (unsigned s_ = 0; s_ < SPIN_CAP; ++s_) { if (__all(misc[24 + (lane & 3)] != 0)) break; __builtin_amdgcn_s_sleep(1); }
#pragma unroll
    for (int i = 0; i < NR; ++i) *(u32x4*)(smem + wslot(i + 12) + hw * 1024 + lane * 16) = wr_[i];
    asm volatile("s_waitcnt lgkmcnt(0)" ::: "memory");
    {
      const u32x4 e5 = {EMPTY, EMPTY, EMPTY, EMPTY};
      if (has_mlp && hw >= 2) STW(e5, WS_YP + (fs >> 2) * 8192 + (hw & 1) * 4096 + (fs & 3) * 1024 + lane * 16);
#pragma unroll
      for (int rr_ = 0; rr_ < XSR; rr_ += 2)
        if (rr_ + (hw >> 1) < XSR) STW(e5, WS_XS + (rr_ + (hw >> 1)) * 524288 + (rp * 16 + ((mt >> 3) & 15)) * 2048 + cb * 128 + (mt & 7) * 16);
    }
    WAITV(0);
    STAMPT(1, 6, 256);
    if (hw == 0) {
      __builtin_amdgcn_s_sleep(SLB);
#pragma nounroll
      for (unsigned s_ = 0; s_ < SPIN_CAP; ++s_) if (__all(ld32(ws, SY_FB + rp * 256 + lane * 4) != EMPTY)) break;
    }
    BAR();
#pragma nounroll
    for (unsigned s_ = 0; s_ < SPIN_CAP; ++s_) { if (__builtin_amdgcn_readfirstlane(misc[20 + hw])) break; __builtin_amdgcn_s_sleep(2); }
#pragma unroll
    for (int i = NW1; i < 24; ++i) { WAITV(THR2 - 1); wload(i); }
    WAITV(0);
#pragma unroll
    for (int i = NW1; i < 24; ++i) wpin(i);
    STAMPT(1, 7, 256);
    const int c16 = lane & 15, kq = lane >> 4;
    u32x4 gf[2][8];
    u32x4 df[4][2];
    {
#pragma nounroll
      for (unsigned s_ = 0; s_ < SPIN_CAP; ++s_) { if (__all(misc[8 + (lane & 3)] != 0)) break; __builtin_amdgcn_s_sleep(1); }
      STAMPT(3, 5, 256);
#pragma unroll
      for (int i = 0; i < 8; ++i) {
        const int go = hw * 8192 + i * 1024 + ((lane ^ (((lane >> 4) & 1) * 2 + (i & 1) * 4)) * 16);
        *(u32x4*)(smem + go) = wgr[i];
        *(u32x4*)(smem + 32768 + go) = wur[i];
        *(u32x4*)(smem + L_DW + hw * 8192 + i * 1024 + ((lane ^ (((lane >> 4) & 3) * 2)) * 16)) = wdr[i];
      }
      asm volatile("s_waitcnt lgkmcnt(0)" ::: "memory");
      STAMPT(3, 6, 256);
      misc[12 + hw] = 1;
#pragma nounroll
      for (unsigned s_ = 0; s_ < SPIN_CAP; ++s_) if (__all(misc[12 + (lane & 3)] != 0)) break;
      asm volatile("" ::: "memory");
      STAMPT(3, 7, 256);
      const unsigned gb = opq(lds0 + (kq * 8 + (c16 >> 2)) * 128 + (hw ^ ((c16 >> 3) + (kq & 1) * 2)) * 32 + (lane & 3) * 8);
      const unsigned db = lds0 + L_DW + hw * 8192 + (kq * 4 + (c16 >> 2)) * 128 + (lane & 3) * 8, dsw = (c16 >> 3) + (kq & 1) * 2;
#pragma unroll
      for (int m = 0; m < 2; ++m)
#pragma unroll
        for (int k = 0; k < 8; ++k) gf[m][k] = cat(trread(gb, m * 32768 + k * 4096), trread(gb, m * 32768 + k * 4096 + 512));
#pragma unroll
      for (int t = 0; t < 4; ++t) {
        const unsigned dt = opq(db + (t ^ dsw) * 32);
#pragma unroll
        for (int e = 0; e < 2; ++e) df[t][e] = cat(trread(dt, ((t + RSG * 2 * e) & 3) * 2048), trread(dt, ((t + RSG * (2 * e + 1)) & 3) * 2048));
      }
      SB0();
#pragma unroll
      for (int k = 0; k < 8; ++k) asm volatile("" : "+v"(gf[0][k]), "+v"(gf[1][k]), "+v"(df[k >> 1][k & 1]));
    }
    const unsigned gu_st = opq(c16 < 4 ? WS_GU + fs * 1024 + hw * 256 + c16 * 64 + kq * 16 : 0x80000000u);
    const unsigned gu_ld = opq(WS_GU + (fs & ~3) * 1024 + c16 * 64 + kq * 16);
    const unsigned yp_st = opq((c16 & 2) ? 0x80000000u : WS_YP + (fs >> 2) * 8192 + (c16 & 1) * 4096 + (fs & 3) * 1024 + hw * 256 + (c16 >> 2) * 64 + kq * 16);
    const unsigned xsb = opq(lds0 + L_XS + (c16 & 1) * 528 + kq * 16);
    STAMPT(1, 8, 256);
    BAR();
    if (has_mlp) {
      f32x4 ga[2] = {{0.f, 0.f, 0.f, 0.f}, {0.f, 0.f, 0.f, 0.f}};
      u32x4 xf[8];
#pragma unroll
      for (int k = 0; k < 8; ++k) xf[k] = ldsr(xsb, k * 64);
#pragma unroll
      for (int k = 0; k < 8; ++k) { ga[0] = mfma16(gf[0][k], xf[k], ga[0]); ga[1] = mfma16(gf[1][k], xf[k], ga[1]); }
      STW(seal(__builtin_bit_cast(u32x4, (c16 & 2) ? ga[1] : ga[0])), gu_st);
      STAMPT(1, 14, 256);
      __builtin_amdgcn_s_sleep(SLG0);
      u32x4 q[4];
#pragma nounroll
      for (unsigned s_ = 0; s_ < SPIN_CAP; ++s_) {
        unsigned mk = 0u;
#pragma unroll
        for (int p = 0; p < 4; ++p) q[p] = LDW(gu_ld + p * 1024);
#pragma unroll
        for (int p = 0; p < 4; ++p) mk = max(max(mk, q[p][1]), q[p][3]);
        if (!__any(mk == EMPTY)) break;
        asm volatile("" ::: "memory");
        __builtin_amdgcn_s_sleep(SLG1);
      }
      STAMPT(1, 15, 256);
      const f32x4 tot = (__builtin_bit_cast(f32x4, q[0]) + __builtin_bit_cast(f32x4, q[1])) + (__builtin_bit_cast(f32x4, q[2]) + __builtin_bit_cast(f32x4, q[3]));
      float av[4];
#pragma unroll
      for (int i = 0; i < 4; ++i) {
        const float v = rbf(tot[i]), o_ = dppf<DPP_X2>(v);
        const float gs = (c16 & 2) ? o_ : v, us = (c16 & 2) ? v : o_;
        av[i] = rbf(gs * __builtin_amdgcn_rcpf(1.0f + __builtin_amdgcn_exp2f(gs * -1.44269504088896340736f))) * us;
      }
      const unsigned a0 = pack2(av[0], av[1]), a1 = pack2(av[2], av[3]);
      const u32x4 b0 = {a0, a1, dppu<DPP_ROR4>(a0), dppu<DPP_ROR4>(a1)}, b1 = {dppu<DPP_ROR8>(a0), dppu<DPP_ROR8>(a1), dppu<DPP_ROR12>(a0), dppu<DPP_ROR12>(a1)};
      f32x4 y[4];
#pragma unroll
      for (int t = 0; t < 4; ++t) y[t] = mfma16(df[t][0], b0, (f32x4){0.f, 0.f, 0.f, 0.f});
#pragma unroll
      for (int t = 0; t < 4; ++t) y[t] = mfma16(df[t][1], b1, y[t]);
      const int T = c16 >> 2;
      STW(seal(__builtin_bit_cast(u32x4, T == 0 ? y[0] : T == 1 ? y[1] : T == 2 ? y[2] : y[3])), yp_st);
      STAMPT(0, 13, 256);
      misc[0] = 1;
    }
  }
}

extern "C" void kernel_launch(void* const* d_in, const int* in_sizes, int n_in, void* d_out, int out_size,
                              void* d_ws, size_t ws_size, hipStream_t stream) {
  (void)hipFuncSetAttribute((const void*)fused, hipFuncAttributeMaxDynamicSharedMemorySize, LDS_TOTAL);
  fused<<<256, 512, LDS_TOTAL, stream>>>((const u16*)d_in[0], (const u16*)d_in[1], (const u16*)d_in[2], (const u16*)d_in[4],
                                         (const u16*)d_in[5], (const u16*)d_in[6], (const u16*)d_in[7], (u16*)d_out, (char*)d_ws);
}
```

```cpp
#include <hip/hip_runtime.h>
#include <stdint.h>

#define LAS __attribute__((address_space(3)))
#define GAS __attribute__((address_space(1)))
#define DI __device__ __forceinline__

#define XSR 1
#define GPF 5
#define GOW 14
#define NE 4
#define WPOL
#define HRM 3
#define OW0 4
#define SLC0 30
#define SLC1 8
#define SLD0 0
#define SLB1 0
#define CPF3 1
#define HRS 4
#define SLD1 0
#define SGS 7
#define CPF2 1
#define RSG 3
#define EOUT 0
#define GB 1
#define NR 20
#define THR3 4
#define THR2 6
#define THR 6
#define NW1 10
#define SLA 8
#define SLB 0
constexpr int CODE_PER = 11;
constexpr int XSL1 = 2;
#define YSL0 12
#define YSL1 4
#define SLG0 14
#define SLG1 2
typedef unsigned short u16;
typedef unsigned long long u64;
typedef __attribute__((ext_vector_type(2))) __bf16 bf16x2;
typedef __attribute__((ext_vector_type(8))) __bf16 bf16x8;
typedef __attribute__((ext_vector_type(4))) short s16x4;
typedef __attribute__((ext_vector_type(16))) float f32x16;
typedef __attribute__((ext_vector_type(4))) float f32x4;
typedef __attribute__((ext_vector_type(2))) float f32x2;
typedef __attribute__((ext_vector_type(4))) unsigned u32x4;
typedef __attribute__((ext_vector_type(2))) unsigned u32x2;
typedef GAS unsigned gu32;
typedef GAS unsigned long long gu64;

constexpr int S_ = 2048, D_ = 1024, F_ = 3584;

constexpr unsigned SY_CH = 0, SY_GO = 1024, SY_RE = 2048, SY_FB = 4096, SY_FD = 8192;
constexpr unsigned WS_RS  = 16384;
constexpr unsigned WS_CS  = WS_RS + 256 * 1024;
constexpr unsigned KVB    = 4352;
constexpr unsigned WS_KVP = WS_CS + 256 * 768;
constexpr unsigned WS_ST2 = WS_KVP + 256 * KVB;
constexpr unsigned WS_GU  = WS_ST2 + 256 * 2048;
constexpr unsigned WS_XS  = WS_GU + 256 * 1024;
constexpr unsigned WS_YP  = WS_XS + XSR * 524288;
constexpr unsigned WS_O   = WS_YP + 56 * 8192;
constexpr unsigned WS_END = WS_O + S_ * D_ * 2;
constexpr size_t WS_DIAG  = 64u << 20;
#define STAMPT(kid, i, T) do {} while (0)

DI float bflo(unsigned u) { return __uint_as_float(u << 16); }
DI float bfhi(unsigned u) { return __uint_as_float(u & 0xffff0000u); }
DI float rbf(float x) { return (float)(__bf16)x; }
DI unsigned pack2(float a, float b) { bf16x2 c; c[0] = (__bf16)a; c[1] = (__bf16)b; return __builtin_bit_cast(unsigned, c); }
DI f32x4 mfma16(u32x4 a, u32x4 b, f32x4 c) {
  return __builtin_amdgcn_mfma_f32_16x16x32_bf16(__builtin_bit_cast(bf16x8, a), __builtin_bit_cast(bf16x8, b), c, 0, 0, 0);
}
DI f32x16 mfma32(u32x4 a, u32x4 b, f32x16 c) {
  return __builtin_amdgcn_mfma_f32_32x32x16_bf16(__builtin_bit_cast(bf16x8, a), __builtin_bit_cast(bf16x8, b), c, 0, 0, 0);
}
DI u32x2 trread(const char* p) {
  s16x4 v = __builtin_amdgcn_ds_read_tr16_b64_v4i16((LAS s16x4*)p);
  return __builtin_bit_cast(u32x2, v);
}
DI unsigned opq(unsigned v) { asm volatile("" : "+v"(v)); return v; }
DI u32x2 trread(unsigned base, int off) {
  s16x4 v = __builtin_amdgcn_ds_read_tr16_b64_v4i16((LAS s16x4*)((LAS char*)(size_t)base + off));
  return __builtin_bit_cast(u32x2, v);
}
DI u32x4 ldsr(unsigned base, int off) { return *(const LAS u32x4*)((const LAS char*)(size_t)base + off); }
DI u32x4 cat(u32x2 a, u32x2 b) { return (u32x4){a[0], a[1], b[0], b[1]}; }
DI u32x4 pick(int c, u32x4 a, u32x4 b) { const unsigned m = c ? ~0u : 0u; return (a & m) | (b & ~m); }
DI f32x4 add4(f32x4 a, f32x4 b) {
  f32x4 c;
#pragma unroll
  for (int i = 0; i < 4; ++i) asm("v_add_f32 %0, %1, %2" : "=v"(c[i]) : "v"(a[i]), "v"(b[i]));
  return c;
}
DI float dot2(unsigned a, unsigned b, float c) {
  return __builtin_amdgcn_fdot2_f32_bf16(__builtin_bit_cast(bf16x2, a), __builtin_bit_cast(bf16x2, b), c, false);
}
DI const char* uni(const void* p) {
  const u64 v = (u64)p;
  return (const char*)(((u64)(unsigned)__builtin_amdgcn_readfirstlane((int)(v >> 32)) << 32) | (unsigned)__builtin_amdgcn_readfirstlane((int)v));
}
DI void glds16(unsigned voff, const void* sbase, unsigned lds) {
  asm volatile("s_mov_b32 m0, %2\n\ts_nop 0\n\tglobal_load_lds_dwordx4 %0, %1" ::"v"(voff), "s"(uni(sbase)), "s"(__builtin_amdgcn_readfirstlane(lds)) : "memory");
}
DI float xh_sum(float v) {
  auto r = __builtin_amdgcn_permlane32_swap(__float_as_uint(v), __float_as_uint(v), false, false);
  return __uint_as_float(r[0]) + __uint_as_float(r[1]);
}
template <int CTRL> DI unsigned dppu(unsigned v) { return (unsigned)__builtin_amdgcn_update_dpp(0, (int)v, CTRL, 0xf, 0xf, true); }
template <int CTRL> DI float dppf(float v) { return __uint_as_float(dppu<CTRL>(__float_as_uint(v))); }
constexpr int DPP_X1 = 0xB1, DPP_X2 = 0x4E, DPP_HMIR = 0x141, DPP_MIR = 0x140, DPP_ROR8 = 0x128, DPP_ROR4 = 0x124, DPP_ROR12 = 0x12C;
DI float oct_sum(float v) { v += dppf<DPP_X1>(v); v += dppf<DPP_X2>(v); v += dppf<DPP_HMIR>(v); return v; }
DI float x16_sum(float v) {
  auto r = __builtin_amdgcn_permlane16_swap(__float_as_uint(v), __float_as_uint(v), false, false);
  return __uint_as_float(r[0]) + __uint_as_float(r[1]);
}
DI unsigned phi_pair(f32x2 t) {
  const f32x2 u = t + 1.000001f;
  const f32x2 l = t * 1.44269504088896340736f;
  const f32x2 e = (f32x2){__builtin_amdgcn_exp2f(l[0]), __builtin_amdgcn_exp2f(l[1])} + 1e-6f;
  return pack2(__builtin_amdgcn_fmed3f(e[0], u[0], 1.000001f), __builtin_amdgcn_fmed3f(e[1], u[1], 1.000001f));
}
#define RLX_AGENT __ATOMIC_RELAXED, __HIP_MEMORY_SCOPE_AGENT
DI void st32(char* ws, unsigned off, unsigned v) { __hip_atomic_store((gu32*)(ws + off), v, RLX_AGENT); }
DI void st64(char* ws, unsigned off, u64 v) { __hip_atomic_store((gu64*)(ws + off), v, RLX_AGENT); }
DI unsigned ld32(char* ws, unsigned off) { return __hip_atomic_load((gu32*)(ws + off), RLX_AGENT); }
DI u64 ld64(char* ws, unsigned off) { return __hip_atomic_load((gu64*)(ws + off), RLX_AGENT); }
#define WAITV(n) asm volatile("s_waitcnt vmcnt(%0)" :: "n"(n) : "memory")
#define BAR() do { asm volatile("" ::: "memory"); __builtin_amdgcn_s_barrier(); asm volatile("" ::: "memory"); } while (0)
#define LBAR() do { asm volatile("s_waitcnt lgkmcnt(0)" ::: "memory"); __builtin_amdgcn_s_barrier(); asm volatile("" ::: "memory"); } while (0)
#define SB0() __builtin_amdgcn_sched_barrier(0)
constexpr unsigned SPIN_CAP = 1u << 22;
constexpr unsigned EMPTY = 0xFFFFFFFFu;
DI u32x4 seal(u32x4 v) { v[1] = min(v[1], 0xFFFFFFFEu); v[3] = min(v[3], 0xFFFFFFFEu); return v; }
DI unsigned mk_key(float v, unsigned idx) {
  const unsigned u = __float_as_uint(v) >> 16;
  const unsigned o = (u & 0x8000u) ? (~u & 0xffffu) : (u | 0x8000u);
  return (o << 16) | (0xFFFFu - idx);
}
DI unsigned key_idx(unsigned k) { return (0xFFFFu - (k & 0xFFFFu)) & (S_ - 1); }
DI float key_val(unsigned k) { const unsigned o = k >> 16; return __uint_as_float(((o & 0x8000u) ? (o & 0x7fffu) : (~o & 0xffffu)) << 16); }
DI void top2_mix(unsigned& b0, unsigned& b1, unsigned c0, unsigned c1) {
  const unsigned lo = min(b0, c0), mx = max(b1, c1);
  b0 = max(b0, c0); b1 = max(lo, mx);
}
DI void top2_wave(unsigned& b0, unsigned& b1) {
  top2_mix(b0, b1, dppu<DPP_X1>(b0), dppu<DPP_X1>(b1));
  top2_mix(b0, b1, dppu<DPP_X2>(b0), dppu<DPP_X2>(b1));
  top2_mix(b0, b1, dppu<DPP_HMIR>(b0), dppu<DPP_HMIR>(b1));
  top2_mix(b0, b1, dppu<DPP_MIR>(b0), dppu<DPP_MIR>(b1));
  { auto p = __builtin_amdgcn_permlane16_swap(b0, b0, false, false); auto q = __builtin_amdgcn_permlane16_swap(b1, b1, false, false);
    b0 = p[0]; b1 = q[0]; top2_mix(b0, b1, p[1], q[1]); }
  { auto p = __builtin_amdgcn_permlane32_swap(b0, b0, false, false); auto q = __builtin_amdgcn_permlane32_swap(b1, b1, false, false);
    b0 = p[0]; b1 = q[0]; top2_mix(b0, b1, p[1], q[1]); }
}

constexpr int L_A = 0, L_Q = 65536, L_KV = 98304, LDS_TOTAL = 163840;
constexpr int L_M  = L_KV;
constexpr int L_SA = L_M, L_SB = L_M + 512, L_CS = L_M + 1024;
constexpr int L_MISC = L_M + 1792;
constexpr int L_XCH = L_M + 2048;
constexpr int L_KVL = L_M + 11264;
constexpr int L_ZL = L_M + 15360;
constexpr int L_XS = L_M + 3072;
constexpr int L_RED = L_M + 5120;
constexpr int L_ACT = L_M + 9216;
constexpr int L_TB = L_M + 9472;
constexpr int L_WR = L_M + 15872;
constexpr int L_DW = 114688;
constexpr int L_X2 = 81920;
DI constexpr int wslot(int j) { return j < 12 ? 114688 + j * 4096 : (j - 12) * 4096; }

__global__ __launch_bounds__(512, 2) void fused(const u16* __restrict__ x, const u16* __restrict__ wqkv,
                                                const u16* __restrict__ wo, const u16* __restrict__ w_r,
                                                const u16* __restrict__ wg, const u16* __restrict__ wu,
                                                const u16* __restrict__ wd, u16* __restrict__ out, char* __restrict__ ws) {
  extern __shared__ __attribute__((aligned(16))) char smem[];
  u64 pc0; unsigned clen;
  asm volatile("s_getpc_b64 %0\n.Lfused_pc:\n\ts_mov_b32 %1, .Lfused_end-.Lfused_pc-16" : "=s"(pc0), "=s"(clen));
  const int tid = threadIdx.x, lane = tid & 63;
  const int wave = __builtin_amdgcn_readfirstlane(tid >> 6);
  STAMPT(0, 0, 0);
  const int bid = blockIdx.x, xcd = bid & 7, idx = bid >> 3;
  const int rb = (xcd & 1) * 8 + (idx & 7);
  const int hp = (xcd >> 1) * 4 + (idx >> 3);
  const int rp = rb, cb = ((3 + (xcd >> 1) + 2 * (xcd & 1)) & 3) * 4 + (hp & 3);
  const int hrot = HRS * (cb & HRM);
  const unsigned lds0 = (unsigned)(size_t)smem;
  const __amdgpu_buffer_rsrc_t wsr = __builtin_amdgcn_make_buffer_rsrc(ws, 0, WS_END, 0x00020000);
#define LDW(off) __builtin_amdgcn_raw_buffer_load_b128(wsr, (off), 0, 16)
#define LDWS(off, so) __builtin_amdgcn_raw_buffer_load_b128(wsr, (off), (so), 16)
#define STW(v, off) __builtin_amdgcn_raw_buffer_store_b128((v), wsr, (off), 0, 16)
#define STWS(v, off, so) __builtin_amdgcn_raw_buffer_store_b128((v), wsr, (off), (so), 16)
  const int kph = 4 * (xcd >> 1) + 8 * (xcd & 1), kg_ = (hp + rb) & 3;
  auto korder = [&](int t) { return ((t + kph) & 15) ^ kg_; };
  u32x4 e0;
  {
    const char* eb = wave < 4 ? (const char*)pc0 : uni(ws);
    const unsigned eo = wave < 4 ? min((unsigned)(idx * CODE_PER + min(tid >> 4, CODE_PER - 1)) * 128u, clen) : wave == 6 && bid == 0 ? SY_RE + lane * 16 : SY_GO + bid * 4;
    asm volatile("global_load_dwordx4 %0, %1, %2 sc1" : "=v"(e0) : "v"(eo), "s"(uni(eb)) : "memory");
  }
  {
    const int r0 = 16 * wave + (lane >> 3), r1 = r0 + 8, pb_ = lane >> 4;
    const unsigned a0 = (unsigned)(r0 * D_ + ((lane & 7) ^ ((r0 >> 1) & 7)) * 8) * 2u, a1 = (unsigned)(r1 * D_ + ((lane & 7) ^ ((r1 >> 1) & 7)) * 8) * 2u;
    const unsigned b0 = (unsigned)((8 * wave + 4 * (pb_ >> 1) + ((lane >> 2) & 3)) * (3 * D_) + (pb_ & 1) * 32 + ((lane & 3) ^ ((wave & 1) * 2)) * 8) * 2u;
    auto first = [&](int t, int dw) {
      const char* pa = uni(x + (size_t)(rb * 128 + 16 * dw) * D_) + korder(t) * 128;
      const char* pb = uni(wqkv + (size_t)(8 * dw) * (3 * D_) + hp * 64) + (size_t)korder(t) * (64 * 3 * D_ * 2);
      const int w = wave + dw;
      glds16(a0, pa, lds0 + L_A + t * 16384 + w * 2048); glds16(a1, pa, lds0 + L_A + t * 16384 + w * 2048 + 1024);
      glds16(b0 + 2048, pb, lds0 + L_KV + t * 16384 + w * 1024); glds16(b0 + 4096, pb, lds0 + L_KV + t * 16384 + 8192 + w * 1024);
      glds16(b0, pb, lds0 + L_Q + t * 8192 + w * 1024);
    };
    first(0, 0);
    BAR();
    if (wave < 6) first(1, 0);
    if (wave == 4 || wave == 5) first(1, 2);
    SB0();
  }
  volatile LAS int* misc = (volatile LAS int*)(size_t)(lds0 + L_MISC);
  const bool has_mlp = bid < 224;
  const int fs = has_mlp ? (bid & 7) * 28 + (bid >> 3) : (bid & 7) * 28;
  const int tcs = (rb & 4) ? 4 : 3;
  const unsigned rs_o = WS_RS + (rb * 16 + korder(0)) * 1024, cs_o = WS_CS + (hp * 16 + korder(tcs)) * 768;
  const u32x4 ONES4 = {0x3F803F80u, 0x3F803F80u, 0x3F803F80u, 0x3F803F80u};

  if (wave < 4) {
    const int wr = wave >> 1, wc = wave & 1;
    const int r = lane & 15, kq = lane >> 4;
    f32x4 acc[4][6];
    {
      unsigned aO[2], bQ[2], bK[2];
#pragma unroll
      for (int k = 0; k < 2; ++k) aO[k] = opq(lds0 + L_A + (64 * wr + r) * 128 + (((4 * k + kq) ^ ((r >> 1) & 7)) * 16));
#pragma unroll
      for (int c = 0; c < 2; ++c) {
        const unsigned o_ = lds0 + kq * 1024 + wc * 256 + (r >> 2) * 64 + ((c ^ (kq & 1)) * 32) + (lane & 3) * 8;
        bQ[c] = opq(o_ + L_Q); bK[c] = opq(o_ + L_KV);
      }
#pragma unroll
      for (int i = 0; i < 4; ++i)
#pragma unroll
        for (int j = 0; j < 6; ++j) acc[i][j] = (f32x4){0.f, 0.f, 0.f, 0.f};
      struct Fr { u32x4 a[4]; u32x2 bl[6], bh[6]; };
      Fr F[2];
      auto bread = [&](int f, int b, int k, int hi) {
        return f < 2 ? trread(bQ[f & 1], k * 4096 + b * 8192 + hi * 512) : trread(bK[f & 1], k * 4096 + b * 16384 + ((f >> 1) - 1) * 8192 + hi * 512);
      };
      auto step = [&](Fr& C, Fr& N, int b, int k, int MC, int ML) {
        int ld = 0;
        constexpr int ORD[6] = {2, 3, 4, 5, 0, 1};
        auto load1 = [&]() {
          if (ML == 0) return;
          if (ld < 4) N.a[ld] = ldsr(aO[k], b * 16384 + ld * 2048);
          else {
            const int e = ld - 4, f = ML == 1 ? (e >> 1) : ORD[(e >> 1) % 6], nf = ML == 1 ? 2 : ML == 2 ? 4 : 6;
            if ((e >> 1) < nf) { if (e & 1) N.bh[f] = bread(f, b, k, 1); else N.bl[f] = bread(f, b, k, 0); }
          }
          ++ld;
        };
#pragma unroll
        for (int m = 0; m < 24; ++m) {
          const int j = ORD[m >> 2], i = m & 3;
          if (!((j < 2 ? 1 : 2) & MC)) continue;
          if (j < 2) acc[i][j] = mfma16(cat(C.bl[j], C.bh[j]), C.a[i], acc[i][j]);
          else acc[i][j] = mfma16(C.a[i], cat(C.bl[j], C.bh[j]), acc[i][j]);
          load1();
          SB0();
        }
      };
      WAITV(5); BAR();
      STAMPT(0, 1, 0);
#pragma unroll
      for (int m = 0; m < 4; ++m) F[0].a[m] = ldsr(aO[0], m * 2048);
#pragma unroll
      for (int f = 0; f < 6; ++f) { F[0].bl[f] = bread(f, 0, 0, 0); F[0].bh[f] = bread(f, 0, 0, 1); }
      for (int t4 = 0; t4 < 12; t4 += 4) {
#pragma unroll
        for (int b = 0; b < 4; ++b) {
          step(F[0], F[1], b, 1, 3, 3);
          if (b == 0 && t4 == 0) { asm volatile("s_waitcnt vmcnt(0)" :: "v"(e0) : "memory"); STAMPT(3, 13, 0); BAR(); STAMPT(3, 14, 0); }
          step(F[1], F[0], (b + 1) & 3, 0, 3, 3);
          BAR();
        }
      }
#pragma unroll
      for (int b = 0; b < 4; ++b) {
        step(F[0], F[1], b, 1, 2, 2);
        step(F[1], F[0], (b + 1) & 3, 0, 2, b < 3 ? 2 : 0);
        if (b < 2) BAR();
      }
      misc[28 + wave] = 1;
#pragma nounroll
      for (unsigned s_ = 0; s_ < SPIN_CAP; ++s_) if (__builtin_amdgcn_readfirstlane(misc[7])) break;
      asm volatile("" ::: "memory");
      STAMPT(0, 2, 0);
      {
        const float* sa = (const float*)(smem + L_SA) + 64 * wr + 4 * kq;
        const float* cs = (const float*)(smem + L_CS) + wc * 96 + r;
        const float c4[4] = {cs[32], cs[48], cs[64], cs[80]};
        unsigned kp[4][2][2], vp[4][2][2];
#pragma unroll
        for (int i = 0; i < 4; ++i) {
          const f32x4 a4 = *(const f32x4*)(sa + 16 * i), b4 = *(const f32x4*)(sa + 128 + 16 * i);
          const f32x2 aa[2] = {{a4[0], a4[1]}, {a4[2], a4[3]}}, bb[2] = {{b4[0], b4[1]}, {b4[2], b4[3]}};
#pragma unroll
          for (int j = 0; j < 2; ++j)
#pragma unroll
            for (int e = 0; e < 2; ++e) {
              const f32x2 tk = (f32x2){acc[i][2 + j][2 * e], acc[i][2 + j][2 * e + 1]} * aa[e] + bb[e] * c4[j];
              const f32x2 tv = (f32x2){acc[i][4 + j][2 * e], acc[i][4 + j][2 * e + 1]} * aa[e] + bb[e] * c4[2 + j];
              kp[i][j][e] = phi_pair(tk);
              vp[i][j][e] = pack2(tv[0], tv[1]);
            }
        }
        f32x4 kv[2][2], zv[2];
#pragma unroll
        for (int d = 0; d < 2; ++d) {
          zv[d] = (f32x4){0.f, 0.f, 0.f, 0.f}; kv[d][0] = zv[d]; kv[d][1] = zv[d];
#pragma unroll
          for (int u = 0; u < 2; ++u) {
            const u32x4 ka = {kp[2 * u][d][0], kp[2 * u][d][1], kp[2 * u + 1][d][0], kp[2 * u + 1][d][1]};
#pragma unroll
            for (int e = 0; e < 2; ++e)
              kv[d][e] = mfma16(ka, (u32x4){vp[2 * u][e][0], vp[2 * u][e][1], vp[2 * u + 1][e][0], vp[2 * u + 1][e][1]}, kv[d][e]);
            zv[d] = mfma16(ka, ONES4, zv[d]);
          }
        }
        char* xo = smem + L_XCH + wave * 2048 + lane * 16;
        *(f32x4*)xo = wr ? kv[0][0] : kv[0][1];
        *(f32x4*)(xo + 1024) = wr ? kv[1][0] : kv[1][1];
        if (r < 2) *(f32x4*)(smem + L_XCH + 8192 + wave * 128 + r * 64 + kq * 16) = r ? zv[1] : zv[0];
#pragma unroll
        for (int m = 0; m < 4; ++m) F[0].a[m] = ldsr(aO[0], m * 2048);
#pragma unroll
        for (int f = 0; f < 2; ++f) { F[0].bl[f] = bread(f, 0, 0, 0); F[0].bh[f] = bread(f, 0, 0, 1); }
        STAMPT(1, 0, 0);
        asm volatile("s_waitcnt lgkmcnt(0)" ::: "memory");
        misc[16 + wave] = 1;
#pragma nounroll
        for (unsigned s_ = 0; s_ < SPIN_CAP; ++s_) if (__builtin_amdgcn_readfirstlane(misc[16 + (wave ^ 2)])) break;
        asm volatile("" ::: "memory");
        const char* xi = smem + L_XCH + (wave ^ 2) * 2048 + lane * 16;
        const f32x4 s0 = *(const f32x4*)xi + (wr ? kv[0][1] : kv[0][0]), s1 = *(const f32x4*)(xi + 1024) + (wr ? kv[1][1] : kv[1][0]);
        const unsigned ko = WS_KVP + (hp * 16 + rb) * KVB + wc * 2176;
        STW(seal((u32x4){pack2(s0[0], s0[1]), pack2(s0[2], s0[3]), pack2(s1[0], s1[1]), pack2(s1[2], s1[3])}), ko + wr * 1024 + lane * 16);
        if (wr == 0 && lane < 8) {
          const f32x4 z = *(const f32x4*)(smem + L_XCH + 8192 + wave * 128 + lane * 16) + *(const f32x4*)(smem + L_XCH + 8192 + (wave + 2) * 128 + lane * 16);
          STW(seal(__builtin_bit_cast(u32x4, z)), ko + 2048 + lane * 16);
        }
                STAMPT(0, 3, 0);
      }
#pragma unroll
      for (int b = 0; b < 4; ++b) {
        step(F[0], F[1], b, 1, 1, 1);
        step(F[1], F[0], (b + 1) & 3, 0, 1, b < 3 ? 1 : 0);
        if (b == GB) misc[16 + wave] = 2;
      }
    }
    u32x4 qp[4];
    {
      const float* sa = (const float*)(smem + L_SA) + 64 * wr + r;
      const float* cs = (const float*)(smem + L_CS) + wc * 96 + 4 * kq;
      const f32x4 c0 = *(const f32x4*)cs, c1 = *(const f32x4*)(cs + 16);
#pragma unroll
      for (int i = 0; i < 4; ++i) {
        const float a = sa[16 * i], b = sa[128 + 16 * i];
        qp[i][0] = phi_pair((f32x2){acc[i][0][0], acc[i][0][1]} * a + (f32x2){c0[0], c0[1]} * b);
        qp[i][1] = phi_pair((f32x2){acc[i][0][2], acc[i][0][3]} * a + (f32x2){c0[2], c0[3]} * b);
        qp[i][2] = phi_pair((f32x2){acc[i][1][0], acc[i][1][1]} * a + (f32x2){c1[0], c1[1]} * b);
        qp[i][3] = phi_pair((f32x2){acc[i][1][2], acc[i][1][3]} * a + (f32x2){c1[2], c1[3]} * b);
        asm volatile("" : "+v"(qp[i]));
      }
    }
    u32x2 xr[8];
    {
      const int row = wave * 32 + (lane & 31);
#pragma unroll
      for (int c = 0; c < 8; ++c) xr[c] = *(const u32x2*)(smem + L_A + ((cb & 3) ^ kg_) * 16384 + row * 128 + ((c ^ ((row >> 1) & 7)) * 16) + (lane >> 5) * 8);
    }
    STAMPT(0, 4, 0);
    LBAR();
    STAMPT(0, 5, 0);
    {
      const u32x4 k0 = *(const u32x4*)(smem + L_KVL + wc * 2048 + lane * 16), k1 = *(const u32x4*)(smem + L_KVL + wc * 2048 + 1024 + lane * 16);
      const float* zl = (const float*)(smem + L_ZL) + wc * 64 + kq * 4;
      const f32x4 za = *(const f32x4*)zl + *(const f32x4*)(zl + 32), zb = *(const f32x4*)(zl + 16) + *(const f32x4*)(zl + 48);
      const u32x4 zf = {pack2(za[0], za[1]), pack2(za[2], za[3]), pack2(zb[0], zb[1]), pack2(zb[2], zb[3])};
      const f32x4 Z4 = {0.f, 0.f, 0.f, 0.f};
      const unsigned oo = WS_O + ((rb * 8 + wr * 4) * 32 + 2 * hp + wc) * 1024 + lane * 16;
#pragma unroll
      for (int i = 0; i < 4; ++i) {
        const f32x4 dn = mfma16(zf, qp[i], Z4), x0 = mfma16(k0, qp[i], Z4), x1 = mfma16(k1, qp[i], Z4);
        const float ri = __builtin_amdgcn_rcpf(dn[0] + 1e-6f);
        STW(((u32x4){pack2(x0[0] * ri, x0[1] * ri), pack2(x0[2] * ri, x0[3] * ri), pack2(x1[0] * ri, x1[1] * ri), pack2(x1[2] * ri, x1[3] * ri)}),
            oo + i * 32768);
      }
      misc[24 + wave] = 1;
      WAITV(0);
      if (lane == 0) st32(ws, SY_FB + rb * 256 + hp * 16 + wave * 4, 1u);
    }
    STAMPT(0, 6, 0);
    {
      const int h = lane >> 5, s32 = lane & 31;
      u32x2 wv[8];
      auto wvld = [&]() {
#pragma unroll
        for (int c = 0; c < 8; ++c) wv[c] = *(const volatile LAS u32x2*)(size_t)(lds0 + L_WR + c * 16 + h * 8);
      };
      wvld();
      u32x4 of[32][2];
      const unsigned ol = WS_O + (rp * 8 + wave * 2 + (kq & 1)) * 32768 + (h * 16 + r) * 16;
      auto oload1 = [&](int j, int e) { of[j][e] = LDWS(ol + e * 512 + (j & (HRS - 1)) * 1024, ((j & ~(HRS - 1)) ^ hrot) * 1024); };
      auto oload = [&](int j) { oload1(j, 0); oload1(j, 1); };
      f32x16 a2[4];
      unsigned wO[3];
#pragma unroll
      for (int e = 0; e < 3; ++e) wO[e] = opq(lds0 + e * 65536 + h * 512 + (r >> 2) * 64 + (((kq & 1) ^ h) * 32) + (lane & 3) * 8);
      constexpr int PF = GPF, OW = GOW;
      u32x2 wl[8], wh[8];
      auto wld = [&](int g) {
        const int j = g >> 2;
        const int o_ = (wslot(j) & 65535) + ((g >> 1) & 1) * 1024 + (g & 1) * 256;
        wl[g & 7] = trread(wO[wslot(j) >> 16], o_); wh[g & 7] = trread(wO[wslot(j) >> 16], o_ + 2048);
      };
      const int row = wave * 32 + s32;
      const unsigned xb = opq(lds0 + L_X2 + row * 128 + h * 8);
      const int sw = (row >> 1) & 7;
      float sw_ = 0.f;
#pragma unroll
      for (int c = 0; c < 16; ++c) sw_ = dot2(wv[c >> 1][c & 1], 0x3F803F80u, sw_);
      sw_ = xh_sum(sw_);
      asm volatile("" : "+v"(sw_));
      BAR();
      STAMPT(0, 7, 0);
#pragma unroll
      for (int j = 0; j < OW0; ++j) { oload(j); SB0(); }
#pragma unroll
      for (int c = 0; c < 8; ++c) {
        const u32x2 xx = xr[c];
#pragma unroll
        for (int i = 0; i < 2; ++i) {
          a2[c >> 2][(c & 3) * 4 + 2 * i] = bflo(xx[i]); a2[c >> 2][(c & 3) * 4 + 2 * i + 1] = bfhi(xx[i]);
          a2[2 + (c >> 2)][(c & 3) * 4 + 2 * i] = 0.f; a2[2 + (c >> 2)][(c & 3) * 4 + 2 * i + 1] = 0.f;
        }
      }
#pragma unroll
      for (int g = 0; g < PF; ++g) wld(g);
      SB0();
#pragma unroll
      for (int j = 0; j < 32; ++j) {
        if (j == 30) wvld();
#pragma unroll
        for (int n = 0; n < 4; ++n) {
          const int g = j * 4 + n;
          a2[n] = mfma32(cat(wl[g & 7], wh[g & 7]), of[j][n >> 1], a2[n]);
          SB0();
          if (g + PF < 128) wld(g + PF);
          {
            constexpr int RAMP = OW - OW0;
            const int q = j < RAMP ? 2 * OW0 + 4 * j + n : (n & 1) ? -1 : 2 * (j + OW) + (n >> 1);
            if (q >= 0 && q < 64) oload1(q >> 1, q & 1);
          }
          if (n == 0 && j + OW == 32) misc[20 + wave] = 1;
          SB0();
          asm volatile("" :: "v"(wl[g & 7]), "v"(wh[g & 7]), "v"(of[j][n >> 1]));
          SB0();
        }
      }
      STAMPT(0, 8, 0); STAMPT(3, 1, 64); STAMPT(3, 2, 128); STAMPT(3, 3, 192);
      a2[0] += a2[2]; a2[1] += a2[3];
      misc[8 + wave] = 1;
      unsigned hb[16];
      {
        float s[2] = {0.f, 0.f}, sq[2] = {0.f, 0.f}, pw[2] = {0.f, 0.f};
#pragma unroll
        for (int i = 0; i < 16; ++i) {
          const unsigned t = pack2(a2[i >> 3][(i & 7) * 2], a2[i >> 3][(i & 7) * 2 + 1]);
          hb[i] = t;
          s[i & 1] = dot2(t, 0x3F803F80u, s[i & 1]); sq[i & 1] = dot2(t, t, sq[i & 1]); pw[i & 1] = dot2(t, wv[i >> 1][i & 1], pw[i & 1]);
        }
        const u32x4 st = {__float_as_uint(xh_sum(s[0] + s[1])), __float_as_uint(xh_sum(sq[0] + sq[1])), __float_as_uint(xh_sum(pw[0] + pw[1])), __float_as_uint(sw_)};
        if (h == 0) STW(seal(st), WS_ST2 + ((rp * 16 + cb) * 128 + row) * 16);
      }
      STAMPT(0, 9, 0);
      u32x4 pu_[8];
      __builtin_amdgcn_s_sleep(SLC0);
#pragma nounroll
      for (unsigned s_ = 0; s_ < SPIN_CAP; ++s_) {
        unsigned mk = 0u;
#pragma unroll
        for (int j = 0; j < 8; ++j) { pu_[j] = LDW(WS_ST2 + ((rp * 16 + h * 8 + j) * 128 + row) * 16); mk = max(max(mk, pu_[j][1]), pu_[j][3]); }
        if (!__any(mk == EMPTY)) break;
        asm volatile("" ::: "memory");
        __builtin_amdgcn_s_sleep(SLC1);
      }
      STAMPT(0, 10, 0);
      f32x4 tt = {0.f, 0.f, 0.f, 0.f};
#pragma unroll
      for (int j = 0; j < 8; ++j) tt += __builtin_bit_cast(f32x4, pu_[j]);
      const float mean = xh_sum(tt[0]) * (1.0f / 1024.0f);
      const float var = fmaxf(xh_sum(tt[1]) * (1.0f / 1024.0f) - mean * mean, 0.f);
      const float mb = rbf(mean), rs = __builtin_amdgcn_rcpf(rbf(__builtin_amdgcn_sqrtf(rbf(rbf(var) + 1e-5f))));
      float sc = rbf((xh_sum(tt[2]) - mb * xh_sum(tt[3])) * rs);
      if (!(sc == sc)) sc = -INFINITY;
      unsigned b0 = h ? 0u : mk_key(sc, rp * 128 + row), b1 = 0;
      top2_wave(b0, b1);
      const int pan = rp * 4 + wave;
      if (lane == 0) st64(ws, SY_FD + cb * 512 + pan * 8, ((u64)b1 << 32) | b0);
      STAMPT(1, 11, 0);
      unsigned nq[16];
#pragma unroll
      for (int i = 0; i < 16; ++i) {
        const unsigned dq = pack2(bflo(hb[i]) - mb, bfhi(hb[i]) - mb);
        nq[i] = pack2(bflo(dq) * rs, bfhi(dq) * rs);
      }
      const int lc0 = key_idx(b0) & 31, lc1 = key_idx(b1) & 31;
      {
        const int c0 = lc0, c1 = lc1;
        char* tb = smem + L_TB + wave * 512;
        if (s32 == c0 || s32 == c1) {
          char* tp = tb + (s32 == c0 ? 0 : 256) + h * 8;
#pragma unroll
          for (int c = 0; c < 8; ++c) { *(u32x2*)(tp + c * 16) = (u32x2){nq[2 * c], nq[2 * c + 1]}; *(u32x2*)(tp + 128 + c * 16) = (u32x2){hb[2 * c], hb[2 * c + 1]}; }
        }
        __builtin_amdgcn_wave_barrier();
        const u32x4 v4 = seal(*(const u32x4*)(tb + s32 * 16));
#pragma unroll
        for (int rr_ = 0; rr_ < XSR; rr_ += 2)
          if (rr_ + h < XSR) STW(v4, WS_XS + (rr_ + h) * 524288 + (pan * 2 + (s32 >> 4)) * 4096 + ((s32 >> 3) & 1) * 2048 + cb * 128 + (s32 & 7) * 16);
      }
      u64 cc = ~0ull;
#pragma unroll
      for (int c = 0; c < 8; ++c) {
        u32x2 ob;
#pragma unroll
        for (int i = 0; i < 2; ++i) ob[i] = pack2(bflo(hb[2 * c + i]) + bflo(nq[2 * c + i]), bfhi(hb[2 * c + i]) + bfhi(nq[2 * c + i]));
        *(LAS u32x2*)((LAS char*)(size_t)(xb + ((c ^ sw) * 16))) = ob;
      }
      auto ost = [&](int rl, bool on) {
        const int rw = wave * 32 + rl;
        const u32x4 v = *(const u32x4*)(smem + L_X2 + rw * 128 + (((lane & 7) ^ ((rw >> 1) & 7)) * 16));
        if (on) *(u32x4*)(out + (size_t)(rp * 128 + rw) * D_ + cb * 64 + (lane & 7) * 8) = v;
      };
      __builtin_amdgcn_wave_barrier();
      if (EOUT == 2 || (EOUT == 1 && wave != 0)) {
#pragma unroll
        for (int m = 0; m < 4; ++m) { const int rl = m * 8 + (lane >> 3); ost(rl, rl != lc0 && rl != lc1); }
      }
      if (wave == 0) {
        __builtin_amdgcn_s_sleep(SLD0);
#pragma nounroll
        for (unsigned s_ = 0; s_ < SPIN_CAP; ++s_) {
          cc = ld64(ws, SY_FD + cb * 512 + lane * 8);
          if (__all((unsigned)(cc >> 32) != EMPTY)) break;
          __builtin_amdgcn_s_sleep(SLD1);
        }
        STAMPT(1, 13, 0);
        unsigned c0 = (unsigned)cc, c1 = (unsigned)(cc >> 32);
        top2_wave(c0, c1);
        const unsigned w0 = key_idx(c0), w1 = key_idx(c1);
        const unsigned k0 = __builtin_amdgcn_readlane((unsigned)cc, w0 >> 5), k1 = __builtin_amdgcn_readlane((unsigned)cc, w1 >> 5);
        const unsigned sl0 = (w0 >> 5) * 2 + (k0 == c0 ? 0 : 1), sl1 = (w1 >> 5) * 2 + (k1 == c1 ? 0 : 1);
        if (lane == 0) { misc[1] = w0; misc[2] = w1; misc[3] = sl0; misc[4] = sl1; misc[5] = c0; misc[6] = c1; }
        if (has_mlp) {
          const unsigned xo = WS_XS + (idx & (XSR - 1)) * 524288u + (h ? sl1 : sl0) * 4096u + (fs & 3) * 512 + s32 * 16;
          u32x4 q;
#pragma nounroll
          for (unsigned s_ = 0; s_ < SPIN_CAP; ++s_) {
            q = LDW(xo);
            if (__all(max(q[1], q[3]) != EMPTY)) break;
            asm volatile("" ::: "memory");
            __builtin_amdgcn_s_sleep(XSL1);
          }
          *(u32x4*)(smem + L_XS + h * 528 + s32 * 16) = q;
        }
      }
      LBAR();
      STAMPT(0, 11, 0);
      const int sel0 = misc[1], sel1 = misc[2];
      if (EOUT == 2 || (EOUT == 1 && wave != 0)) {
        const int rl = (lane & 8) ? lc1 : lc0, grow = rp * 128 + wave * 32 + rl;
        ost(rl, lane < 16 && grow != sel0 && grow != sel1);
      } else {
#pragma unroll
        for (int m = 0; m < 4; ++m) { const int rl = m * 8 + (lane >> 3), grow = rp * 128 + wave * 32 + rl; ost(rl, grow != sel0 && grow != sel1); }
      }
      STAMPT(0, 12, 0);
      if (bid < 64 && wave == 0) {
        const int tk = bid >> 5, c0 = (bid & 31) * 32;
        const unsigned xo = WS_XS + (idx & (XSR - 1)) * 524288u + (unsigned)misc[3 + tk] * 4096u + c0 * 2 + (lane & 7) * 8;
        u64 xn, xh;
#pragma nounroll
        for (unsigned s_ = 0; s_ < SPIN_CAP; ++s_) {
          xn = ld64(ws, xo); xh = ld64(ws, xo + 2048);
          if (__all((unsigned)(xn >> 32) != EMPTY && (unsigned)(xh >> 32) != EMPTY)) break;
        }
        const float sg = rbf(__builtin_amdgcn_rcpf(1.0f + __builtin_amdgcn_exp2f(key_val((unsigned)misc[5 + tk]) * -1.44269504088896340736f)));
        u16* op = out + (size_t)(tk ? sel1 : sel0) * D_ + c0 + (lane & 7) * 4;
        const unsigned yo = opq(WS_YP + (lane >> 3) * 8192 + tk * 4096 + c0 * 4 + (lane & 7) * 16);
#pragma nounroll
        for (unsigned s_ = 0; s_ < SPIN_CAP; ++s_) { if (__builtin_amdgcn_readfirstlane(misc[0])) break; __builtin_amdgcn_s_sleep(1); }
        __builtin_amdgcn_s_sleep(YSL0);
        u32x4 pv[7];
#pragma nounroll
        for (unsigned s_ = 0; s_ < SPIN_CAP; ++s_) {
          unsigned mk = 0u;
#pragma unroll
          for (int p = 0; p < 7; ++p) pv[p] = LDWS(yo, p * 65536);
#pragma unroll
          for (int p = 0; p < 7; ++p) mk = max(max(mk, pv[p][1]), pv[p][3]);
          if (!__any(mk == EMPTY)) break;
          asm volatile("" ::: "memory");
          __builtin_amdgcn_s_sleep(YSL1);
        }
        STAMPT(0, 15, 0);
        f32x4 y = __builtin_bit_cast(f32x4, pv[0]);
#pragma unroll
        for (int p = 1; p < 7; ++p) y += __builtin_bit_cast(f32x4, pv[p]);
        float t[4];
#pragma unroll
        for (int i = 0; i < 4; ++i) {
          float v = y[i]; v += dppf<DPP_ROR8>(v); v = xh_sum(x16_sum(v));
          const unsigned nn = (unsigned)(xn >> (32 * (i >> 1))), hh = (unsigned)(xh >> (32 * (i >> 1)));
          t[i] = ((i & 1) ? bfhi(hh) : bflo(hh)) + rbf(((i & 1) ? bfhi(nn) : bflo(nn)) + rbf(rbf(v) * sg));
        }
        if (lane < 8) *(u32x2*)op = (u32x2){pack2(t[0], t[1]), pack2(t[2], t[3])};
        asm volatile(".Lfused_end:" ::: "memory");
        STAMPT(0, 14, 0);
      }
    }
  } else {
    __builtin_amdgcn_s_setprio(2);
    const int hw = wave - 4, mt = tid & 255;
    u32x4 wgr[8], wur[8], wdr[8];
    const int krow = hw * 64 + (lane >> 3);
    const unsigned gof = opq((unsigned)(((fs & 3) * 256 + krow) * F_ + (fs >> 2) * 64 + (lane & 7) * 8) * 2u);
    const unsigned dof = opq((unsigned)(((fs >> 2) * 64 + (lane >> 3)) * D_ + (fs & 3) * 256 + hw * 64 + (lane & 7) * 8) * 2u);
    const unsigned wto = opq((unsigned)((idx * 32 + (mt >> 3)) * 2048 + (cb >> 2) * 512 + ((mt >> 1) & 3) * 128));
#define WLOAD(dst, vo, base) asm volatile("global_load_dwordx4 %0, %1, %2" WPOL : "=v"(dst) : "v"(vo), "s"(uni(base)) : "memory")
    auto wload = [&](int i) {
      if (i < 8) WLOAD(wgr[i], gof, wg + i * (8 * F_));
      else if (i < 16) WLOAD(wur[i - 8], gof, wu + (i - 8) * (8 * F_));
      else WLOAD(wdr[i - 16], dof, wd + (i - 16) * (8 * D_));
    };
    auto wpin = [&](int i) {
      if (i < 8) asm volatile("" : "+v"(wgr[i])); else if (i < 16) asm volatile("" : "+v"(wur[i - 8])); else asm volatile("" : "+v"(wdr[i - 16]));
    };
    const int pbi = lane >> 4, pq = (lane >> 2) & 3, pch = lane & 3;
    const unsigned voW = opq((unsigned)((8 * hw + 4 * (pbi >> 1) + pq) * D_ + (pbi & 1) * 32 + (pch ^ ((pbi >> 1) * 2)) * 8) * 2u);
    auto stageW = [&](int j) { glds16(voW, uni(wo + cb * 64) + (size_t)(j ^ hrot) * (32 * D_ * 2), lds0 + wslot(j) + hw * 1024); };
    u32x4 e4 = {EMPTY, EMPTY, EMPTY, EMPTY};
    asm volatile("" : "+v"(e4));
    const unsigned l16 = opq(lane * 16);
    if (hw < 2) {
      const char* baseA = uni(x + (size_t)rb * 128 * D_);
      const char* baseB = uni(wqkv + hp * 64);
      unsigned voA[2], voB[2];
      {
        const int r0 = 64 * hw + (lane >> 3), r1 = r0 + 8;
        voA[0] = (unsigned)(r0 * D_ + ((lane & 7) ^ ((r0 >> 1) & 7)) * 8) * 2u;
        voA[1] = (unsigned)(r1 * D_ + ((lane & 7) ^ ((r1 >> 1) & 7)) * 8) * 2u;
        voB[0] = (unsigned)((32 * hw + 4 * (pbi >> 1) + pq) * (3 * D_) + (pbi & 1) * 32 + pch * 8) * 2u;
        voB[1] = (unsigned)((32 * hw + 8 + 4 * (pbi >> 1) + pq) * (3 * D_) + (pbi & 1) * 32 + (pch ^ 2) * 8) * 2u;
      }
      auto stage = [&](int t) {
        const int kt = korder(t), b = t & 3;
        const char* pa = baseA + kt * 128;
        const char* pb = baseB + (size_t)kt * (64 * 3 * D_ * 2);
#pragma unroll
        for (int j = 0; j < 8; ++j) glds16(voA[j & 1] + (j >> 1) * (16 * D_ * 2), pa, lds0 + L_A + b * 16384 + (8 * hw + j) * 1024);
#pragma unroll
        for (int j = 0; j < 4; ++j) {
          const unsigned vo = voB[j & 1] + (j >> 1) * (16 * 3 * D_ * 2);
          glds16(vo + 2048, pb, lds0 + L_KV + b * 16384 + (4 * hw + j) * 1024);
          glds16(vo + 4096, pb, lds0 + L_KV + b * 16384 + 8192 + (4 * hw + j) * 1024);
          glds16(vo, pb, lds0 + L_Q + b * 8192 + (4 * hw + j) * 1024);
        }
      };
      WAITV(10); BAR();
      stage(2); stage(3);
      WAITV(40); BAR();
      for (int t = 0; t < 12; ++t) { WAITV(20); BAR(); stage(t + 4); }
      WAITV(20); BAR();
      asm volatile("s_waitcnt vmcnt(0)" :: "v"(e0) : "memory");
      BAR();
    } else {
      const int aw = hw - 2, at = tid & 127;
      const bool master = bid == 0 && aw == 0;
      unsigned hs_cur = 0, hs_g0 = 0;
      u32x4 ms_exp = {0, 0, 0, 0}, ms_go = {0, 0, 0, 0}, pm = {0, 0, 0, 0};
      const unsigned p_off = opq(master ? SY_RE + lane * 16 : (lane == 1 ? SY_GO : SY_CH) + bid * 4);
      const char* wsu = uni(ws);
#define HS_PROBE() asm volatile("global_load_dwordx4 %0, %1, %2 sc1" : "+v"(pm) : "v"(p_off), "s"(wsu) : "memory")
#define HS_WAIT() asm volatile("s_waitcnt vmcnt(0)" : "+v"(pm) :: "memory")
      bool done = false;
      auto hs_check = [&]() __attribute__((always_inline)) {
        HS_WAIT();
        if (!master) {
          const unsigned c = __builtin_amdgcn_readlane(pm[0], 0), go = __builtin_amdgcn_readlane(pm[0], 1);
          if (go != hs_g0) done = true;
          else if (c != hs_cur) { hs_cur = c; st32(ws, SY_RE + bid * 4, c); }
        } else if (!done && __all(pm[0] == ms_exp[0] && pm[1] == ms_exp[1] && pm[2] == ms_exp[2] && pm[3] == ms_exp[3])) {
          STW(ms_go, SY_GO + lane * 16);
          done = true;
        }
      };
      const bool prober = master || aw == 1;
      const int kq = lane >> 4, r = lane & 15;
      constexpr unsigned OOB = 0x80000000u;
      const unsigned ko = WS_KVP + (hp * 16 + rb) * KVB + aw * 2176;
      const unsigned vm0 = opq(lane < 32 ? rs_o + aw * 512 + lane * 16 : lane < 56 ? cs_o + aw * 384 + (lane - 32) * 16 : ko + 2048 + (lane - 56) * 16);
      u32x4 e1;
      asm volatile("s_waitcnt vmcnt(0)\n\tv_mov_b32 %0, %4\n\tv_mov_b32 %1, %5\n\tv_mov_b32 %2, %6\n\tv_mov_b32 %3, %7"
                   : "=&v"(e1[0]), "=&v"(e1[1]), "=&v"(e1[2]), "=&v"(e1[3]) : "v"(e0[0]), "v"(e0[1]), "v"(e0[2]), "v"(e0[3]) : "memory");
      hs_g0 = __builtin_amdgcn_readfirstlane(e1[0]);
      u32x4 g0 = {0u, 0u, 0u, 0u};
      BAR();
      if (master) { ms_exp = e1 + 1u; STW(ms_exp, SY_CH + lane * 16); g0 = LDW(SY_GO + lane * 16); }
      u32x2 rst[4];
      {
        u32x4 a[4][2];
#pragma unroll
        for (int i = 0; i < 4; ++i)
#pragma unroll
          for (int k = 0; k < 2; ++k) a[i][k] = *(const u32x4*)(smem + L_A + (64 * aw + 16 * i + r) * 128 + (((4 * k + kq) ^ ((r >> 1) & 7)) * 16));
        const f32x4 Z4 = {0.f, 0.f, 0.f, 0.f};
        const int l3 = lane & 3;
#pragma unroll
        for (int i = 0; i < 4; ++i) {
          const f32x4 s1 = mfma16(ONES4, a[i][1], mfma16(ONES4, a[i][0], Z4)), s2 = mfma16(a[i][1], a[i][1], mfma16(a[i][0], a[i][0], Z4));
          const float q = l3 == 0 ? s2[0] : l3 == 1 ? s2[1] : l3 == 2 ? s2[2] : s2[3];
          rst[i] = (u32x2){min(__float_as_uint(s1[0]), 0xFFFFFFFEu), min(__float_as_uint(q), 0xFFFFFFFEu)};
          asm volatile("" : "+v"(rst[i]));
        }
      }
      const unsigned vm1 = opq(aw ? (lane < 2 ? SY_FD + cb * 512 + rp * 32 + lane * 16 : lane == 2 ? SY_FB + rb * 256 + hp * 16 : OOB)
                                  : (has_mlp ? WS_GU + fs * 1024 + lane * 16 : OOB));
      const unsigned vrs = opq((r >> 2) == kq ? rs_o + (64 * aw + r) * 8 : OOB);
      const unsigned vc0 = opq(cs_o + aw * 384 + lane * 4), vc1 = opq(lane < 32 ? cs_o + aw * 384 + 256 + lane * 4 : OOB);
      const unsigned sg_off = opq(aw ? WS_CS + hp * (16 * 768) + min(lane, 47) * 16 : WS_RS + rb * 16384 + lane * 16), sg_st = aw ? 768 : 1024;
      unsigned cb_[2][2];
#pragma unroll
      for (int c = 0; c < 2; ++c) {
        const unsigned o_ = lds0 + kq * 1024 + aw * 256 + (r >> 2) * 64 + ((c ^ (kq & 1)) * 32) + (lane & 3) * 8;
        cb_[0][c] = opq(o_ + L_Q + (tcs & 3) * 8192); cb_[1][c] = opq(o_ + L_KV + (tcs & 3) * 16384);
      }
      const int hq = (3 - (int)((size_t)wqkv >> 7) - (hp & ~3)) & 7;
      const int qln = ((((hp & 3) - hq - 1) & 3) * 8 + (idx & 7)) * 64 + (at >> 1);
      const unsigned vq = opq((unsigned)((((kph + 8) * 64 + qln / 3) & 1023) * 6144 + (qln % 3) * 2048 + ((hp & ~3) + (hq & 3)) * 128));
      const unsigned lw0 = opq(lds0 + (aw ? L_CS + lane * 16 : L_SA + lane * 8)), lw1 = opq(lds0 + L_WR + (lane & 7) * 16);
      const unsigned vcp = opq(min((unsigned)(idx * CODE_PER + min(at >> 3, CODE_PER - 1)) * 128u, clen));
      BAR();
      ms_go = g0 + 1u;
      STW(e4, vm0); STW(e4, vm1); STWS(e4, l16, ko); STWS(e4, l16, ko + 1024);
      BAR();
      STAMPT(2, 0, 384);
      if (aw == 1) HS_PROBE();
      HS_WAIT();
      BAR();
      STAMPT(2, 1, 384);
      if (aw == 1) { hs_cur = __builtin_amdgcn_readlane(pm[0], 0); st32(ws, SY_RE + bid * 4, hs_cur); }
#pragma unroll
      for (int i = 0; i < 4; ++i) __builtin_amdgcn_raw_buffer_store_b64(rst[i], wsr, vrs + i * 128, 0, 16);
      unsigned qt = 0;
      if (hq < 4 && (hp & 3) != hq) asm volatile("global_load_dword %0, %1, %2" : "=v"(qt) : "v"(vq), "s"(uni(wqkv)) : "memory");
      BAR();
      STAMPT(2, 2, 384);
      if (prober) HS_PROBE();
      unsigned cv0, cv1;
      {
        f32x4 cacc[6];
#pragma unroll
        for (int f = 0; f < 6; ++f) {
          cacc[f] = (f32x4){0.f, 0.f, 0.f, 0.f};
#pragma unroll
          for (int k = 0; k < 2; ++k) {
            const int o_ = (f >= 4 ? 8192 : 0) + k * 4096;
            cacc[f] = mfma16(ONES4, cat(trread(cb_[f >= 2][f & 1], o_), trread(cb_[f >= 2][f & 1], o_ + 512)), cacc[f]);
          }
        }
        cv0 = min(__float_as_uint(kq == 0 ? cacc[0][0] : kq == 1 ? cacc[1][0] : kq == 2 ? cacc[2][0] : cacc[3][0]), 0xFFFFFFFEu);
        cv1 = min(__float_as_uint(kq == 0 ? cacc[4][0] : cacc[5][0]), 0xFFFFFFFEu);
        asm volatile("" : "+v"(cv0), "+v"(cv1));
      }
      BAR();
      STAMPT(2, 3, 384);
      HS_WAIT();
      asm volatile("" :: "v"(qt));
      if (prober) hs_check();
      if (master) HS_PROBE();
      __builtin_amdgcn_raw_buffer_store_b32(cv0, wsr, vc0, 0, 16); __builtin_amdgcn_raw_buffer_store_b32(cv1, wsr, vc1, 0, 16);
      STWS(e4, l16, WS_ST2 + (rp * 16 + cb) * 2048 + aw * 1024);
      BAR();
      STAMPT(2, 4, 384);
      if (master) { hs_check(); HS_PROBE(); }
      BAR();
      STAMPT(2, 5, 384);
      if (master) { hs_check(); HS_PROBE(); }
      BAR();
      STAMPT(2, 6, 384);
      if (master) hs_check();
      if (prober) HS_PROBE();
      BAR();
      STAMPT(2, 7, 384);
      if (prober) {
        hs_check();
#pragma nounroll
        for (unsigned s_ = 0; s_ < SPIN_CAP && !done; ++s_) { HS_PROBE(); hs_check(); }
      }
      BAR();
      STAMPT(2, 8, 384);
      unsigned cpf = 0;
      if (CPF2) asm volatile("global_load_dword %0, %1, %2" : "=v"(cpf) : "v"(vcp), "s"(pc0) : "memory");
      u32x4 sg[16], wrv;
      asm volatile("global_load_dwordx4 %0, %1, %2" : "=v"(wrv) : "v"(l16 & 127u), "s"(uni(w_r + cb * 64)) : "memory");
#pragma unroll
      for (int i = 0; i < 16; ++i) {
        asm volatile("global_load_dwordx4 %0, %1, %2 sc1" : "=v"(sg[i]) : "v"(sg_off), "s"(wsu + i * sg_st) : "memory");
        if (i == SGS) { BAR(); STAMPT(2, 9, 384); }
      }
      auto tsum = [&](int o) __attribute__((always_inline)) {
        f32x4 t[4];
#pragma unroll
        for (int i = 0; i < 4; ++i) t[i] = add4(__builtin_bit_cast(f32x4, sg[o + i]), __builtin_bit_cast(f32x4, sg[o + 4 + i]));
        return add4(add4(t[0], t[2]), add4(t[1], t[3]));
      };
      WAITV(8);
#pragma unroll
      for (int i = 0; i < 8; ++i) asm volatile("" : "+v"(sg[i]));
      asm volatile("" : "+v"(wrv) : "v"(cpf));
      f32x4 sm = tsum(0), sn;
      asm volatile("" : "+v"(sm));
      BAR();
      STAMPT(2, 10, 384);
      WAITV(0);
#pragma unroll
      for (int i = 8; i < 16; ++i) asm volatile("" : "+v"(sg[i]));
      sn = tsum(8);
      asm volatile("" : "+v"(sn));
      BAR();
      STAMPT(2, 11, 384);
      sm = add4(sm, sn);

      const float chk = (sm[0] + sm[1]) + (sm[2] + sm[3]);
      if (__builtin_expect(__any(chk != chk), 0)) {
#pragma nounroll
        for (unsigned s_ = 0; s_ < SPIN_CAP; ++s_) {
          unsigned mx = 0;
#pragma unroll
          for (int i = 0; i < 16; ++i) { sg[i] = LDWS(sg_off, i * sg_st); mx = max(max(mx, max(sg[i][0], sg[i][1])), max(sg[i][2], sg[i][3])); }
          if (!__any(mx == EMPTY)) break;
        }
        sm = add4(tsum(0), tsum(8));
      }
      STAMPT(1, 1, 384);
      BAR();
      STAMPT(2, 12, 384);
      float mean[2], vr[2];
      if (aw == 1) {
        *(LAS f32x4*)(size_t)lw0 = lane < 48 ? sm : (f32x4){0.f, 0.f, 0.f, 0.f};
        *(LAS u32x4*)(size_t)lw1 = wrv;
      } else {
#pragma unroll
        for (int e = 0; e < 2; ++e) {
          mean[e] = sm[2 * e] * (1.0f / 1024.0f);
          vr[e] = fmaxf(sm[2 * e + 1] * (1.0f / 1024.0f) - mean[e] * mean[e], 0.f);
          asm volatile("" : "+v"(mean[e]), "+v"(vr[e]));
        }
      }
      LBAR();
      STAMPT(2, 13, 384);
      if (aw == 0) {
        f32x2 ra, rb_;
#pragma unroll
        for (int e = 0; e < 2; ++e) {
          ra[e] = __builtin_amdgcn_rcpf(rbf(__builtin_amdgcn_sqrtf(rbf(rbf(vr[e]) + 1e-5f))));
          rb_[e] = -rbf(mean[e]) * ra[e];
        }
        *(LAS f32x2*)(size_t)lw0 = ra; *(LAS f32x2*)(size_t)(lw0 + 512) = rb_;
        asm volatile("s_waitcnt lgkmcnt(0)" ::: "memory");
        misc[7] = 1;
      }
      STAMPT(1, 2, 384);
    }
    unsigned wt;
    asm volatile("global_load_dword %0, %1, %2" : "=v"(wt) : "v"(wto), "s"(uni(wo)) : "memory");
#pragma unroll
    for (int i = 0; i < NW1; ++i) { WAITV(THR - 1); wload(i); }
    u32x4 wr_[20];
#define RLOAD(dst, vo, base) asm volatile("global_load_dwordx4 %0, %1, %2" : "=v"(dst) : "v"(vo), "s"(uni(base)) : "memory")
#pragma unroll
    for (int i = 0; i < NR; ++i) {
      WAITV(THR - 1);
      RLOAD(wr_[i], voW, uni(wo + cb * 64) + (size_t)((i + 12) ^ hrot) * (32 * D_ * 2));
      if (i % 6 == 0) STAMPT(3, 8 + i / 6, 256);
    }
#pragma nounroll
    for (unsigned s_ = 0; s_ < SPIN_CAP; ++s_) if (__all(misc[28 + (lane & 3)] != 0)) break;
#pragma unroll
    for (int j = 0; j < NE; ++j) { WAITV(THR - 1); stageW(j); }
    WAITV(0);
#pragma unroll
    for (int i = 0; i < NR; ++i) asm volatile("" : "+v"(wr_[i]));
#pragma unroll
    for (int i = 0; i < NW1; ++i) wpin(i);
    asm volatile("" :: "v"(wt));
    STAMPT(1, 3, 256);
    {
      u32x4 pv[16], zq;
      const unsigned ko = WS_KVP + hp * 16 * KVB + (hw >> 1) * 2176;
      unsigned kl = ko + (hw & 1) * 1024 + lane * 16, zo = ko + ((hw & 1) * 8 + (lane >> 3)) * KVB + 2048 + (lane & 7) * 16;
      u32x4 sel[2];
#pragma unroll
      for (int e = 0; e < 2; ++e)
#pragma unroll
        for (int i = 0; i < 4; ++i)
          sel[e][i] = ((lane & 15) >> 2) != (lane >> 4) || (i >> 1) != e || (i & 1) != ((lane & 3) >> 1) ? 0u : (lane & 1) ? 0x3F800000u : 0x3F80u;
      f32x4 s[2][2];
      auto ksum = [&]() __attribute__((always_inline)) {
#pragma unroll
        for (int p = 0; p < 16; ++p) {
#pragma unroll
          for (int e = 0; e < 2; ++e) s[p >> 3][e] = mfma16(sel[e], pv[p], (p & 7) ? s[p >> 3][e] : (f32x4){0.f, 0.f, 0.f, 0.f});
          SB0();
        }
        s[0][0] += s[1][0]; s[0][1] += s[1][1];
      };
      asm volatile("" : "+v"(sel[0]), "+v"(sel[1]), "+v"(kl), "+v"(zo));
#pragma nounroll
      for (unsigned s_ = 0; s_ < SPIN_CAP; ++s_) { if (__builtin_amdgcn_readfirstlane(misc[16 + hw]) == 2) break; __builtin_amdgcn_s_sleep(1); }
      zq = LDW(zo);
#pragma unroll
      for (int p = 0; p < 16; ++p) { pv[p] = LDWS(kl + (p & 7) * KVB, ((p >> 3) ^ (idx & 1)) * (8 * KVB)); SB0(); }
      ksum();
      const float chk = s[0][0][0] + __uint_as_float(zq[1]) + __uint_as_float(zq[3]);
      if (__any(chk != chk)) {
        unsigned need = 0x1FFFFu;
#pragma nounroll
        for (unsigned s_ = 0; s_ < SPIN_CAP; ++s_) {
#pragma unroll
          for (int p = 0; p < 16; ++p) if ((need & (1u << p)) && !__any(max(pv[p][1], pv[p][3]) == EMPTY)) need &= ~(1u << p);
          if (!__any(max(zq[1], zq[3]) == EMPTY)) need &= ~0x10000u;
          if (!need) break;
          asm volatile("" ::: "memory");
#pragma unroll
          for (int p = 0; p < 16; ++p) if (need & (1u << p)) pv[p] = LDWS(kl + (p & 7) * KVB, ((p >> 3) ^ (idx & 1)) * (8 * KVB));
          if (need & 0x10000u) zq = LDW(zo);
        }
        ksum();
      }
      STAMPT(1, 4, 256);
      *(u32x4*)(smem + L_KVL + hw * 1024 + lane * 16) =
          (u32x4){pack2(s[0][0][0], s[0][0][1]), pack2(s[0][0][2], s[0][0][3]), pack2(s[0][1][0], s[0][1][1]), pack2(s[0][1][2], s[0][1][3])};
      f32x4 z = __builtin_bit_cast(f32x4, zq);
#pragma unroll
      for (int i = 0; i < 4; ++i) { float v = z[i]; v += dppf<DPP_ROR8>(v); z[i] = xh_sum(x16_sum(v)); }
      if (lane < 8) *(f32x4*)(smem + L_ZL + hw * 128 + lane * 16) = z;
    }
    LBAR();
#pragma unroll
    for (int j = NE; j < 32; ++j) if (j < 12 || j >= NR + 12) { WAITV(THR3 - 1); stageW(j); }
#pragma nounroll
    for (unsigned s_ = 0; s_ < SPIN_CAP; ++s_) { if (__all(misc[24 + (lane & 3)] != 0)) break; __builtin_amdgcn_s_sleep(1); }
#pragma unroll
    for (int i = 0; i < NR; ++i) *(u32x4*)(smem + wslot(i + 12) + hw * 1024 + lane * 16) = wr_[i];
    asm volatile("s_waitcnt lgkmcnt(0)" ::: "memory");
    {
      const u32x4 e5 = {EMPTY, EMPTY, EMPTY, EMPTY};
      if (has_mlp && hw >= 2) STW(e5, WS_YP + (fs >> 2) * 8192 + (hw & 1) * 4096 + (fs & 3) * 1024 + lane * 16);
#pragma unroll
      for (int rr_ = 0; rr_ < XSR; rr_ += 2)
        if (rr_ + (hw >> 1) < XSR) STW(e5, WS_XS + (rr_ + (hw >> 1)) * 524288 + (rp * 16 + ((mt >> 3) & 15)) * 2048 + cb * 128 + (mt & 7) * 16);
    }
    WAITV(0);
    STAMPT(1, 6, 256);
    if (hw == 0) {
      __builtin_amdgcn_s_sleep(SLB);
#pragma nounroll
      for (unsigned s_ = 0; s_ < SPIN_CAP; ++s_) { if (__all(ld32(ws, SY_FB + rp * 256 + lane * 4) != EMPTY)) break; __builtin_amdgcn_s_sleep(SLB1); }
    }
    BAR();
#pragma nounroll
    for (unsigned s_ = 0; s_ < SPIN_CAP; ++s_) { if (__builtin_amdgcn_readfirstlane(misc[20 + hw])) break; __builtin_amdgcn_s_sleep(2); }
    unsigned cp3 = 0;
    if (CPF3) asm volatile("global_load_dword %0, %1, %2" : "=v"(cp3) : "v"(min((unsigned)(idx * CODE_PER + min(mt >> 4, CODE_PER - 1)) * 128u, clen)), "s"(pc0) : "memory");
#pragma unroll
    for (int i = NW1; i < 24; ++i) { WAITV(THR2 - 1); wload(i); }
    WAITV(0);
    asm volatile("" :: "v"(cp3));
#pragma unroll
    for (int i = NW1; i < 24; ++i) wpin(i);
    STAMPT(1, 7, 256);
    const int c16 = lane & 15, kq = lane >> 4;
    u32x4 gf[2][8];
    u32x4 df[4][2];
    {
#pragma nounroll
      for (unsigned s_ = 0; s_ < SPIN_CAP; ++s_) { if (__all(misc[8 + (lane & 3)] != 0)) break; __builtin_amdgcn_s_sleep(1); }
      STAMPT(3, 5, 256);
#pragma unroll
      for (int i = 0; i < 8; ++i) {
        const int go = hw * 8192 + i * 1024 + ((lane ^ (((lane >> 4) & 1) * 2 + (i & 1) * 4)) * 16);
        *(u32x4*)(smem + go) = wgr[i];
        *(u32x4*)(smem + 32768 + go) = wur[i];
        *(u32x4*)(smem + L_DW + hw * 8192 + i * 1024 + ((lane ^ (((lane >> 4) & 3) * 2)) * 16)) = wdr[i];
      }
      asm volatile("s_waitcnt lgkmcnt(0)" ::: "memory");
      STAMPT(3, 6, 256);
      misc[12 + hw] = 1;
#pragma nounroll
      for (unsigned s_ = 0; s_ < SPIN_CAP; ++s_) if (__all(misc[12 + (lane & 3)] != 0)) break;
      asm volatile("" ::: "memory");
      STAMPT(3, 7, 256);
      const unsigned gb = opq(lds0 + (kq * 8 + (c16 >> 2)) * 128 + (hw ^ ((c16 >> 3) + (kq & 1) * 2)) * 32 + (lane & 3) * 8);
      const unsigned db = lds0 + L_DW + hw * 8192 + (kq * 4 + (c16 >> 2)) * 128 + (lane & 3) * 8, dsw = (c16 >> 3) + (kq & 1) * 2;
#pragma unroll
      for (int m = 0; m < 2; ++m)
#pragma unroll
        for (int k = 0; k < 8; ++k) gf[m][k] = cat(trread(gb, m * 32768 + k * 4096), trread(gb, m * 32768 + k * 4096 + 512));
#pragma unroll
      for (int t = 0; t < 4; ++t) {
        const unsigned dt = opq(db + (t ^ dsw) * 32);
#pragma unroll
        for (int e = 0; e < 2; ++e) df[t][e] = cat(trread(dt, ((t + RSG * 2 * e) & 3) * 2048), trread(dt, ((t + RSG * (2 * e + 1)) & 3) * 2048));
      }
      SB0();
#pragma unroll
      for (int k = 0; k < 8; ++k) asm volatile("" : "+v"(gf[0][k]), "+v"(gf[1][k]), "+v"(df[k >> 1][k & 1]));
    }
    const unsigned gu_st = opq(c16 < 4 ? WS_GU + fs * 1024 + hw * 256 + c16 * 64 + kq * 16 : 0x80000000u);
    const unsigned gu_ld = opq(WS_GU + (fs & ~3) * 1024 + c16 * 64 + kq * 16);
    const unsigned yp_st = opq((c16 & 2) ? 0x80000000u : WS_YP + (fs >> 2) * 8192 + (c16 & 1) * 4096 + (fs & 3) * 1024 + hw * 256 + (c16 >> 2) * 64 + kq * 16);
    const unsigned xsb = opq(lds0 + L_XS + (c16 & 1) * 528 + kq * 16);
    STAMPT(1, 8, 256);
    BAR();
    if (has_mlp) {
      f32x4 ga[2] = {{0.f, 0.f, 0.f, 0.f}, {0.f, 0.f, 0.f, 0.f}};
      u32x4 xf[8];
#pragma unroll
      for (int k = 0; k < 8; ++k) xf[k] = ldsr(xsb, k * 64);
#pragma unroll
      for (int k = 0; k < 8; ++k) { ga[0] = mfma16(gf[0][k], xf[k], ga[0]); ga[1] = mfma16(gf[1][k], xf[k], ga[1]); }
      STW(seal(__builtin_bit_cast(u32x4, (c16 & 2) ? ga[1] : ga[0])), gu_st);
      STAMPT(1, 14, 256);
      __builtin_amdgcn_s_sleep(SLG0);
      u32x4 q[4];
#pragma nounroll
      for (unsigned s_ = 0; s_ < SPIN_CAP; ++s_) {
        unsigned mk = 0u;
#pragma unroll
        for (int p = 0; p < 4; ++p) q[p] = LDW(gu_ld + p * 1024);
#pragma unroll
        for (int p = 0; p < 4; ++p) mk = max(max(mk, q[p][1]), q[p][3]);
        if (!__any(mk == EMPTY)) break;
        asm volatile("" ::: "memory");
        __builtin_amdgcn_s_sleep(SLG1);
      }
      STAMPT(1, 15, 256);
      const f32x4 tot = (__builtin_bit_cast(f32x4, q[0]) + __builtin_bit_cast(f32x4, q[1])) + (__builtin_bit_cast(f32x4, q[2]) + __builtin_bit_cast(f32x4, q[3]));
      float av[4];
#pragma unroll
      for (int i = 0; i < 4; ++i) {
        const float v = rbf(tot[i]), o_ = dppf<DPP_X2>(v);
        const float gs = (c16 & 2) ? o_ : v, us = (c16 & 2) ? v : o_;
        av[i] = rbf(gs * __builtin_amdgcn_rcpf(1.0f + __builtin_amdgcn_exp2f(gs * -1.44269504088896340736f))) * us;
      }
      const unsigned a0 = pack2(av[0], av[1]), a1 = pack2(av[2], av[3]);
      const u32x4 b0 = {a0, a1, dppu<DPP_ROR4>(a0), dppu<DPP_ROR4>(a1)}, b1 = {dppu<DPP_ROR8>(a0), dppu<DPP_ROR8>(a1), dppu<DPP_ROR12>(a0), dppu<DPP_ROR12>(a1)};
      f32x4 y[4];
#pragma unroll
      for (int t = 0; t < 4; ++t) y[t] = mfma16(df[t][0], b0, (f32x4){0.f, 0.f, 0.f, 0.f});
#pragma unroll
      for (int t = 0; t < 4; ++t) y[t] = mfma16(df[t][1], b1, y[t]);
      const int T = c16 >> 2;
      STW(seal(__builtin_bit_cast(u32x4, T == 0 ? y[0] : T == 1 ? y[1] : T == 2 ? y[2] : y[3])), yp_st);
      STAMPT(0, 13, 256);
      misc[0] = 1;
    }
  }
}

extern "C" void kernel_launch(void* const* d_in, const int* in_sizes, int n_in, void* d_out, int out_size,
                              void* d_ws, size_t ws_size, hipStream_t stream) {
  (void)hipFuncSetAttribute((const void*)fused, hipFuncAttributeMaxDynamicSharedMemorySize, LDS_TOTAL);
  fused<<<256, 512, LDS_TOTAL, stream>>>((const u16*)d_in[0], (const u16*)d_in[1], (const u16*)d_in[2], (const u16*)d_in[4],
                                         (const u16*)d_in[5], (const u16*)d_in[6], (const u16*)d_in[7], (u16*)d_out, (char*)d_ws);
}
```

```cpp
#include <hip/hip_runtime.h>
#include <stdint.h>

#define LAS __attribute__((address_space(3)))
#define GAS __attribute__((address_space(1)))
#define DI __device__ __forceinline__

#define XSR 1
#define GPF 5
#define GOW 14
#define NE 4
#define WPOL
#define HRM 3
#define OW0 4
#define SLC0 30
#define SLC1 8
#define SLD0 0
#define SLB1 0
#define CPF3 1
#define HRS 4
#define SLD1 0
#define SGS 7
#define CPF2 1
#define RSG 3
#define EOUT 0
#define GB 1
#define NR 20
#define THR3 4
#define THR2 6
#define THR 6
#define NW1 10
#define SLA 8
#define SLB 0
constexpr int CODE_PER = 11;
constexpr int XSL1 = 2;
#define YSL0 12
#define YSL1 4
#define SLG0 14
#define SLG1 2
typedef unsigned short u16;
typedef unsigned long long u64;
typedef __attribute__((ext_vector_type(2))) __bf16 bf16x2;
typedef __attribute__((ext_vector_type(8))) __bf16 bf16x8;
typedef __attribute__((ext_vector_type(4))) short s16x4;
typedef __attribute__((ext_vector_type(16))) float f32x16;
typedef __attribute__((ext_vector_type(4))) float f32x4;
typedef __attribute__((ext_vector_type(2))) float f32x2;
typedef __attribute__((ext_vector_type(4))) unsigned u32x4;
typedef __attribute__((ext_vector_type(2))) unsigned u32x2;
typedef GAS unsigned gu32;
typedef GAS unsigned long long gu64;

constexpr int S_ = 2048, D_ = 1024, F_ = 3584;

constexpr unsigned SY_CH = 0, SY_GO = 1024, SY_RE = 2048, SY_FB = 4096, SY_FD = 8192;
constexpr unsigned WS_RS  = 16384;
constexpr unsigned WS_CS  = WS_RS + 256 * 1024;
constexpr unsigned KVB    = 4352;
constexpr unsigned WS_KVP = WS_CS + 256 * 768;
constexpr unsigned WS_ST2 = WS_KVP + 256 * KVB;
constexpr unsigned WS_GU  = WS_ST2 + 256 * 2048;
constexpr unsigned WS_XS  = WS_GU + 256 * 1024;
constexpr unsigned WS_YP  = WS_XS + XSR * 524288;
constexpr unsigned WS_O   = WS_YP + 56 * 8192;
constexpr unsigned WS_END = WS_O + S_ * D_ * 2;
constexpr size_t WS_DIAG  = 64u << 20;
#define STAMPT(kid, i, T) do {} while (0)

DI float bflo(unsigned u) { return __uint_as_float(u << 16); }
DI float bfhi(unsigned u) { return __uint_as_float(u & 0xffff0000u); }
DI float rbf(float x) { return (float)(__bf16)x; }
DI unsigned pack2(float a, float b) { bf16x2 c; c[0] = (__bf16)a; c[1] = (__bf16)b; return __builtin_bit_cast(unsigned, c); }
DI f32x4 mfma16(u32x4 a, u32x4 b, f32x4 c) {
  return __builtin_amdgcn_mfma_f32_16x16x32_bf16(__builtin_bit_cast(bf16x8, a), __builtin_bit_cast(bf16x8, b), c, 0, 0, 0);
}
DI f32x16 mfma32(u32x4 a, u32x4 b, f32x16 c) {
  return __builtin_amdgcn_mfma_f32_32x32x16_bf16(__builtin_bit_cast(bf16x8, a), __builtin_bit_cast(bf16x8, b), c, 0, 0, 0);
}
DI u32x2 trread(const char* p) {
  s16x4 v = __builtin_amdgcn_ds_read_tr16_b64_v4i16((LAS s16x4*)p);
  return __builtin_bit_cast(u32x2, v);
}
DI unsigned opq(unsigned v) { asm volatile("" : "+v"(v)); return v; }
DI u32x2 trread(unsigned base, int off) {
  s16x4 v = __builtin_amdgcn_ds_read_tr16_b64_v4i16((LAS s16x4*)((LAS char*)(size_t)base + off));
  return __builtin_bit_cast(u32x2, v);
}
DI u32x4 ldsr(unsigned base, int off) { return *(const LAS u32x4*)((const LAS char*)(size_t)base + off); }
DI u32x4 cat(u32x2 a, u32x2 b) { return (u32x4){a[0], a[1], b[0], b[1]}; }
DI u32x4 pick(int c, u32x4 a, u32x4 b) { const unsigned m = c ? ~0u : 0u; return (a & m) | (b & ~m); }
DI f32x4 add4(f32x4 a, f32x4 b) {
  f32x4 c;
#pragma unroll
  for (int i = 0; i < 4; ++i) asm("v_add_f32 %0, %1, %2" : "=v"(c[i]) : "v"(a[i]), "v"(b[i]));
  return c;
}
DI float dot2(unsigned a, unsigned b, float c) {
  return __builtin_amdgcn_fdot2_f32_bf16(__builtin_bit_cast(bf16x2, a), __builtin_bit_cast(bf16x2, b), c, false);
}
DI const char* uni(const void* p) {
  const u64 v = (u64)p;
  return (const char*)(((u64)(unsigned)__builtin_amdgcn_readfirstlane((int)(v >> 32)) << 32) | (unsigned)__builtin_amdgcn_readfirstlane((int)v));
}
DI void glds16(unsigned voff, const void* sbase, unsigned lds) {
  asm volatile("s_mov_b32 m0, %2\n\ts_nop 0\n\tglobal_load_lds_dwordx4 %0, %1" ::"v"(voff), "s"(uni(sbase)), "s"(__builtin_amdgcn_readfirstlane(lds)) : "memory");
}
DI float xh_sum(float v) {
  auto r = __builtin_amdgcn_permlane32_swap(__float_as_uint(v), __float_as_uint(v), false, false);
  return __uint_as_float(r[0]) + __uint_as_float(r[1]);
}
template <int CTRL> DI unsigned dppu(unsigned v) { return (unsigned)__builtin_amdgcn_update_dpp(0, (int)v, CTRL, 0xf, 0xf, true); }
template <int CTRL> DI float dppf(float v) { return __uint_as_float(dppu<CTRL>(__float_as_uint(v))); }
constexpr int DPP_X1 = 0xB1, DPP_X2 = 0x4E, DPP_HMIR = 0x141, DPP_MIR = 0x140, DPP_ROR8 = 0x128, DPP_ROR4 = 0x124, DPP_ROR12 = 0x12C;
DI float oct_sum(float v) { v += dppf<DPP_X1>(v); v += dppf<DPP_X2>(v); v += dppf<DPP_HMIR>(v); return v; }
DI float x16_sum(float v) {
  auto r = __builtin_amdgcn_permlane16_swap(__float_as_uint(v), __float_as_uint(v), false, false);
  return __uint_as_float(r[0]) + __uint_as_float(r[1]);
}
DI unsigned phi_pair(f32x2 t) {
  const f32x2 u = t + 1.000001f;
  const f32x2 l = t * 1.44269504088896340736f;
  const f32x2 e = (f32x2){__builtin_amdgcn_exp2f(l[0]), __builtin_amdgcn_exp2f(l[1])} + 1e-6f;
  return pack2(__builtin_amdgcn_fmed3f(e[0], u[0], 1.000001f), __builtin_amdgcn_fmed3f(e[1], u[1], 1.000001f));
}
#define RLX_AGENT __ATOMIC_RELAXED, __HIP_MEMORY_SCOPE_AGENT
DI void st32(char* ws, unsigned off, unsigned v) { __hip_atomic_store((gu32*)(ws + off), v, RLX_AGENT); }
DI void st64(char* ws, unsigned off, u64 v) { __hip_atomic_store((gu64*)(ws + off), v, RLX_AGENT); }
DI unsigned ld32(char* ws, unsigned off) { return __hip_atomic_load((gu32*)(ws + off), RLX_AGENT); }
DI u64 ld64(char* ws, unsigned off) { return __hip_atomic_load((gu64*)(ws + off), RLX_AGENT); }
#define WAITV(n) asm volatile("s_waitcnt vmcnt(%0)" :: "n"(n) : "memory")
#define BAR() do { asm volatile("" ::: "memory"); __builtin_amdgcn_s_barrier(); asm volatile("" ::: "memory"); } while (0)
#define LBAR() do { asm volatile("s_waitcnt lgkmcnt(0)" ::: "memory"); __builtin_amdgcn_s_barrier(); asm volatile("" ::: "memory"); } while (0)
#define SB0() __builtin_amdgcn_sched_barrier(0)
constexpr unsigned SPIN_CAP = 1u << 22;
constexpr unsigned EMPTY = 0xFFFFFFFFu;
DI u32x4 seal(u32x4 v) { v[1] = min(v[1], 0xFFFFFFFEu); v[3] = min(v[3], 0xFFFFFFFEu); return v; }
DI unsigned mk_key(float v, unsigned idx) {
  const unsigned u = __float_as_uint(v) >> 16;
  const unsigned o = (u & 0x8000u) ? (~u & 0xffffu) : (u | 0x8000u);
  return (o << 16) | (0xFFFFu - idx);
}
DI unsigned key_idx(unsigned k) { return (0xFFFFu - (k & 0xFFFFu)) & (S_ - 1); }
DI float key_val(unsigned k) { const unsigned o = k >> 16; return __uint_as_float(((o & 0x8000u) ? (o & 0x7fffu) : (~o & 0xffffu)) << 16); }
DI void top2_mix(unsigned& b0, unsigned& b1, unsigned c0, unsigned c1) {
  const unsigned lo = min(b0, c0), mx = max(b1, c1);
  b0 = max(b0, c0); b1 = max(lo, mx);
}
DI void top2_wave(unsigned& b0, unsigned& b1) {
  top2_mix(b0, b1, dppu<DPP_X1>(b0), dppu<DPP_X1>(b1));
  top2_mix(b0, b1, dppu<DPP_X2>(b0), dppu<DPP_X2>(b1));
  top2_mix(b0, b1, dppu<DPP_HMIR>(b0), dppu<DPP_HMIR>(b1));
  top2_mix(b0, b1, dppu<DPP_MIR>(b0), dppu<DPP_MIR>(b1));
  { auto p = __builtin_amdgcn_permlane16_swap(b0, b0, false, false); auto q = __builtin_amdgcn_permlane16_swap(b1, b1, false, false);
    b0 = p[0]; b1 = q[0]; top2_mix(b0, b1, p[1], q[1]); }
  { auto p = __builtin_amdgcn_permlane32_swap(b0, b0, false, false); auto q = __builtin_amdgcn_permlane32_swap(b1, b1, false, false);
    b0 = p[0]; b1 = q[0]; top2_mix(b0, b1, p[1], q[1]); }
}

constexpr int L_A = 0, L_Q = 65536, L_KV = 98304, LDS_TOTAL = 163840;
constexpr int L_M  = L_KV;
constexpr int L_SA = L_M, L_SB = L_M + 512, L_CS = L_M + 1024;
constexpr int L_MISC = L_M + 1792;
constexpr int L_XCH = L_M + 2048;
constexpr int L_KVL = L_M + 11264;
constexpr int L_ZL = L_M + 15360;
constexpr int L_XS = L_M + 3072;
constexpr int L_RED = L_M + 5120;
constexpr int L_ACT = L_M + 9216;
constexpr int L_TB = L_M + 9472;
constexpr int L_WR = L_M + 15872;
constexpr int L_DW = 114688;
constexpr int L_X2 = 81920;
DI constexpr int wslot(int j) { return j < 12 ? 114688 + j * 4096 : (j - 12) * 4096; }

__global__ __launch_bounds__(512, 2) void fused(const u16* __restrict__ x, const u16* __restrict__ wqkv,
                                                const u16* __restrict__ wo, const u16* __restrict__ w_r,
                                                const u16* __restrict__ wg, const u16* __restrict__ wu,
                                                const u16* __restrict__ wd, u16* __restrict__ out, char* __restrict__ ws) {
  extern __shared__ __attribute__((aligned(16))) char smem[];
  u64 pc0; unsigned clen;
  asm volatile("s_getpc_b64 %0\n.Lfused_pc:\n\ts_mov_b32 %1, .Lfused_end-.Lfused_pc-16" : "=s"(pc0), "=s"(clen));
  SB0();
  const int tid = threadIdx.x, lane = tid & 63;
  const int wave = __builtin_amdgcn_readfirstlane(tid >> 6);
  STAMPT(0, 0, 0);
  const int bid = blockIdx.x, xcd = bid & 7, idx = bid >> 3;
  const int rb = (xcd & 1) * 8 + (idx & 7);
  const int hp = (xcd >> 1) * 4 + (idx >> 3);
  const int rp = rb, cb = ((3 + (xcd >> 1) + 2 * (xcd & 1)) & 3) * 4 + (hp & 3);
  const int hrot = HRS * (cb & HRM);
  const unsigned lds0 = (unsigned)(size_t)smem;
  const __amdgpu_buffer_rsrc_t wsr = __builtin_amdgcn_make_buffer_rsrc(ws, 0, WS_END, 0x00020000);
#define LDW(off) __builtin_amdgcn_raw_buffer_load_b128(wsr, (off), 0, 16)
#define LDWS(off, so) __builtin_amdgcn_raw_buffer_load_b128(wsr, (off), (so), 16)
#define STW(v, off) __builtin_amdgcn_raw_buffer_store_b128((v), wsr, (off), 0, 16)
#define STWS(v, off, so) __builtin_amdgcn_raw_buffer_store_b128((v), wsr, (off), (so), 16)
  const int kph = 4 * (xcd >> 1) + 8 * (xcd & 1), kg_ = (hp + rb) & 3;
  auto korder = [&](int t) { return ((t + kph) & 15) ^ kg_; };
  u32x4 e0;
  {
    const int r0 = 16 * wave + (lane >> 3), r1 = r0 + 8, pb_ = lane >> 4;
    const unsigned a0 = opq((unsigned)(r0 * D_ + ((lane & 7) ^ ((r0 >> 1) & 7)) * 8) * 2u), a1 = opq((unsigned)(r1 * D_ + ((lane & 7) ^ ((r1 >> 1) & 7)) * 8) * 2u);
    const unsigned b0 = opq((unsigned)((8 * wave + 4 * (pb_ >> 1) + ((lane >> 2) & 3)) * (3 * D_) + (pb_ & 1) * 32 + ((lane & 3) ^ ((wave & 1) * 2)) * 8) * 2u);
    SB0();
    auto first = [&](int t, int dw) {
      const char* pa = uni(x + (size_t)(rb * 128 + 16 * dw) * D_) + korder(t) * 128;
      const char* pb = uni(wqkv + (size_t)(8 * dw) * (3 * D_) + hp * 64) + (size_t)korder(t) * (64 * 3 * D_ * 2);
      const int w = wave + dw;
      glds16(a0, pa, lds0 + L_A + t * 16384 + w * 2048); glds16(a1, pa, lds0 + L_A + t * 16384 + w * 2048 + 1024);
      glds16(b0 + 2048, pb, lds0 + L_KV + t * 16384 + w * 1024); glds16(b0 + 4096, pb, lds0 + L_KV + t * 16384 + 8192 + w * 1024);
      glds16(b0, pb, lds0 + L_Q + t * 8192 + w * 1024);
    };
    STAMPT(3, 0, 0);
    first(0, 0);
    STAMPT(3, 4, 0);
    SB0();
    {
      const char* eb = wave < 4 ? (const char*)pc0 : uni(ws);
      const unsigned eo = wave < 4 ? min((unsigned)(idx * CODE_PER + min(tid >> 4, CODE_PER - 1)) * 128u, clen) : wave == 6 && bid == 0 ? SY_RE + lane * 16 : SY_GO + bid * 4;
      asm volatile("global_load_dwordx4 %0, %1, %2 sc1" : "=v"(e0) : "v"(eo), "s"(uni(eb)) : "memory");
    }
    BAR();
    STAMPT(3, 12, 0);
    if (wave < 6) first(1, 0);
    if (wave == 4 || wave == 5) first(1, 2);
    SB0();
  }
  volatile LAS int* misc = (volatile LAS int*)(size_t)(lds0 + L_MISC);
  const bool has_mlp = bid < 224;
  const int fs = has_mlp ? (bid & 7) * 28 + (bid >> 3) : (bid & 7) * 28;
  const int tcs = (rb & 4) ? 4 : 3;
  const unsigned rs_o = WS_RS + (rb * 16 + korder(0)) * 1024, cs_o = WS_CS + (hp * 16 + korder(tcs)) * 768;
  const u32x4 ONES4 = {0x3F803F80u, 0x3F803F80u, 0x3F803F80u, 0x3F803F80u};

  if (wave < 4) {
    const int wr = wave >> 1, wc = wave & 1;
    const int r = lane & 15, kq = lane >> 4;
    f32x4 acc[4][6];
    {
      unsigned aO[2], bQ[2], bK[2];
#pragma unroll
      for (int k = 0; k < 2; ++k) aO[k] = opq(lds0 + L_A + (64 * wr + r) * 128 + (((4 * k + kq) ^ ((r >> 1) & 7)) * 16));
#pragma unroll
      for (int c = 0; c < 2; ++c) {
        const unsigned o_ = lds0 + kq * 1024 + wc * 256 + (r >> 2) * 64 + ((c ^ (kq & 1)) * 32) + (lane & 3) * 8;
        bQ[c] = opq(o_ + L_Q); bK[c] = opq(o_ + L_KV);
      }
#pragma unroll
      for (int i = 0; i < 4; ++i)
#pragma unroll
        for (int j = 0; j < 6; ++j) acc[i][j] = (f32x4){0.f, 0.f, 0.f, 0.f};
      struct Fr { u32x4 a[4]; u32x2 bl[6], bh[6]; };
      Fr F[2];
      auto bread = [&](int f, int b, int k, int hi) {
        return f < 2 ? trread(bQ[f & 1], k * 4096 + b * 8192 + hi * 512) : trread(bK[f & 1], k * 4096 + b * 16384 + ((f >> 1) - 1) * 8192 + hi * 512);
      };
      auto step = [&](Fr& C, Fr& N, int b, int k, int MC, int ML) {
        int ld = 0;
        constexpr int ORD[6] = {2, 3, 4, 5, 0, 1};
        auto load1 = [&]() {
          if (ML == 0) return;
          if (ld < 4) N.a[ld] = ldsr(aO[k], b * 16384 + ld * 2048);
          else {
            const int e = ld - 4, f = ML == 1 ? (e >> 1) : ORD[(e >> 1) % 6], nf = ML == 1 ? 2 : ML == 2 ? 4 : 6;
            if ((e >> 1) < nf) { if (e & 1) N.bh[f] = bread(f, b, k, 1); else N.bl[f] = bread(f, b, k, 0); }
          }
          ++ld;
        };
#pragma unroll
        for (int m = 0; m < 24; ++m) {
          const int j = ORD[m >> 2], i = m & 3;
          if (!((j < 2 ? 1 : 2) & MC)) continue;
          if (j < 2) acc[i][j] = mfma16(cat(C.bl[j], C.bh[j]), C.a[i], acc[i][j]);
          else acc[i][j] = mfma16(C.a[i], cat(C.bl[j], C.bh[j]), acc[i][j]);
          load1();
          SB0();
        }
      };
      WAITV(5); BAR();
      STAMPT(0, 1, 0);
#pragma unroll
      for (int m = 0; m < 4; ++m) F[0].a[m] = ldsr(aO[0], m * 2048);
#pragma unroll
      for (int f = 0; f < 6; ++f) { F[0].bl[f] = bread(f, 0, 0, 0); F[0].bh[f] = bread(f, 0, 0, 1); }
      for (int t4 = 0; t4 < 12; t4 += 4) {
#pragma unroll
        for (int b = 0; b < 4; ++b) {
          step(F[0], F[1], b, 1, 3, 3);
          if (b == 0 && t4 == 0) { asm volatile("s_waitcnt vmcnt(0)" :: "v"(e0) : "memory"); STAMPT(3, 13, 0); BAR(); STAMPT(3, 14, 0); }
          step(F[1], F[0], (b + 1) & 3, 0, 3, 3);
          BAR();
        }
      }
#pragma unroll
      for (int b = 0; b < 4; ++b) {
        step(F[0], F[1], b, 1, 2, 2);
        step(F[1], F[0], (b + 1) & 3, 0, 2, b < 3 ? 2 : 0);
        if (b < 2) BAR();
      }
      misc[28 + wave] = 1;
#pragma nounroll
      for (unsigned s_ = 0; s_ < SPIN_CAP; ++s_) if (__builtin_amdgcn_readfirstlane(misc[7])) break;
      asm volatile("" ::: "memory");
      STAMPT(0, 2, 0);
      {
        const float* sa = (const float*)(smem + L_SA) + 64 * wr + 4 * kq;
        const float* cs = (const float*)(smem + L_CS) + wc * 96 + r;
        const float c4[4] = {cs[32], cs[48], cs[64], cs[80]};
        unsigned kp[4][2][2], vp[4][2][2];
#pragma unroll
        for (int i = 0; i < 4; ++i) {
          const f32x4 a4 = *(const f32x4*)(sa + 16 * i), b4 = *(const f32x4*)(sa + 128 + 16 * i);
          const f32x2 aa[2] = {{a4[0], a4[1]}, {a4[2], a4[3]}}, bb[2] = {{b4[0], b4[1]}, {b4[2], b4[3]}};
#pragma unroll
          for (int j = 0; j < 2; ++j)
#pragma unroll
            for (int e = 0; e < 2; ++e) {
              const f32x2 tk = (f32x2){acc[i][2 + j][2 * e], acc[i][2 + j][2 * e + 1]} * aa[e] + bb[e] * c4[j];
              const f32x2 tv = (f32x2){acc[i][4 + j][2 * e], acc[i][4 + j][2 * e + 1]} * aa[e] + bb[e] * c4[2 + j];
              kp[i][j][e] = phi_pair(tk);
              vp[i][j][e] = pack2(tv[0], tv[1]);
            }
        }
        f32x4 kv[2][2], zv[2];
#pragma unroll
        for (int d = 0; d < 2; ++d) {
          zv[d] = (f32x4){0.f, 0.f, 0.f, 0.f}; kv[d][0] = zv[d]; kv[d][1] = zv[d];
#pragma unroll
          for (int u = 0; u < 2; ++u) {
            const u32x4 ka = {kp[2 * u][d][0], kp[2 * u][d][1], kp[2 * u + 1][d][0], kp[2 * u + 1][d][1]};
#pragma unroll
            for (int e = 0; e < 2; ++e)
              kv[d][e] = mfma16(ka, (u32x4){vp[2 * u][e][0], vp[2 * u][e][1], vp[2 * u + 1][e][0], vp[2 * u + 1][e][1]}, kv[d][e]);
            zv[d] = mfma16(ka, ONES4, zv[d]);
          }
        }
        char* xo = smem + L_XCH + wave * 2048 + lane * 16;
        *(f32x4*)xo = wr ? kv[0][0] : kv[0][1];
        *(f32x4*)(xo + 1024) = wr ? kv[1][0] : kv[1][1];
        if (r < 2) *(f32x4*)(smem + L_XCH + 8192 + wave * 128 + r * 64 + kq * 16) = r ? zv[1] : zv[0];
#pragma unroll
        for (int m = 0; m < 4; ++m) F[0].a[m] = ldsr(aO[0], m * 2048);
#pragma unroll
        for (int f = 0; f < 2; ++f) { F[0].bl[f] = bread(f, 0, 0, 0); F[0].bh[f] = bread(f, 0, 0, 1); }
        STAMPT(1, 0, 0);
        asm volatile("s_waitcnt lgkmcnt(0)" ::: "memory");
        misc[16 + wave] = 1;
#pragma nounroll
        for (unsigned s_ = 0; s_ < SPIN_CAP; ++s_) if (__builtin_amdgcn_readfirstlane(misc[16 + (wave ^ 2)])) break;
        asm volatile("" ::: "memory");
        const char* xi = smem + L_XCH + (wave ^ 2) * 2048 + lane * 16;
        const f32x4 s0 = *(const f32x4*)xi + (wr ? kv[0][1] : kv[0][0]), s1 = *(const f32x4*)(xi + 1024) + (wr ? kv[1][1] : kv[1][0]);
        const unsigned ko = WS_KVP + (hp * 16 + rb) * KVB + wc * 2176;
        STW(seal((u32x4){pack2(s0[0], s0[1]), pack2(s0[2], s0[3]), pack2(s1[0], s1[1]), pack2(s1[2], s1[3])}), ko + wr * 1024 + lane * 16);
        if (wr == 0 && lane < 8) {
          const f32x4 z = *(const f32x4*)(smem + L_XCH + 8192 + wave * 128 + lane * 16) + *(const f32x4*)(smem + L_XCH + 8192 + (wave + 2) * 128 + lane * 16);
          STW(seal(__builtin_bit_cast(u32x4, z)), ko + 2048 + lane * 16);
        }
                STAMPT(0, 3, 0);
      }
#pragma unroll
      for (int b = 0; b < 4; ++b) {
        step(F[0], F[1], b, 1, 1, 1);
        step(F[1], F[0], (b + 1) & 3, 0, 1, b < 3 ? 1 : 0);
        if (b == GB) misc[16 + wave] = 2;
      }
    }
    u32x4 qp[4];
    {
      const float* sa = (const float*)(smem + L_SA) + 64 * wr + r;
      const float* cs = (const float*)(smem + L_CS) + wc * 96 + 4 * kq;
      const f32x4 c0 = *(const f32x4*)cs, c1 = *(const f32x4*)(cs + 16);
#pragma unroll
      for (int i = 0; i < 4; ++i) {
        const float a = sa[16 * i], b = sa[128 + 16 * i];
        qp[i][0] = phi_pair((f32x2){acc[i][0][0], acc[i][0][1]} * a + (f32x2){c0[0], c0[1]} * b);
        qp[i][1] = phi_pair((f32x2){acc[i][0][2], acc[i][0][3]} * a + (f32x2){c0[2], c0[3]} * b);
        qp[i][2] = phi_pair((f32x2){acc[i][1][0], acc[i][1][1]} * a + (f32x2){c1[0], c1[1]} * b);
        qp[i][3] = phi_pair((f32x2){acc[i][1][2], acc[i][1][3]} * a + (f32x2){c1[2], c1[3]} * b);
        asm volatile("" : "+v"(qp[i]));
      }
    }
    u32x2 xr[8];
    {
      const int row = wave * 32 + (lane & 31);
#pragma unroll
      for (int c = 0; c < 8; ++c) xr[c] = *(const u32x2*)(smem + L_A + ((cb & 3) ^ kg_) * 16384 + row * 128 + ((c ^ ((row >> 1) & 7)) * 16) + (lane >> 5) * 8);
    }
    STAMPT(0, 4, 0);
    LBAR();
    STAMPT(0, 5, 0);
    {
      const u32x4 k0 = *(const u32x4*)(smem + L_KVL + wc * 2048 + lane * 16), k1 = *(const u32x4*)(smem + L_KVL + wc * 2048 + 1024 + lane * 16);
      const float* zl = (const float*)(smem + L_ZL) + wc * 64 + kq * 4;
      const f32x4 za = *(const f32x4*)zl + *(const f32x4*)(zl + 32), zb = *(const f32x4*)(zl + 16) + *(const f32x4*)(zl + 48);
      const u32x4 zf = {pack2(za[0], za[1]), pack2(za[2], za[3]), pack2(zb[0], zb[1]), pack2(zb[2], zb[3])};
      const f32x4 Z4 = {0.f, 0.f, 0.f, 0.f};
      const unsigned oo = WS_O + ((rb * 8 + wr * 4) * 32 + 2 * hp + wc) * 1024 + lane * 16;
#pragma unroll
      for (int i = 0; i < 4; ++i) {
        const f32x4 dn = mfma16(zf, qp[i], Z4), x0 = mfma16(k0, qp[i], Z4), x1 = mfma16(k1, qp[i], Z4);
        const float ri = __builtin_amdgcn_rcpf(dn[0] + 1e-6f);
        STW(((u32x4){pack2(x0[0] * ri, x0[1] * ri), pack2(x0[2] * ri, x0[3] * ri), pack2(x1[0] * ri, x1[1] * ri), pack2(x1[2] * ri, x1[3] * ri)}),
            oo + i * 32768);
      }
      misc[24 + wave] = 1;
      WAITV(0);
      if (lane == 0) st32(ws, SY_FB + rb * 256 + hp * 16 + wave * 4, 1u);
    }
    STAMPT(0, 6, 0);
    {
      const int h = lane >> 5, s32 = lane & 31;
      u32x2 wv[8];
      auto wvld = [&]() {
#pragma unroll
        for (int c = 0; c < 8; ++c) wv[c] = *(const volatile LAS u32x2*)(size_t)(lds0 + L_WR + c * 16 + h * 8);
      };
      wvld();
      u32x4 of[32][2];
      const unsigned ol = WS_O + (rp * 8 + wave * 2 + (kq & 1)) * 32768 + (h * 16 + r) * 16;
      auto oload1 = [&](int j, int e) { of[j][e] = LDWS(ol + e * 512 + (j & (HRS - 1)) * 1024, ((j & ~(HRS - 1)) ^ hrot) * 1024); };
      auto oload = [&](int j) { oload1(j, 0); oload1(j, 1); };
      f32x16 a2[4];
      unsigned wO[3];
#pragma unroll
      for (int e = 0; e < 3; ++e) wO[e] = opq(lds0 + e * 65536 + h * 512 + (r >> 2) * 64 + (((kq & 1) ^ h) * 32) + (lane & 3) * 8);
      constexpr int PF = GPF, OW = GOW;
      u32x2 wl[8], wh[8];
      auto wld = [&](int g) {
        const int j = g >> 2;
        const int o_ = (wslot(j) & 65535) + ((g >> 1) & 1) * 1024 + (g & 1) * 256;
        wl[g & 7] = trread(wO[wslot(j) >> 16], o_); wh[g & 7] = trread(wO[wslot(j) >> 16], o_ + 2048);
      };
      const int row = wave * 32 + s32;
      const unsigned xb = opq(lds0 + L_X2 + row * 128 + h * 8);
      const int sw = (row >> 1) & 7;
      float sw_ = 0.f;
#pragma unroll
      for (int c = 0; c < 16; ++c) sw_ = dot2(wv[c >> 1][c & 1], 0x3F803F80u, sw_);
      sw_ = xh_sum(sw_);
      asm volatile("" : "+v"(sw_));
      BAR();
      STAMPT(0, 7, 0);
#pragma unroll
      for (int j = 0; j < OW0; ++j) { oload(j); SB0(); }
#pragma unroll
      for (int c = 0; c < 8; ++c) {
        const u32x2 xx = xr[c];
#pragma unroll
        for (int i = 0; i < 2; ++i) {
          a2[c >> 2][(c & 3) * 4 + 2 * i] = bflo(xx[i]); a2[c >> 2][(c & 3) * 4 + 2 * i + 1] = bfhi(xx[i]);
          a2[2 + (c >> 2)][(c & 3) * 4 + 2 * i] = 0.f; a2[2 + (c >> 2)][(c & 3) * 4 + 2 * i + 1] = 0.f;
        }
      }
#pragma unroll
      for (int g = 0; g < PF; ++g) wld(g);
      SB0();
#pragma unroll
      for (int j = 0; j < 32; ++j) {
        if (j == 30) wvld();
#pragma unroll
        for (int n = 0; n < 4; ++n) {
          const int g = j * 4 + n;
          a2[n] = mfma32(cat(wl[g & 7], wh[g & 7]), of[j][n >> 1], a2[n]);
          SB0();
          if (g + PF < 128) wld(g + PF);
          {
            constexpr int RAMP = OW - OW0;
            const int q = j < RAMP ? 2 * OW0 + 4 * j + n : (n & 1) ? -1 : 2 * (j + OW) + (n >> 1);
            if (q >= 0 && q < 64) oload1(q >> 1, q & 1);
          }
          if (n == 0 && j + OW == 32) misc[20 + wave] = 1;
          SB0();
          asm volatile("" :: "v"(wl[g & 7]), "v"(wh[g & 7]), "v"(of[j][n >> 1]));
          SB0();
        }
      }
      STAMPT(0, 8, 0); STAMPT(3, 1, 64); STAMPT(3, 2, 128); STAMPT(3, 3, 192);
      a2[0] += a2[2]; a2[1] += a2[3];
      misc[8 + wave] = 1;
      unsigned hb[16];
      {
        float s[2] = {0.f, 0.f}, sq[2] = {0.f, 0.f}, pw[2] = {0.f, 0.f};
#pragma unroll
        for (int i = 0; i < 16; ++i) {
          const unsigned t = pack2(a2[i >> 3][(i & 7) * 2], a2[i >> 3][(i & 7) * 2 + 1]);
          hb[i] = t;
          s[i & 1] = dot2(t, 0x3F803F80u, s[i & 1]); sq[i & 1] = dot2(t, t, sq[i & 1]); pw[i & 1] = dot2(t, wv[i >> 1][i & 1], pw[i & 1]);
        }
        const u32x4 st = {__float_as_uint(xh_sum(s[0] + s[1])), __float_as_uint(xh_sum(sq[0] + sq[1])), __float_as_uint(xh_sum(pw[0] + pw[1])), __float_as_uint(sw_)};
        if (h == 0) STW(seal(st), WS_ST2 + ((rp * 16 + cb) * 128 + row) * 16);
      }
      STAMPT(0, 9, 0);
      u32x4 pu_[8];
      __builtin_amdgcn_s_sleep(SLC0);
#pragma nounroll
      for (unsigned s_ = 0; s_ < SPIN_CAP; ++s_) {
        unsigned mk = 0u;
#pragma unroll
        for (int j = 0; j < 8; ++j) { pu_[j] = LDW(WS_ST2 + ((rp * 16 + h * 8 + j) * 128 + row) * 16); mk = max(max(mk, pu_[j][1]), pu_[j][3]); }
        if (!__any(mk == EMPTY)) break;
        asm volatile("" ::: "memory");
        __builtin_amdgcn_s_sleep(SLC1);
      }
      STAMPT(0, 10, 0);
      f32x4 tt = {0.f, 0.f, 0.f, 0.f};
#pragma unroll
      for (int j = 0; j < 8; ++j) tt += __builtin_bit_cast(f32x4, pu_[j]);
      const float mean = xh_sum(tt[0]) * (1.0f / 1024.0f);
      const float var = fmaxf(xh_sum(tt[1]) * (1.0f / 1024.0f) - mean * mean, 0.f);
      const float mb = rbf(mean), rs = __builtin_amdgcn_rcpf(rbf(__builtin_amdgcn_sqrtf(rbf(rbf(var) + 1e-5f))));
      float sc = rbf((xh_sum(tt[2]) - mb * xh_sum(tt[3])) * rs);
      if (!(sc == sc)) sc = -INFINITY;
      unsigned b0 = h ? 0u : mk_key(sc, rp * 128 + row), b1 = 0;
      top2_wave(b0, b1);
      const int pan = rp * 4 + wave;
      if (lane == 0) st64(ws, SY_FD + cb * 512 + pan * 8, ((u64)b1 << 32) | b0);
      STAMPT(1, 11, 0);
      unsigned nq[16];
#pragma unroll
      for (int i = 0; i < 16; ++i) {
        const unsigned dq = pack2(bflo(hb[i]) - mb, bfhi(hb[i]) - mb);
        nq[i] = pack2(bflo(dq) * rs, bfhi(dq) * rs);
      }
      const int lc0 = key_idx(b0) & 31, lc1 = key_idx(b1) & 31;
      {
        const int c0 = lc0, c1 = lc1;
        char* tb = smem + L_TB + wave * 512;
        if (s32 == c0 || s32 == c1) {
          char* tp = tb + (s32 == c0 ? 0 : 256) + h * 8;
#pragma unroll
          for (int c = 0; c < 8; ++c) { *(u32x2*)(tp + c * 16) = (u32x2){nq[2 * c], nq[2 * c + 1]}; *(u32x2*)(tp + 128 + c * 16) = (u32x2){hb[2 * c], hb[2 * c + 1]}; }
        }
        __builtin_amdgcn_wave_barrier();
        const u32x4 v4 = seal(*(const u32x4*)(tb + s32 * 16));
#pragma unroll
        for (int rr_ = 0; rr_ < XSR; rr_ += 2)
          if (rr_ + h < XSR) STW(v4, WS_XS + (rr_ + h) * 524288 + (pan * 2 + (s32 >> 4)) * 4096 + ((s32 >> 3) & 1) * 2048 + cb * 128 + (s32 & 7) * 16);
      }
      u64 cc = ~0ull;
#pragma unroll
      for (int c = 0; c < 8; ++c) {
        u32x2 ob;
#pragma unroll
        for (int i = 0; i < 2; ++i) ob[i] = pack2(bflo(hb[2 * c + i]) + bflo(nq[2 * c + i]), bfhi(hb[2 * c + i]) + bfhi(nq[2 * c + i]));
        *(LAS u32x2*)((LAS char*)(size_t)(xb + ((c ^ sw) * 16))) = ob;
      }
      auto ost = [&](int rl, bool on) {
        const int rw = wave * 32 + rl;
        const u32x4 v = *(const u32x4*)(smem + L_X2 + rw * 128 + (((lane & 7) ^ ((rw >> 1) & 7)) * 16));
        if (on) *(u32x4*)(out + (size_t)(rp * 128 + rw) * D_ + cb * 64 + (lane & 7) * 8) = v;
      };
      __builtin_amdgcn_wave_barrier();
      if (EOUT == 2 || (EOUT == 1 && wave != 0)) {
#pragma unroll
        for (int m = 0; m < 4; ++m) { const int rl = m * 8 + (lane >> 3); ost(rl, rl != lc0 && rl != lc1); }
      }
      if (wave == 0) {
        __builtin_amdgcn_s_sleep(SLD0);
#pragma nounroll
        for (unsigned s_ = 0; s_ < SPIN_CAP; ++s_) {
          cc = ld64(ws, SY_FD + cb * 512 + lane * 8);
          if (__all((unsigned)(cc >> 32) != EMPTY)) break;
          __builtin_amdgcn_s_sleep(SLD1);
        }
        STAMPT(1, 13, 0);
        unsigned c0 = (unsigned)cc, c1 = (unsigned)(cc >> 32);
        top2_wave(c0, c1);
        const unsigned w0 = key_idx(c0), w1 = key_idx(c1);
        const unsigned k0 = __builtin_amdgcn_readlane((unsigned)cc, w0 >> 5), k1 = __builtin_amdgcn_readlane((unsigned)cc, w1 >> 5);
        const unsigned sl0 = (w0 >> 5) * 2 + (k0 == c0 ? 0 : 1), sl1 = (w1 >> 5) * 2 + (k1 == c1 ? 0 : 1);
        if (lane == 0) { misc[1] = w0; misc[2] = w1; misc[3] = sl0; misc[4] = sl1; misc[5] = c0; misc[6] = c1; }
        if (has_mlp) {
          const unsigned xo = WS_XS + (idx & (XSR - 1)) * 524288u + (h ? sl1 : sl0) * 4096u + (fs & 3) * 512 + s32 * 16;
          u32x4 q;
#pragma nounroll
          for (unsigned s_ = 0; s_ < SPIN_CAP; ++s_) {
            q = LDW(xo);
            if (__all(max(q[1], q[3]) != EMPTY)) break;
            asm volatile("" ::: "memory");
            __builtin_amdgcn_s_sleep(XSL1);
          }
          *(u32x4*)(smem + L_XS + h * 528 + s32 * 16) = q;
        }
      }
      LBAR();
      STAMPT(0, 11, 0);
      const int sel0 = misc[1], sel1 = misc[2];
      if (EOUT == 2 || (EOUT == 1 && wave != 0)) {
        const int rl = (lane & 8) ? lc1 : lc0, grow = rp * 128 + wave * 32 + rl;
        ost(rl, lane < 16 && grow != sel0 && grow != sel1);
      } else {
#pragma unroll
        for (int m = 0; m < 4; ++m) { const int rl = m * 8 + (lane >> 3), grow = rp * 128 + wave * 32 + rl; ost(rl, grow != sel0 && grow != sel1); }
      }
      STAMPT(0, 12, 0);
      if (bid < 64 && wave == 0) {
        const int tk = bid >> 5, c0 = (bid & 31) * 32;
        const unsigned xo = WS_XS + (idx & (XSR - 1)) * 524288u + (unsigned)misc[3 + tk] * 4096u + c0 * 2 + (lane & 7) * 8;
        u64 xn, xh;
#pragma nounroll
        for (unsigned s_ = 0; s_ < SPIN_CAP; ++s_) {
          xn = ld64(ws, xo); xh = ld64(ws, xo + 2048);
          if (__all((unsigned)(xn >> 32) != EMPTY && (unsigned)(xh >> 32) != EMPTY)) break;
        }
        const float sg = rbf(__builtin_amdgcn_rcpf(1.0f + __builtin_amdgcn_exp2f(key_val((unsigned)misc[5 + tk]) * -1.44269504088896340736f)));
        u16* op = out + (size_t)(tk ? sel1 : sel0) * D_ + c0 + (lane & 7) * 4;
        const unsigned yo = opq(WS_YP + (lane >> 3) * 8192 + tk * 4096 + c0 * 4 + (lane & 7) * 16);
#pragma nounroll
        for (unsigned s_ = 0; s_ < SPIN_CAP; ++s_) { if (__builtin_amdgcn_readfirstlane(misc[0])) break; __builtin_amdgcn_s_sleep(1); }
        __builtin_amdgcn_s_sleep(YSL0);
        u32x4 pv[7];
#pragma nounroll
        for (unsigned s_ = 0; s_ < SPIN_CAP; ++s_) {
          unsigned mk = 0u;
#pragma unroll
          for (int p = 0; p < 7; ++p) pv[p] = LDWS(yo, p * 65536);
#pragma unroll
          for (int p = 0; p < 7; ++p) mk = max(max(mk, pv[p][1]), pv[p][3]);
          if (!__any(mk == EMPTY)) break;
          asm volatile("" ::: "memory");
          __builtin_amdgcn_s_sleep(YSL1);
        }
        STAMPT(0, 15, 0);
        f32x4 y = __builtin_bit_cast(f32x4, pv[0]);
#pragma unroll
        for (int p = 1; p < 7; ++p) y += __builtin_bit_cast(f32x4, pv[p]);
        float t[4];
#pragma unroll
        for (int i = 0; i < 4; ++i) {
          float v = y[i]; v += dppf<DPP_ROR8>(v); v = xh_sum(x16_sum(v));
          const unsigned nn = (unsigned)(xn >> (32 * (i >> 1))), hh = (unsigned)(xh >> (32 * (i >> 1)));
          t[i] = ((i & 1) ? bfhi(hh) : bflo(hh)) + rbf(((i & 1) ? bfhi(nn) : bflo(nn)) + rbf(rbf(v) * sg));
        }
        if (lane < 8) *(u32x2*)op = (u32x2){pack2(t[0], t[1]), pack2(t[2], t[3])};
        asm volatile(".Lfused_end:" ::: "memory");
        STAMPT(0, 14, 0);
      }
    }
  } else {
    __builtin_amdgcn_s_setprio(2);
    const int hw = wave - 4, mt = tid & 255;
    u32x4 wgr[8], wur[8], wdr[8];
    const int krow = hw * 64 + (lane >> 3);
    const unsigned gof = opq((unsigned)(((fs & 3) * 256 + krow) * F_ + (fs >> 2) * 64 + (lane & 7) * 8) * 2u);
    const unsigned dof = opq((unsigned)(((fs >> 2) * 64 + (lane >> 3)) * D_ + (fs & 3) * 256 + hw * 64 + (lane & 7) * 8) * 2u);
    const unsigned wto = opq((unsigned)((idx * 32 + (mt >> 3)) * 2048 + (cb >> 2) * 512 + ((mt >> 1) & 3) * 128));
#define WLOAD(dst, vo, base) asm volatile("global_load_dwordx4 %0, %1, %2" WPOL : "=v"(dst) : "v"(vo), "s"(uni(base)) : "memory")
    auto wload = [&](int i) {
      if (i < 8) WLOAD(wgr[i], gof, wg + i * (8 * F_));
      else if (i < 16) WLOAD(wur[i - 8], gof, wu + (i - 8) * (8 * F_));
      else WLOAD(wdr[i - 16], dof, wd + (i - 16) * (8 * D_));
    };
    auto wpin = [&](int i) {
      if (i < 8) asm volatile("" : "+v"(wgr[i])); else if (i < 16) asm volatile("" : "+v"(wur[i - 8])); else asm volatile("" : "+v"(wdr[i - 16]));
    };
    const int pbi = lane >> 4, pq = (lane >> 2) & 3, pch = lane & 3;
    const unsigned voW = opq((unsigned)((8 * hw + 4 * (pbi >> 1) + pq) * D_ + (pbi & 1) * 32 + (pch ^ ((pbi >> 1) * 2)) * 8) * 2u);
    auto stageW = [&](int j) { glds16(voW, uni(wo + cb * 64) + (size_t)(j ^ hrot) * (32 * D_ * 2), lds0 + wslot(j) + hw * 1024); };
    u32x4 e4 = {EMPTY, EMPTY, EMPTY, EMPTY};
    asm volatile("" : "+v"(e4));
    const unsigned l16 = opq(lane * 16);
    if (hw < 2) {
      const char* baseA = uni(x + (size_t)rb * 128 * D_);
      const char* baseB = uni(wqkv + hp * 64);
      unsigned voA[2], voB[2];
      {
        const int r0 = 64 * hw + (lane >> 3), r1 = r0 + 8;
        voA[0] = (unsigned)(r0 * D_ + ((lane & 7) ^ ((r0 >> 1) & 7)) * 8) * 2u;
        voA[1] = (unsigned)(r1 * D_ + ((lane & 7) ^ ((r1 >> 1) & 7)) * 8) * 2u;
        voB[0] = (unsigned)((32 * hw + 4 * (pbi >> 1) + pq) * (3 * D_) + (pbi & 1) * 32 + pch * 8) * 2u;
        voB[1] = (unsigned)((32 * hw + 8 + 4 * (pbi >> 1) + pq) * (3 * D_) + (pbi & 1) * 32 + (pch ^ 2) * 8) * 2u;
      }
      auto stage = [&](int t) {
        const int kt = korder(t), b = t & 3;
        const char* pa = baseA + kt * 128;
        const char* pb = baseB + (size_t)kt * (64 * 3 * D_ * 2);
#pragma unroll
        for (int j = 0; j < 8; ++j) glds16(voA[j & 1] + (j >> 1) * (16 * D_ * 2), pa, lds0 + L_A + b * 16384 + (8 * hw + j) * 1024);
#pragma unroll
        for (int j = 0; j < 4; ++j) {
          const unsigned vo = voB[j & 1] + (j >> 1) * (16 * 3 * D_ * 2);
          glds16(vo + 2048, pb, lds0 + L_KV + b * 16384 + (4 * hw + j) * 1024);
          glds16(vo + 4096, pb, lds0 + L_KV + b * 16384 + 8192 + (4 * hw + j) * 1024);
          glds16(vo, pb, lds0 + L_Q + b * 8192 + (4 * hw + j) * 1024);
        }
      };
      WAITV(10); BAR();
      stage(2); stage(3);
      WAITV(40); BAR();
      for (int t = 0; t < 12; ++t) { WAITV(20); BAR(); stage(t + 4); }
      WAITV(20); BAR();
      asm volatile("s_waitcnt vmcnt(0)" :: "v"(e0) : "memory");
      BAR();
    } else {
      const int aw = hw - 2, at = tid & 127;
      const bool master = bid == 0 && aw == 0;
      unsigned hs_cur = 0, hs_g0 = 0;
      u32x4 ms_exp = {0, 0, 0, 0}, ms_go = {0, 0, 0, 0}, pm = {0, 0, 0, 0};
      const unsigned p_off = opq(master ? SY_RE + lane * 16 : (lane == 1 ? SY_GO : SY_CH) + bid * 4);
      const char* wsu = uni(ws);
#define HS_PROBE() asm volatile("global_load_dwordx4 %0, %1, %2 sc1" : "+v"(pm) : "v"(p_off), "s"(wsu) : "memory")
#define HS_WAIT() asm volatile("s_waitcnt vmcnt(0)" : "+v"(pm) :: "memory")
      bool done = false;
      auto hs_check = [&]() __attribute__((always_inline)) {
        HS_WAIT();
        if (!master) {
          const unsigned c = __builtin_amdgcn_readlane(pm[0], 0), go = __builtin_amdgcn_readlane(pm[0], 1);
          if (go != hs_g0) done = true;
          else if (c != hs_cur) { hs_cur = c; st32(ws, SY_RE + bid * 4, c); }
        } else if (!done && __all(pm[0] == ms_exp[0] && pm[1] == ms_exp[1] && pm[2] == ms_exp[2] && pm[3] == ms_exp[3])) {
          STW(ms_go, SY_GO + lane * 16);
          done = true;
        }
      };
      const bool prober = master || aw == 1;
      const int kq = lane >> 4, r = lane & 15;
      constexpr unsigned OOB = 0x80000000u;
      const unsigned ko = WS_KVP + (hp * 16 + rb) * KVB + aw * 2176;
      const unsigned vm0 = opq(lane < 32 ? rs_o + aw * 512 + lane * 16 : lane < 56 ? cs_o + aw * 384 + (lane - 32) * 16 : ko + 2048 + (lane - 56) * 16);
      u32x4 e1;
      asm volatile("s_waitcnt vmcnt(0)\n\tv_mov_b32 %0, %4\n\tv_mov_b32 %1, %5\n\tv_mov_b32 %2, %6\n\tv_mov_b32 %3, %7"
                   : "=&v"(e1[0]), "=&v"(e1[1]), "=&v"(e1[2]), "=&v"(e1[3]) : "v"(e0[0]), "v"(e0[1]), "v"(e0[2]), "v"(e0[3]) : "memory");
      hs_g0 = __builtin_amdgcn_readfirstlane(e1[0]);
      u32x4 g0 = {0u, 0u, 0u, 0u};
      BAR();
      if (master) { ms_exp = e1 + 1u; STW(ms_exp, SY_CH + lane * 16); g0 = LDW(SY_GO + lane * 16); }
      u32x2 rst[4];
      {
        u32x4 a[4][2];
#pragma unroll
        for (int i = 0; i < 4; ++i)
#pragma unroll
          for (int k = 0; k < 2; ++k) a[i][k] = *(const u32x4*)(smem + L_A + (64 * aw + 16 * i + r) * 128 + (((4 * k + kq) ^ ((r >> 1) & 7)) * 16));
        const f32x4 Z4 = {0.f, 0.f, 0.f, 0.f};
        const int l3 = lane & 3;
#pragma unroll
        for (int i = 0; i < 4; ++i) {
          const f32x4 s1 = mfma16(ONES4, a[i][1], mfma16(ONES4, a[i][0], Z4)), s2 = mfma16(a[i][1], a[i][1], mfma16(a[i][0], a[i][0], Z4));
          const float q = l3 == 0 ? s2[0] : l3 == 1 ? s2[1] : l3 == 2 ? s2[2] : s2[3];
          rst[i] = (u32x2){min(__float_as_uint(s1[0]), 0xFFFFFFFEu), min(__float_as_uint(q), 0xFFFFFFFEu)};
          asm volatile("" : "+v"(rst[i]));
        }
      }
      const unsigned vm1 = opq(aw ? (lane < 2 ? SY_FD + cb * 512 + rp * 32 + lane * 16 : lane == 2 ? SY_FB + rb * 256 + hp * 16 : OOB)
                                  : (has_mlp ? WS_GU + fs * 1024 + lane * 16 : OOB));
      const unsigned vrs = opq((r >> 2) == kq ? rs_o + (64 * aw + r) * 8 : OOB);
      const unsigned vc0 = opq(cs_o + aw * 384 + lane * 4), vc1 = opq(lane < 32 ? cs_o + aw * 384 + 256 + lane * 4 : OOB);
      const unsigned sg_off = opq(aw ? WS_CS + hp * (16 * 768) + min(lane, 47) * 16 : WS_RS + rb * 16384 + lane * 16), sg_st = aw ? 768 : 1024;
      unsigned cb_[2][2];
#pragma unroll
      for (int c = 0; c < 2; ++c) {
        const unsigned o_ = lds0 + kq * 1024 + aw * 256 + (r >> 2) * 64 + ((c ^ (kq & 1)) * 32) + (lane & 3) * 8;
        cb_[0][c] = opq(o_ + L_Q + (tcs & 3) * 8192); cb_[1][c] = opq(o_ + L_KV + (tcs & 3) * 16384);
      }
      const int hq = (3 - (int)((size_t)wqkv >> 7) - (hp & ~3)) & 7;
      const int qln = ((((hp & 3) - hq - 1) & 3) * 8 + (idx & 7)) * 64 + (at >> 1);
      const unsigned vq = opq((unsigned)((((kph + 8) * 64 + qln / 3) & 1023) * 6144 + (qln % 3) * 2048 + ((hp & ~3) + (hq & 3)) * 128));
      const unsigned lw0 = opq(lds0 + (aw ? L_CS + lane * 16 : L_SA + lane * 8)), lw1 = opq(lds0 + L_WR + (lane & 7) * 16);
      const unsigned vcp = opq(min((unsigned)(idx * CODE_PER + min(at >> 3, CODE_PER - 1)) * 128u, clen));
      BAR();
      ms_go = g0 + 1u;
      STW(e4, vm0); STW(e4, vm1); STWS(e4, l16, ko); STWS(e4, l16, ko + 1024);
      BAR();
      STAMPT(2, 0, 384);
      if (aw == 1) HS_PROBE();
      HS_WAIT();
      BAR();
      STAMPT(2, 1, 384);
      if (aw == 1) { hs_cur = __builtin_amdgcn_readlane(pm[0], 0); st32(ws, SY_RE + bid * 4, hs_cur); }
#pragma unroll
      for (int i = 0; i < 4; ++i) __builtin_amdgcn_raw_buffer_store_b64(rst[i], wsr, vrs + i * 128, 0, 16);
      unsigned qt = 0;
      if (hq < 4 && (hp & 3) != hq) asm volatile("global_load_dword %0, %1, %2" : "=v"(qt) : "v"(vq), "s"(uni(wqkv)) : "memory");
      BAR();
      STAMPT(2, 2, 384);
      if (prober) HS_PROBE();
      unsigned cv0, cv1;
      {
        f32x4 cacc[6];
#pragma unroll
        for (int f = 0; f < 6; ++f) {
          cacc[f] = (f32x4){0.f, 0.f, 0.f, 0.f};
#pragma unroll
          for (int k = 0; k < 2; ++k) {
            const int o_ = (f >= 4 ? 8192 : 0) + k * 4096;
            cacc[f] = mfma16(ONES4, cat(trread(cb_[f >= 2][f & 1], o_), trread(cb_[f >= 2][f & 1], o_ + 512)), cacc[f]);
          }
        }
        cv0 = min(__float_as_uint(kq == 0 ? cacc[0][0] : kq == 1 ? cacc[1][0] : kq == 2 ? cacc[2][0] : cacc[3][0]), 0xFFFFFFFEu);
        cv1 = min(__float_as_uint(kq == 0 ? cacc[4][0] : cacc[5][0]), 0xFFFFFFFEu);
        asm volatile("" : "+v"(cv0), "+v"(cv1));
      }
      BAR();
      STAMPT(2, 3, 384);
      HS_WAIT();
      asm volatile("" :: "v"(qt));
      if (prober) hs_check();
      if (master) HS_PROBE();
      __builtin_amdgcn_raw_buffer_store_b32(cv0, wsr, vc0, 0, 16); __builtin_amdgcn_raw_buffer_store_b32(cv1, wsr, vc1, 0, 16);
      STWS(e4, l16, WS_ST2 + (rp * 16 + cb) * 2048 + aw * 1024);
      BAR();
      STAMPT(2, 4, 384);
      if (master) { hs_check(); HS_PROBE(); }
      BAR();
      STAMPT(2, 5, 384);
      if (master) { hs_check(); HS_PROBE(); }
      BAR();
      STAMPT(2, 6, 384);
      if (master) hs_check();
      if (prober) HS_PROBE();
      BAR();
      STAMPT(2, 7, 384);
      if (prober) {
        hs_check();
#pragma nounroll
        for (unsigned s_ = 0; s_ < SPIN_CAP && !done; ++s_) { HS_PROBE(); hs_check(); }
      }
      BAR();
      STAMPT(2, 8, 384);
      unsigned cpf = 0;
      if (CPF2) asm volatile("global_load_dword %0, %1, %2" : "=v"(cpf) : "v"(vcp), "s"(pc0) : "memory");
      u32x4 sg[16], wrv;
      asm volatile("global_load_dwordx4 %0, %1, %2" : "=v"(wrv) : "v"(l16 & 127u), "s"(uni(w_r + cb * 64)) : "memory");
#pragma unroll
      for (int i = 0; i < 16; ++i) {
        asm volatile("global_load_dwordx4 %0, %1, %2 sc1" : "=v"(sg[i]) : "v"(sg_off), "s"(wsu + i * sg_st) : "memory");
        if (i == SGS) { BAR(); STAMPT(2, 9, 384); }
      }
      auto tsum = [&](int o) __attribute__((always_inline)) {
        f32x4 t[4];
#pragma unroll
        for (int i = 0; i < 4; ++i) t[i] = add4(__builtin_bit_cast(f32x4, sg[o + i]), __builtin_bit_cast(f32x4, sg[o + 4 + i]));
        return add4(add4(t[0], t[2]), add4(t[1], t[3]));
      };
      WAITV(8);
#pragma unroll
      for (int i = 0; i < 8; ++i) asm volatile("" : "+v"(sg[i]));
      asm volatile("" : "+v"(wrv) : "v"(cpf));
      f32x4 sm = tsum(0), sn;
      asm volatile("" : "+v"(sm));
      BAR();
      STAMPT(2, 10, 384);
      WAITV(0);
#pragma unroll
      for (int i = 8; i < 16; ++i) asm volatile("" : "+v"(sg[i]));
      sn = tsum(8);
      asm volatile("" : "+v"(sn));
      BAR();
      STAMPT(2, 11, 384);
      sm = add4(sm, sn);

      const float chk = (sm[0] + sm[1]) + (sm[2] + sm[3]);
      if (__builtin_expect(__any(chk != chk), 0)) {
#pragma nounroll
        for (unsigned s_ = 0; s_ < SPIN_CAP; ++s_) {
          unsigned mx = 0;
#pragma unroll
          for (int i = 0; i < 16; ++i) { sg[i] = LDWS(sg_off, i * sg_st); mx = max(max(mx, max(sg[i][0], sg[i][1])), max(sg[i][2], sg[i][3])); }
          if (!__any(mx == EMPTY)) break;
          asm volatile("" ::: "memory");
        }
        sm = add4(tsum(0), tsum(8));
      }
      STAMPT(1, 1, 384);
      BAR();
      STAMPT(2, 12, 384);
      float mean[2], vr[2];
      if (aw == 1) {
        *(LAS f32x4*)(size_t)lw0 = lane < 48 ? sm : (f32x4){0.f, 0.f, 0.f, 0.f};
        *(LAS u32x4*)(size_t)lw1 = wrv;
      } else {
#pragma unroll
        for (int e = 0; e < 2; ++e) {
          mean[e] = sm[2 * e] * (1.0f / 1024.0f);
          vr[e] = fmaxf(sm[2 * e + 1] * (1.0f / 1024.0f) - mean[e] * mean[e], 0.f);
          asm volatile("" : "+v"(mean[e]), "+v"(vr[e]));
        }
      }
      LBAR();
      STAMPT(2, 13, 384);
      if (aw == 0) {
        f32x2 ra, rb_;
#pragma unroll
        for (int e = 0; e < 2; ++e) {
          ra[e] = __builtin_amdgcn_rcpf(rbf(__builtin_amdgcn_sqrtf(rbf(rbf(vr[e]) + 1e-5f))));
          rb_[e] = -rbf(mean[e]) * ra[e];
        }
        *(LAS f32x2*)(size_t)lw0 = ra; *(LAS f32x2*)(size_t)(lw0 + 512) = rb_;
        asm volatile("s_waitcnt lgkmcnt(0)" ::: "memory");
        misc[7] = 1;
      }
      STAMPT(1, 2, 384);
    }
    unsigned wt;
    asm volatile("global_load_dword %0, %1, %2" : "=v"(wt) : "v"(wto), "s"(uni(wo)) : "memory");
#pragma unroll
    for (int i = 0; i < NW1; ++i) { WAITV(THR - 1); wload(i); }
    u32x4 wr_[20];
#define RLOAD(dst, vo, base) asm volatile("global_load_dwordx4 %0, %1, %2" : "=v"(dst) : "v"(vo), "s"(uni(base)) : "memory")
#pragma unroll
    for (int i = 0; i < NR; ++i) {
      WAITV(THR - 1);
      RLOAD(wr_[i], voW, uni(wo + cb * 64) + (size_t)((i + 12) ^ hrot) * (32 * D_ * 2));
      if (i % 6 == 0) STAMPT(3, 8 + i / 6, 256);
    }
#pragma nounroll
    for (unsigned s_ = 0; s_ < SPIN_CAP; ++s_) if (__all(misc[28 + (lane & 3)] != 0)) break;
#pragma unroll
    for (int j = 0; j < NE; ++j) { WAITV(THR - 1); stageW(j); }
    WAITV(0);
#pragma unroll
    for (int i = 0; i < NR; ++i) asm volatile("" : "+v"(wr_[i]));
#pragma unroll
    for (int i = 0; i < NW1; ++i) wpin(i);
    asm volatile("" :: "v"(wt));
    STAMPT(1, 3, 256);
    {
      u32x4 pv[16], zq;
      const unsigned ko = WS_KVP + hp * 16 * KVB + (hw >> 1) * 2176;
      unsigned kl = ko + (hw & 1) * 1024 + lane * 16, zo = ko + ((hw & 1) * 8 + (lane >> 3)) * KVB + 2048 + (lane & 7) * 16;
      u32x4 sel[2];
#pragma unroll
      for (int e = 0; e < 2; ++e)
#pragma unroll
        for (int i = 0; i < 4; ++i)
          sel[e][i] = ((lane & 15) >> 2) != (lane >> 4) || (i >> 1) != e || (i & 1) != ((lane & 3) >> 1) ? 0u : (lane & 1) ? 0x3F800000u : 0x3F80u;
      f32x4 s[2][2];
      auto ksum = [&]() __attribute__((always_inline)) {
#pragma unroll
        for (int p = 0; p < 16; ++p) {
#pragma unroll
          for (int e = 0; e < 2; ++e) s[p >> 3][e] = mfma16(sel[e], pv[p], (p & 7) ? s[p >> 3][e] : (f32x4){0.f, 0.f, 0.f, 0.f});
          SB0();
        }
        s[0][0] += s[1][0]; s[0][1] += s[1][1];
      };
      asm volatile("" : "+v"(sel[0]), "+v"(sel[1]), "+v"(kl), "+v"(zo));
#pragma nounroll
      for (unsigned s_ = 0; s_ < SPIN_CAP; ++s_) { if (__builtin_amdgcn_readfirstlane(misc[16 + hw]) == 2) break; __builtin_amdgcn_s_sleep(1); }
      zq = LDW(zo);
#pragma unroll
      for (int p = 0; p < 16; ++p) { pv[p] = LDWS(kl + (p & 7) * KVB, ((p >> 3) ^ (idx & 1)) * (8 * KVB)); SB0(); }
      ksum();
      const float chk = s[0][0][0] + __uint_as_float(zq[1]) + __uint_as_float(zq[3]);
      if (__any(chk != chk)) {
        unsigned need = 0x1FFFFu;
#pragma nounroll
        for (unsigned s_ = 0; s_ < SPIN_CAP; ++s_) {
#pragma unroll
          for (int p = 0; p < 16; ++p) if ((need & (1u << p)) && !__any(max(pv[p][1], pv[p][3]) == EMPTY)) need &= ~(1u << p);
          if (!__any(max(zq[1], zq[3]) == EMPTY)) need &= ~0x10000u;
          if (!need) break;
          asm volatile("" ::: "memory");
#pragma unroll
          for (int p = 0; p < 16; ++p) if (need & (1u << p)) pv[p] = LDWS(kl + (p & 7) * KVB, ((p >> 3) ^ (idx & 1)) * (8 * KVB));
          if (need & 0x10000u) zq = LDW(zo);
        }
        ksum();
      }
      STAMPT(1, 4, 256);
      *(u32x4*)(smem + L_KVL + hw * 1024 + lane * 16) =
          (u32x4){pack2(s[0][0][0], s[0][0][1]), pack2(s[0][0][2], s[0][0][3]), pack2(s[0][1][0], s[0][1][1]), pack2(s[0][1][2], s[0][1][3])};
      f32x4 z = __builtin_bit_cast(f32x4, zq);
#pragma unroll
      for (int i = 0; i < 4; ++i) { float v = z[i]; v += dppf<DPP_ROR8>(v); z[i] = xh_sum(x16_sum(v)); }
      if (lane < 8) *(f32x4*)(smem + L_ZL + hw * 128 + lane * 16) = z;
    }
    LBAR();
#pragma unroll
    for (int j = NE; j < 32; ++j) if (j < 12 || j >= NR + 12) { WAITV(THR3 - 1); stageW(j); }
#pragma nounroll
    for (unsigned s_ = 0; s_ < SPIN_CAP; ++s_) { if (__all(misc[24 + (lane & 3)] != 0)) break; __builtin_amdgcn_s_sleep(1); }
#pragma unroll
    for (int i = 0; i < NR; ++i) *(u32x4*)(smem + wslot(i + 12) + hw * 1024 + lane * 16) = wr_[i];
    asm volatile("s_waitcnt lgkmcnt(0)" ::: "memory");
    {
      const u32x4 e5 = {EMPTY, EMPTY, EMPTY, EMPTY};
      if (has_mlp && hw >= 2) STW(e5, WS_YP + (fs >> 2) * 8192 + (hw & 1) * 4096 + (fs & 3) * 1024 + lane * 16);
#pragma unroll
      for (int rr_ = 0; rr_ < XSR; rr_ += 2)
        if (rr_ + (hw >> 1) < XSR) STW(e5, WS_XS + (rr_ + (hw >> 1)) * 524288 + (rp * 16 + ((mt >> 3) & 15)) * 2048 + cb * 128 + (mt & 7) * 16);
    }
    WAITV(0);
    STAMPT(1, 6, 256);
    if (hw == 0) {
      __builtin_amdgcn_s_sleep(SLB);
#pragma nounroll
      for (unsigned s_ = 0; s_ < SPIN_CAP; ++s_) { if (__all(ld32(ws, SY_FB + rp * 256 + lane * 4) != EMPTY)) break; __builtin_amdgcn_s_sleep(SLB1); }
    }
    BAR();
#pragma nounroll
    for (unsigned s_ = 0; s_ < SPIN_CAP; ++s_) { if (__builtin_amdgcn_readfirstlane(misc[20 + hw])) break; __builtin_amdgcn_s_sleep(2); }
    unsigned cp3 = 0;
    if (CPF3) asm volatile("global_load_dword %0, %1, %2" : "=v"(cp3) : "v"(min((unsigned)(idx * CODE_PER + min(mt >> 4, CODE_PER - 1)) * 128u, clen)), "s"(pc0) : "memory");
#pragma unroll
    for (int i = NW1; i < 24; ++i) { WAITV(THR2 - 1); wload(i); }
    WAITV(0);
    asm volatile("" :: "v"(cp3));
#pragma unroll
    for (int i = NW1; i < 24; ++i) wpin(i);
    STAMPT(1, 7, 256);
    const int c16 = lane & 15, kq = lane >> 4;
    u32x4 gf[2][8];
    u32x4 df[4][2];
    {
#pragma nounroll
      for (unsigned s_ = 0; s_ < SPIN_CAP; ++s_) { if (__all(misc[8 + (lane & 3)] != 0)) break; __builtin_amdgcn_s_sleep(1); }
      STAMPT(3, 5, 256);
#pragma unroll
      for (int i = 0; i < 8; ++i) {
        const int go = hw * 8192 + i * 1024 + ((lane ^ (((lane >> 4) & 1) * 2 + (i & 1) * 4)) * 16);
        *(u32x4*)(smem + go) = wgr[i];
        *(u32x4*)(smem + 32768 + go) = wur[i];
        *(u32x4*)(smem + L_DW + hw * 8192 + i * 1024 + ((lane ^ (((lane >> 4) & 3) * 2)) * 16)) = wdr[i];
      }
      asm volatile("s_waitcnt lgkmcnt(0)" ::: "memory");
      STAMPT(3, 6, 256);
      misc[12 + hw] = 1;
#pragma nounroll
      for (unsigned s_ = 0; s_ < SPIN_CAP; ++s_) if (__all(misc[12 + (lane & 3)] != 0)) break;
      asm volatile("" ::: "memory");
      STAMPT(3, 7, 256);
      const unsigned gb = opq(lds0 + (kq * 8 + (c16 >> 2)) * 128 + (hw ^ ((c16 >> 3) + (kq & 1) * 2)) * 32 + (lane & 3) * 8);
      const unsigned db = lds0 + L_DW + hw * 8192 + (kq * 4 + (c16 >> 2)) * 128 + (lane & 3) * 8, dsw = (c16 >> 3) + (kq & 1) * 2;
#pragma unroll
      for (int m = 0; m < 2; ++m)
#pragma unroll
        for (int k = 0; k < 8; ++k) gf[m][k] = cat(trread(gb, m * 32768 + k * 4096), trread(gb, m * 32768 + k * 4096 + 512));
#pragma unroll
      for (int t = 0; t < 4; ++t) {
        const unsigned dt = opq(db + (t ^ dsw) * 32);
#pragma unroll
        for (int e = 0; e < 2; ++e) df[t][e] = cat(trread(dt, ((t + RSG * 2 * e) & 3) * 2048), trread(dt, ((t + RSG * (2 * e + 1)) & 3) * 2048));
      }
      SB0();
#pragma unroll
      for (int k = 0; k < 8; ++k) asm volatile("" : "+v"(gf[0][k]), "+v"(gf[1][k]), "+v"(df[k >> 1][k & 1]));
    }
    const unsigned gu_st = opq(c16 < 4 ? WS_GU + fs * 1024 + hw * 256 + c16 * 64 + kq * 16 : 0x80000000u);
    const unsigned gu_ld = opq(WS_GU + (fs & ~3) * 1024 + c16 * 64 + kq * 16);
    const unsigned yp_st = opq((c16 & 2) ? 0x80000000u : WS_YP + (fs >> 2) * 8192 + (c16 & 1) * 4096 + (fs & 3) * 1024 + hw * 256 + (c16 >> 2) * 64 + kq * 16);
    const unsigned xsb = opq(lds0 + L_XS + (c16 & 1) * 528 + kq * 16);
    STAMPT(1, 8, 256);
    BAR();
    if (has_mlp) {
      f32x4 ga[2] = {{0.f, 0.f, 0.f, 0.f}, {0.f, 0.f, 0.f, 0.f}};
      u32x4 xf[8];
#pragma unroll
      for (int k = 0; k < 8; ++k) xf[k] = ldsr(xsb, k * 64);
#pragma unroll
      for (int k = 0; k < 8; ++k) { ga[0] = mfma16(gf[0][k], xf[k], ga[0]); ga[1] = mfma16(gf[1][k], xf[k], ga[1]); }
      STW(seal(__builtin_bit_cast(u32x4, (c16 & 2) ? ga[1] : ga[0])), gu_st);
      STAMPT(1, 14, 256);
      __builtin_amdgcn_s_sleep(SLG0);
      u32x4 q[4];
#pragma nounroll
      for (unsigned s_ = 0; s_ < SPIN_CAP; ++s_) {
        unsigned mk = 0u;
#pragma unroll
        for (int p = 0; p < 4; ++p) q[p] = LDW(gu_ld + p * 1024);
#pragma unroll
        for (int p = 0; p < 4; ++p) mk = max(max(mk, q[p][1]), q[p][3]);
        if (!__any(mk == EMPTY)) break;
        asm volatile("" ::: "memory");
        __builtin_amdgcn_s_sleep(SLG1);
      }
      STAMPT(1, 15, 256);
      const f32x4 tot = (__builtin_bit_cast(f32x4, q[0]) + __builtin_bit_cast(f32x4, q[1])) + (__builtin_bit_cast(f32x4, q[2]) + __builtin_bit_cast(f32x4, q[3]));
      float av[4];
#pragma unroll
      for (int i = 0; i < 4; ++i) {
        const float v = rbf(tot[i]), o_ = dppf<DPP_X2>(v);
        const float gs = (c16 & 2) ? o_ : v, us = (c16 & 2) ? v : o_;
        av[i] = rbf(gs * __builtin_amdgcn_rcpf(1.0f + __builtin_amdgcn_exp2f(gs * -1.44269504088896340736f))) * us;
      }
      const unsigned a0 = pack2(av[0], av[1]), a1 = pack2(av[2], av[3]);
      const u32x4 b0 = {a0, a1, dppu<DPP_ROR4>(a0), dppu<DPP_ROR4>(a1)}, b1 = {dppu<DPP_ROR8>(a0), dppu<DPP_ROR8>(a1), dppu<DPP_ROR12>(a0), dppu<DPP_ROR12>(a1)};
      f32x4 y[4];
#pragma unroll
      for (int t = 0; t < 4; ++t) y[t] = mfma16(df[t][0], b0, (f32x4){0.f, 0.f, 0.f, 0.f});
#pragma unroll
      for (int t = 0; t < 4; ++t) y[t] = mfma16(df[t][1], b1, y[t]);
      const int T = c16 >> 2;
      STW(seal(__builtin_bit_cast(u32x4, T == 0 ? y[0] : T == 1 ? y[1] : T == 2 ? y[2] : y[3])), yp_st);
      STAMPT(0, 13, 256);
      misc[0] = 1;
    }
  }
}

extern "C" void kernel_launch(void* const* d_in, const int* in_sizes, int n_in, void* d_out, int out_size,
                              void* d_ws, size_t ws_size, hipStream_t stream) {
  (void)hipFuncSetAttribute((const void*)fused, hipFuncAttributeMaxDynamicSharedMemorySize, LDS_TOTAL);
  fused<<<256, 512, LDS_TOTAL, stream>>>((const u16*)d_in[0], (const u16*)d_in[1], (const u16*)d_in[2], (const u16*)d_in[4],
                                         (const u16*)d_in[5], (const u16*)d_in[6], (const u16*)d_in[7], (u16*)d_out, (char*)d_ws);
}
```

```cpp
#include <hip/hip_runtime.h>
#include <stdint.h>

#define LAS __attribute__((address_space(3)))
#define GAS __attribute__((address_space(1)))
#define DI __device__ __forceinline__

#define XSR 1
#define GPF 5
#define GOW 14
#define NE 4
#define HRM 3
#define OW0 4
#define SLC0 30
#define SLC1 8
#define SLD0 0
#define SLB1 0
#define HRS 4
#define SLD1 0
#define SGS 7
#define GB 1
#define NR 20
#define THR3 4
#define THR2 6
#define THR 6
#define NW1 10
#define SLB 0
constexpr int CODE_PER = 11;
constexpr int XSL1 = 2;
#define YSL0 12
#define YSL1 4
#define SLG0 14
#define SLG1 2
typedef unsigned short u16;
typedef unsigned long long u64;
typedef __attribute__((ext_vector_type(2))) __bf16 bf16x2;
typedef __attribute__((ext_vector_type(8))) __bf16 bf16x8;
typedef __attribute__((ext_vector_type(4))) short s16x4;
typedef __attribute__((ext_vector_type(16))) float f32x16;
typedef __attribute__((ext_vector_type(4))) float f32x4;
typedef __attribute__((ext_vector_type(2))) float f32x2;
typedef __attribute__((ext_vector_type(4))) unsigned u32x4;
typedef __attribute__((ext_vector_type(2))) unsigned u32x2;
typedef GAS unsigned gu32;
typedef GAS unsigned long long gu64;

constexpr int S_ = 2048, D_ = 1024, F_ = 3584;

constexpr unsigned SY_CH = 0, SY_GO = 1024, SY_RE = 2048, SY_FB = 4096, SY_FD = 8192;
constexpr unsigned WS_RS  = 16384;
constexpr unsigned WS_CS  = WS_RS + 256 * 1024;
constexpr unsigned KVB    = 4352;
constexpr unsigned WS_KVP = WS_CS + 256 * 768;
constexpr unsigned WS_ST2 = WS_KVP + 256 * KVB;
constexpr unsigned WS_GU  = WS_ST2 + 256 * 2048;
constexpr unsigned WS_XS  = WS_GU + 256 * 1024;
constexpr unsigned WS_YP  = WS_XS + XSR * 524288;
constexpr unsigned WS_O   = WS_YP + 56 * 8192;
constexpr unsigned WS_END = WS_O + S_ * D_ * 2;
constexpr size_t WS_DIAG  = 64u << 20;
#define STAMPT(kid, i, T) do {} while (0)

DI float bflo(unsigned u) { return __uint_as_float(u << 16); }
DI float bfhi(unsigned u) { return __uint_as_float(u & 0xffff0000u); }
DI float rbf(float x) { return (float)(__bf16)x; }
DI unsigned pack2(float a, float b) { bf16x2 c; c[0] = (__bf16)a; c[1] = (__bf16)b; return __builtin_bit_cast(unsigned, c); }
DI f32x4 mfma16(u32x4 a, u32x4 b, f32x4 c) {
  return __builtin_amdgcn_mfma_f32_16x16x32_bf16(__builtin_bit_cast(bf16x8, a), __builtin_bit_cast(bf16x8, b), c, 0, 0, 0);
}
DI f32x16 mfma32(u32x4 a, u32x4 b, f32x16 c) {
  return __builtin_amdgcn_mfma_f32_32x32x16_bf16(__builtin_bit_cast(bf16x8, a), __builtin_bit_cast(bf16x8, b), c, 0, 0, 0);
}
DI u32x2 trread(const char* p) {
  s16x4 v = __builtin_amdgcn_ds_read_tr16_b64_v4i16((LAS s16x4*)p);
  return __builtin_bit_cast(u32x2, v);
}
DI unsigned opq(unsigned v) { asm volatile("" : "+v"(v)); return v; }
DI u32x2 trread(unsigned base, int off) {
  s16x4 v = __builtin_amdgcn_ds_read_tr16_b64_v4i16((LAS s16x4*)((LAS char*)(size_t)base + off));
  return __builtin_bit_cast(u32x2, v);
}
DI u32x4 ldsr(unsigned base, int off) { return *(const LAS u32x4*)((const LAS char*)(size_t)base + off); }
DI u32x4 cat(u32x2 a, u32x2 b) { return (u32x4){a[0], a[1], b[0], b[1]}; }
DI f32x4 add4(f32x4 a, f32x4 b) {
  f32x4 c;
#pragma unroll
  for (int i = 0; i < 4; ++i) asm("v_add_f32 %0, %1, %2" : "=v"(c[i]) : "v"(a[i]), "v"(b[i]));
  return c;
}
DI float dot2(unsigned a, unsigned b, float c) {
  return __builtin_amdgcn_fdot2_f32_bf16(__builtin_bit_cast(bf16x2, a), __builtin_bit_cast(bf16x2, b), c, false);
}
DI const char* uni(const void* p) {
  const u64 v = (u64)p;
  return (const char*)(((u64)(unsigned)__builtin_amdgcn_readfirstlane((int)(v >> 32)) << 32) | (unsigned)__builtin_amdgcn_readfirstlane((int)v));
}
DI void glds16(unsigned voff, const void* sbase, unsigned lds) {
  asm volatile("s_mov_b32 m0, %2\n\ts_nop 0\n\tglobal_load_lds_dwordx4 %0, %1" ::"v"(voff), "s"(uni(sbase)), "s"(__builtin_amdgcn_readfirstlane(lds)) : "memory");
}
DI float xh_sum(float v) {
  auto r = __builtin_amdgcn_permlane32_swap(__float_as_uint(v), __float_as_uint(v), false, false);
  return __uint_as_float(r[0]) + __uint_as_float(r[1]);
}
template <int CTRL> DI unsigned dppu(unsigned v) { return (unsigned)__builtin_amdgcn_update_dpp(0, (int)v, CTRL, 0xf, 0xf, true); }
template <int CTRL> DI float dppf(float v) { return __uint_as_float(dppu<CTRL>(__float_as_uint(v))); }
constexpr int DPP_X1 = 0xB1, DPP_X2 = 0x4E, DPP_HMIR = 0x141, DPP_MIR = 0x140, DPP_ROR8 = 0x128, DPP_ROR4 = 0x124, DPP_ROR12 = 0x12C;
DI float x16_sum(float v) {
  auto r = __builtin_amdgcn_permlane16_swap(__float_as_uint(v), __float_as_uint(v), false, false);
  return __uint_as_float(r[0]) + __uint_as_float(r[1]);
}
DI unsigned phi_pair(f32x2 t) {
  const f32x2 u = t + 1.000001f;
  const f32x2 l = t * 1.44269504088896340736f;
  const f32x2 e = (f32x2){__builtin_amdgcn_exp2f(l[0]), __builtin_amdgcn_exp2f(l[1])} + 1e-6f;
  return pack2(__builtin_amdgcn_fmed3f(e[0], u[0], 1.000001f), __builtin_amdgcn_fmed3f(e[1], u[1], 1.000001f));
}
#define RLX_AGENT __ATOMIC_RELAXED, __HIP_MEMORY_SCOPE_AGENT
DI void st32(char* ws, unsigned off, unsigned v) { __hip_atomic_store((gu32*)(ws + off), v, RLX_AGENT); }
DI void st64(char* ws, unsigned off, u64 v) { __hip_atomic_store((gu64*)(ws + off), v, RLX_AGENT); }
DI unsigned ld32(char* ws, unsigned off) { return __hip_atomic_load((gu32*)(ws + off), RLX_AGENT); }
DI u64 ld64(char* ws, unsigned off) { return __hip_atomic_load((gu64*)(ws + off), RLX_AGENT); }
#define WAITV(n) asm volatile("s_waitcnt vmcnt(%0)" :: "n"(n) : "memory")
#define BAR() do { asm volatile("" ::: "memory"); __builtin_amdgcn_s_barrier(); asm volatile("" ::: "memory"); } while (0)
#define LBAR() do { asm volatile("s_waitcnt lgkmcnt(0)" ::: "memory"); __builtin_amdgcn_s_barrier(); asm volatile("" ::: "memory"); } while (0)
#define SB0() __builtin_amdgcn_sched_barrier(0)
constexpr unsigned SPIN_CAP = 1u << 22;
constexpr unsigned EMPTY = 0xFFFFFFFFu;
DI u32x4 seal(u32x4 v) { v[1] = min(v[1], 0xFFFFFFFEu); v[3] = min(v[3], 0xFFFFFFFEu); return v; }
DI unsigned mk_key(float v, unsigned idx) {
  const unsigned u = __float_as_uint(v) >> 16;
  const unsigned o = (u & 0x8000u) ? (~u & 0xffffu) : (u | 0x8000u);
  return (o << 16) | (0xFFFFu - idx);
}
DI unsigned key_idx(unsigned k) { return (0xFFFFu - (k & 0xFFFFu)) & (S_ - 1); }
DI float key_val(unsigned k) { const unsigned o = k >> 16; return __uint_as_float(((o & 0x8000u) ? (o & 0x7fffu) : (~o & 0xffffu)) << 16); }
DI void top2_mix(unsigned& b0, unsigned& b1, unsigned c0, unsigned c1) {
  const unsigned lo = min(b0, c0), mx = max(b1, c1);
  b0 = max(b0, c0); b1 = max(lo, mx);
}
DI void top2_wave(unsigned& b0, unsigned& b1) {
  top2_mix(b0, b1, dppu<DPP_X1>(b0), dppu<DPP_X1>(b1));
  top2_mix(b0, b1, dppu<DPP_X2>(b0), dppu<DPP_X2>(b1));
  top2_mix(b0, b1, dppu<DPP_HMIR>(b0), dppu<DPP_HMIR>(b1));
  top2_mix(b0, b1, dppu<DPP_MIR>(b0), dppu<DPP_MIR>(b1));
  { auto p = __builtin_amdgcn_permlane16_swap(b0, b0, false, false); auto q = __builtin_amdgcn_permlane16_swap(b1, b1, false, false);
    b0 = p[0]; b1 = q[0]; top2_mix(b0, b1, p[1], q[1]); }
  { auto p = __builtin_amdgcn_permlane32_swap(b0, b0, false, false); auto q = __builtin_amdgcn_permlane32_swap(b1, b1, false, false);
    b0 = p[0]; b1 = q[0]; top2_mix(b0, b1, p[1], q[1]); }
}

constexpr int L_A = 0, L_Q = 65536, L_KV = 98304, LDS_TOTAL = 163840;
constexpr int L_M  = L_KV;
constexpr int L_SA = L_M, L_SB = L_M + 512, L_CS = L_M + 1024;
constexpr int L_MISC = L_M + 1792;
constexpr int L_XCH = L_M + 2048;
constexpr int L_KVL = L_M + 11264;
constexpr int L_ZL = L_M + 15360;
constexpr int L_XS = L_M + 3072;
constexpr int L_TB = L_M + 9472;
constexpr int L_WR = L_M + 15872;
constexpr int L_DW = 114688;
constexpr int L_X2 = 81920;
DI constexpr int wslot(int j) { return j < 12 ? 114688 + j * 4096 : (j - 12) * 4096; }

__global__ __launch_bounds__(512, 2) void fused(const u16* __restrict__ x, const u16* __restrict__ wqkv,
                                                const u16* __restrict__ wo, const u16* __restrict__ w_r,
                                                const u16* __restrict__ wg, const u16* __restrict__ wu,
                                                const u16* __restrict__ wd, u16* __restrict__ out, char* __restrict__ ws) {
  extern __shared__ __attribute__((aligned(16))) char smem[];
  u64 pc0; unsigned clen;
  asm volatile("s_getpc_b64 %0\n.Lfused_pc:\n\ts_mov_b32 %1, .Lfused_end-.Lfused_pc-16" : "=s"(pc0), "=s"(clen));
  SB0();
  const int tid = threadIdx.x, lane = tid & 63;
  const int wave = __builtin_amdgcn_readfirstlane(tid >> 6);
  STAMPT(0, 0, 0);
  const int bid = blockIdx.x, xcd = bid & 7, idx = bid >> 3;
  const int rb = (xcd & 1) * 8 + (idx & 7);
  const int hp = (xcd >> 1) * 4 + (idx >> 3);
  const int rp = rb, cb = ((3 + (xcd >> 1) + 2 * (xcd & 1)) & 3) * 4 + (hp & 3);
  const int hrot = HRS * (cb & HRM);
  const unsigned lds0 = (unsigned)(size_t)smem;
  const __amdgpu_buffer_rsrc_t wsr = __builtin_amdgcn_make_buffer_rsrc(ws, 0, WS_END, 0x00020000);
#define LDW(off) __builtin_amdgcn_raw_buffer_load_b128(wsr, (off), 0, 16)
#define LDWS(off, so) __builtin_amdgcn_raw_buffer_load_b128(wsr, (off), (so), 16)
#define STW(v, off) __builtin_amdgcn_raw_buffer_store_b128((v), wsr, (off), 0, 16)
#define STWS(v, off, so) __builtin_amdgcn_raw_buffer_store_b128((v), wsr, (off), (so), 16)
  const int kph = 4 * (xcd >> 1) + 8 * (xcd & 1), kg_ = (hp + rb) & 3;
  auto korder = [&](int t) { return ((t + kph) & 15) ^ kg_; };
  u32x4 e0;
  {
    const int r0 = 16 * wave + (lane >> 3), r1 = r0 + 8, pb_ = lane >> 4;
    const unsigned a0 = opq((unsigned)(r0 * D_ + ((lane & 7) ^ ((r0 >> 1) & 7)) * 8) * 2u), a1 = opq((unsigned)(r1 * D_ + ((lane & 7) ^ ((r1 >> 1) & 7)) * 8) * 2u);
    const unsigned b0 = opq((unsigned)((8 * wave + 4 * (pb_ >> 1) + ((lane >> 2) & 3)) * (3 * D_) + (pb_ & 1) * 32 + ((lane & 3) ^ ((wave & 1) * 2)) * 8) * 2u);
    SB0();
    auto first = [&](int t, int dw) {
      const char* pa = uni(x + (size_t)(rb * 128 + 16 * dw) * D_) + korder(t) * 128;
      const char* pb = uni(wqkv + (size_t)(8 * dw) * (3 * D_) + hp * 64) + (size_t)korder(t) * (64 * 3 * D_ * 2);
      const int w = wave + dw;
      glds16(a0, pa, lds0 + L_A + t * 16384 + w * 2048); glds16(a1, pa, lds0 + L_A + t * 16384 + w * 2048 + 1024);
      glds16(b0 + 2048, pb, lds0 + L_KV + t * 16384 + w * 1024); glds16(b0 + 4096, pb, lds0 + L_KV + t * 16384 + 8192 + w * 1024);
      glds16(b0, pb, lds0 + L_Q + t * 8192 + w * 1024);
    };
    STAMPT(3, 0, 0);
    first(0, 0);
    STAMPT(3, 4, 0);
    SB0();
    {
      const char* eb = wave < 4 ? (const char*)pc0 : uni(ws);
      const unsigned eo = wave < 4 ? min((unsigned)(idx * CODE_PER + min(tid >> 4, CODE_PER - 1)) * 128u, clen) : wave == 6 && bid == 0 ? SY_RE + lane * 16 : SY_GO + bid * 4;
      asm volatile("global_load_dwordx4 %0, %1, %2 sc1" : "=v"(e0) : "v"(eo), "s"(uni(eb)) : "memory");
    }
    BAR();
    STAMPT(3, 12, 0);
    if (wave < 6) first(1, 0);
    if (wave == 4 || wave == 5) first(1, 2);
    SB0();
  }
  volatile LAS int* misc = (volatile LAS int*)(size_t)(lds0 + L_MISC);
  const bool has_mlp = bid < 224;
  const int fs = has_mlp ? (bid & 7) * 28 + (bid >> 3) : (bid & 7) * 28;
  const int tcs = (rb & 4) ? 4 : 3;
  const unsigned rs_o = WS_RS + (rb * 16 + korder(0)) * 1024, cs_o = WS_CS + (hp * 16 + korder(tcs)) * 768;
  const u32x4 ONES4 = {0x3F803F80u, 0x3F803F80u, 0x3F803F80u, 0x3F803F80u};

  if (wave < 4) {
    const int wr = wave >> 1, wc = wave & 1;
    const int r = lane & 15, kq = lane >> 4;
    f32x4 acc[4][6];
    {
      unsigned aO[2], bQ[2], bK[2], bV[2];
#pragma unroll
      for (int k = 0; k < 2; ++k) aO[k] = opq(lds0 + L_A + (64 * wr + r) * 128 + (((4 * k + kq) ^ ((r >> 1) & 7)) * 16));
#pragma unroll
      for (int c = 0; c < 2; ++c) {
        const unsigned o_ = lds0 + kq * 1024 + wc * 256 + (r >> 2) * 64 + ((c ^ (kq & 1)) * 32) + (lane & 3) * 8;
        bQ[c] = opq(o_ + L_Q); bK[c] = opq(o_ + L_KV); bV[c] = opq((o_ ^ (wr * 32)) + L_KV);
      }
#pragma unroll
      for (int i = 0; i < 4; ++i)
#pragma unroll
        for (int j = 0; j < 6; ++j) acc[i][j] = (f32x4){0.f, 0.f, 0.f, 0.f};
      struct Fr { u32x4 a[4]; u32x2 bl[6], bh[6]; };
      Fr F[2];
      auto bread = [&](int f, int b, int k, int hi) {
        return f < 2 ? trread(bQ[f & 1], k * 4096 + b * 8192 + hi * 512) : trread(f < 4 ? bK[f & 1] : bV[f & 1], k * 4096 + b * 16384 + ((f >> 1) - 1) * 8192 + hi * 512);
      };
      auto step = [&](Fr& C, Fr& N, int b, int k, int MC, int ML) {
        int ld = 0;
        constexpr int ORD[6] = {2, 3, 4, 5, 0, 1};
        auto load1 = [&]() {
          if (ML == 0) return;
          if (ld < 4) N.a[ld] = ldsr(aO[k], b * 16384 + ld * 2048);
          else {
            const int e = ld - 4, f = ML == 1 ? (e >> 1) : ORD[(e >> 1) % 6], nf = ML == 1 ? 2 : ML == 2 ? 4 : 6;
            if ((e >> 1) < nf) { if (e & 1) N.bh[f] = bread(f, b, k, 1); else N.bl[f] = bread(f, b, k, 0); }
          }
          ++ld;
        };
#pragma unroll
        for (int m = 0; m < 24; ++m) {
          const int j = ORD[m >> 2], i = m & 3;
          if (!((j < 2 ? 1 : 2) & MC)) continue;
          if (j < 2) acc[i][j] = mfma16(cat(C.bl[j], C.bh[j]), C.a[i], acc[i][j]);
          else acc[i][j] = mfma16(C.a[i], cat(C.bl[j], C.bh[j]), acc[i][j]);
          load1();
          SB0();
        }
      };
      WAITV(5); BAR();
      STAMPT(0, 1, 0);
#pragma unroll
      for (int m = 0; m < 4; ++m) F[0].a[m] = ldsr(aO[0], m * 2048);
#pragma unroll
      for (int f = 0; f < 6; ++f) { F[0].bl[f] = bread(f, 0, 0, 0); F[0].bh[f] = bread(f, 0, 0, 1); }
      for (int t4 = 0; t4 < 12; t4 += 4) {
#pragma unroll
        for (int b = 0; b < 4; ++b) {
          step(F[0], F[1], b, 1, 3, 3);
          if (b == 0 && t4 == 0) { asm volatile("s_waitcnt vmcnt(0)" :: "v"(e0) : "memory"); STAMPT(3, 13, 0); BAR(); STAMPT(3, 14, 0); }
          step(F[1], F[0], (b + 1) & 3, 0, 3, 3);
          BAR();
        }
      }
#pragma unroll
      for (int b = 0; b < 4; ++b) {
        step(F[0], F[1], b, 1, 2, 2);
        step(F[1], F[0], (b + 1) & 3, 0, 2, b < 3 ? 2 : 0);
        if (b < 2) BAR();
      }
      misc[28 + wave] = 1;
#pragma nounroll
      for (unsigned s_ = 0; s_ < SPIN_CAP; ++s_) if (__builtin_amdgcn_readfirstlane(misc[7])) break;
      asm volatile("" ::: "memory");
      STAMPT(0, 2, 0);
      {
        const float* sa = (const float*)(smem + L_SA) + 64 * wr + 4 * kq;
        const float* cs = (const float*)(smem + L_CS) + wc * 96 + r;
        const float c4[4] = {cs[32], cs[48], cs[64 + 16 * wr], cs[80 - 16 * wr]};
        unsigned kp[4][2][2], vp[4][2][2];
#pragma unroll
        for (int i = 0; i < 4; ++i) {
          const f32x4 a4 = *(const f32x4*)(sa + 16 * i), b4 = *(const f32x4*)(sa + 128 + 16 * i);
          const f32x2 aa[2] = {{a4[0], a4[1]}, {a4[2], a4[3]}}, bb[2] = {{b4[0], b4[1]}, {b4[2], b4[3]}};
#pragma unroll
          for (int j = 0; j < 2; ++j)
#pragma unroll
            for (int e = 0; e < 2; ++e) {
              const f32x2 tk = (f32x2){acc[i][2 + j][2 * e], acc[i][2 + j][2 * e + 1]} * aa[e] + bb[e] * c4[j];
              const f32x2 tv = (f32x2){acc[i][4 + j][2 * e], acc[i][4 + j][2 * e + 1]} * aa[e] + bb[e] * c4[2 + j];
              kp[i][j][e] = phi_pair(tk);
              vp[i][j][e] = pack2(tv[0], tv[1]);
            }
        }
        f32x4 kv[2][2], zv[2];
#pragma unroll
        for (int d = 0; d < 2; ++d) {
          zv[d] = (f32x4){0.f, 0.f, 0.f, 0.f}; kv[d][0] = zv[d]; kv[d][1] = zv[d];
#pragma unroll
          for (int u = 0; u < 2; ++u) {
            const u32x4 ka = {kp[2 * u][d][0], kp[2 * u][d][1], kp[2 * u + 1][d][0], kp[2 * u + 1][d][1]};
#pragma unroll
            for (int e = 0; e < 2; ++e)
              kv[d][e] = mfma16(ka, (u32x4){vp[2 * u][e][0], vp[2 * u][e][1], vp[2 * u + 1][e][0], vp[2 * u + 1][e][1]}, kv[d][e]);
            zv[d] = mfma16(ka, ONES4, zv[d]);
          }
        }
        char* xo = smem + L_XCH + wave * 2048 + lane * 16;
        *(f32x4*)xo = kv[0][1];
        *(f32x4*)(xo + 1024) = kv[1][1];
        if (r < 2) *(f32x4*)(smem + L_XCH + 8192 + wave * 128 + r * 64 + kq * 16) = r ? zv[1] : zv[0];
        STAMPT(1, 0, 0);
        asm volatile("s_waitcnt lgkmcnt(0)" ::: "memory");
        misc[16 + wave] = 1;
#pragma nounroll
        for (unsigned s_ = 0; s_ < SPIN_CAP; ++s_) if (__builtin_amdgcn_readfirstlane(misc[16 + (wave ^ 2)])) break;
        asm volatile("" ::: "memory");
        const char* xi = smem + L_XCH + (wave ^ 2) * 2048 + lane * 16;
        const f32x4 s0 = *(const f32x4*)xi + kv[0][0], s1 = *(const f32x4*)(xi + 1024) + kv[1][0];
        const unsigned ko = WS_KVP + (hp * 16 + rb) * KVB + wc * 2176;
        STW(seal((u32x4){pack2(s0[0], s0[1]), pack2(s0[2], s0[3]), pack2(s1[0], s1[1]), pack2(s1[2], s1[3])}), ko + wr * 1024 + lane * 16);
        if (wr == 0 && lane < 8) {
          const f32x4 z = *(const f32x4*)(smem + L_XCH + 8192 + wave * 128 + lane * 16) + *(const f32x4*)(smem + L_XCH + 8192 + (wave + 2) * 128 + lane * 16);
          STW(seal(__builtin_bit_cast(u32x4, z)), ko + 2048 + lane * 16);
        }
                STAMPT(0, 3, 0);
#pragma unroll
        for (int m = 0; m < 4; ++m) F[0].a[m] = ldsr(aO[0], m * 2048);
#pragma unroll
        for (int f = 0; f < 2; ++f) { F[0].bl[f] = bread(f, 0, 0, 0); F[0].bh[f] = bread(f, 0, 0, 1); }
      }
#pragma unroll
      for (int b = 0; b < 4; ++b) {
        step(F[0], F[1], b, 1, 1, 1);
        step(F[1], F[0], (b + 1) & 3, 0, 1, b < 3 ? 1 : 0);
        if (b == GB) misc[16 + wave] = 2;
      }
    }
    u32x4 qp[4];
    {
      const float* sa = (const float*)(smem + L_SA) + 64 * wr + r;
      const float* cs = (const float*)(smem + L_CS) + wc * 96 + 4 * kq;
      const f32x4 c0 = *(const f32x4*)cs, c1 = *(const f32x4*)(cs + 16);
#pragma unroll
      for (int i = 0; i < 4; ++i) {
        const float a = sa[16 * i], b = sa[128 + 16 * i];
        qp[i][0] = phi_pair((f32x2){acc[i][0][0], acc[i][0][1]} * a + (f32x2){c0[0], c0[1]} * b);
        qp[i][1] = phi_pair((f32x2){acc[i][0][2], acc[i][0][3]} * a + (f32x2){c0[2], c0[3]} * b);
        qp[i][2] = phi_pair((f32x2){acc[i][1][0], acc[i][1][1]} * a + (f32x2){c1[0], c1[1]} * b);
        qp[i][3] = phi_pair((f32x2){acc[i][1][2], acc[i][1][3]} * a + (f32x2){c1[2], c1[3]} * b);
        asm volatile("" : "+v"(qp[i]));
      }
    }
    u32x2 xr[8];
    {
      const int row = wave * 32 + (lane & 31);
#pragma unroll
      for (int c = 0; c < 8; ++c) xr[c] = *(const u32x2*)(smem + L_A + ((cb & 3) ^ kg_) * 16384 + row * 128 + ((c ^ ((row >> 1) & 7)) * 16) + (lane >> 5) * 8);
    }
    const unsigned lzf = opq(lds0 + L_ZL + wc * 64 + kq * 16), lkf = opq(lds0 + L_KVL + wc * 2048 + lane * 16);
    const unsigned oo = opq(WS_O + ((rb * 8 + wr * 4) * 32 + 2 * hp + wc) * 1024 + lane * 16);
    STAMPT(0, 4, 0);
    LBAR();
    STAMPT(0, 5, 0);
    {
      const u32x4 zf = ldsr(lzf, 0), k0 = ldsr(lkf, 0), k1 = ldsr(lkf, 1024);
      const f32x4 Z4 = {0.f, 0.f, 0.f, 0.f};
      f32x4 dn[4], x0[4], x1[4];
#pragma unroll
      for (int i = 0; i < 4; ++i) dn[i] = mfma16(zf, qp[i], Z4);
#pragma unroll
      for (int i = 0; i < 4; ++i) { x0[i] = mfma16(k0, qp[i], Z4); x1[i] = mfma16(k1, qp[i], Z4); }
      SB0();
#pragma unroll
      for (int i = 0; i < 4; ++i) {
        const float ri = __builtin_amdgcn_rcpf(dn[i][0] + 1e-6f);
        const f32x4 y0 = x0[i] * ri, y1 = x1[i] * ri;
        STW(((u32x4){pack2(y0[0], y0[1]), pack2(y0[2], y0[3]), pack2(y1[0], y1[1]), pack2(y1[2], y1[3])}), oo + i * 32768);
      }
      misc[24 + wave] = 1;
      WAITV(0);
      if (lane == 0) st32(ws, SY_FB + rb * 256 + hp * 16 + wave * 4, 1u);
    }
    STAMPT(0, 6, 0);
    {
      const int h = lane >> 5, s32 = lane & 31;
      u32x2 wv[8];
      auto wvld = [&]() {
#pragma unroll
        for (int c = 0; c < 8; ++c) wv[c] = *(const volatile LAS u32x2*)(size_t)(lds0 + L_WR + c * 16 + h * 8);
      };
      wvld();
      u32x4 of[32][2];
      const unsigned ol = WS_O + (rp * 8 + wave * 2 + (kq & 1)) * 32768 + (h * 16 + r) * 16;
      auto oload1 = [&](int j, int e) { of[j][e] = LDWS(ol + e * 512 + (j & (HRS - 1)) * 1024, ((j & ~(HRS - 1)) ^ hrot) * 1024); };
      auto oload = [&](int j) { oload1(j, 0); oload1(j, 1); };
      f32x16 a2[4];
      unsigned wO[3];
#pragma unroll
      for (int e = 0; e < 3; ++e) wO[e] = opq(lds0 + e * 65536 + h * 512 + (r >> 2) * 64 + (((kq & 1) ^ h) * 32) + (lane & 3) * 8);
      constexpr int PF = GPF, OW = GOW;
      u32x2 wl[8], wh[8];
      auto wld = [&](int g) {
        const int j = g >> 2;
        const int o_ = (wslot(j) & 65535) + ((g >> 1) & 1) * 1024 + (g & 1) * 256;
        wl[g & 7] = trread(wO[wslot(j) >> 16], o_); wh[g & 7] = trread(wO[wslot(j) >> 16], o_ + 2048);
      };
      const int row = wave * 32 + s32;
      const unsigned xb = opq(lds0 + L_X2 + row * 128 + h * 8);
      const int sw = (row >> 1) & 7;
      float sw_ = 0.f;
#pragma unroll
      for (int c = 0; c < 16; ++c) sw_ = dot2(wv[c >> 1][c & 1], 0x3F803F80u, sw_);
      sw_ = xh_sum(sw_);
      asm volatile("" : "+v"(sw_));
      BAR();
      STAMPT(0, 7, 0);
#pragma unroll
      for (int j = 0; j < OW0; ++j) { oload(j); SB0(); }
#pragma unroll
      for (int c = 0; c < 8; ++c) {
        const u32x2 xx = xr[c];
#pragma unroll
        for (int i = 0; i < 2; ++i) {
          a2[c >> 2][(c & 3) * 4 + 2 * i] = bflo(xx[i]); a2[c >> 2][(c & 3) * 4 + 2 * i + 1] = bfhi(xx[i]);
          a2[2 + (c >> 2)][(c & 3) * 4 + 2 * i] = 0.f; a2[2 + (c >> 2)][(c & 3) * 4 + 2 * i + 1] = 0.f;
        }
      }
#pragma unroll
      for (int g = 0; g < PF; ++g) wld(g);
      SB0();
#pragma unroll
      for (int j = 0; j < 32; ++j) {
        if (j == 30) wvld();
#pragma unroll
        for (int n = 0; n < 4; ++n) {
          const int g = j * 4 + n;
          a2[n] = mfma32(cat(wl[g & 7], wh[g & 7]), of[j][n >> 1], a2[n]);
          SB0();
          if (g + PF < 128) wld(g + PF);
          {
            constexpr int RAMP = OW - OW0;
            const int q = j < RAMP ? 2 * OW0 + 4 * j + n : (n & 1) ? -1 : 2 * (j + OW) + (n >> 1);
            if (q >= 0 && q < 64) oload1(q >> 1, q & 1);
          }
          if (n == 0 && j + OW == 32) misc[20 + wave] = 1;
          SB0();
          asm volatile("" :: "v"(wl[g & 7]), "v"(wh[g & 7]), "v"(of[j][n >> 1]));
          SB0();
        }
      }
      STAMPT(0, 8, 0); STAMPT(3, 1, 64); STAMPT(3, 2, 128); STAMPT(3, 3, 192);
      a2[0] += a2[2]; a2[1] += a2[3];
      misc[8 + wave] = 1;
      unsigned hb[16];
      {
        float s[2] = {0.f, 0.f}, sq[2] = {0.f, 0.f}, pw[2] = {0.f, 0.f};
#pragma unroll
        for (int i = 0; i < 16; ++i) {
          const unsigned t = pack2(a2[i >> 3][(i & 7) * 2], a2[i >> 3][(i & 7) * 2 + 1]);
          hb[i] = t;
          s[i & 1] = dot2(t, 0x3F803F80u, s[i & 1]); sq[i & 1] = dot2(t, t, sq[i & 1]); pw[i & 1] = dot2(t, wv[i >> 1][i & 1], pw[i & 1]);
        }
        const u32x4 st = {__float_as_uint(xh_sum(s[0] + s[1])), __float_as_uint(xh_sum(sq[0] + sq[1])), __float_as_uint(xh_sum(pw[0] + pw[1])), __float_as_uint(sw_)};
        if (h == 0) STW(seal(st), WS_ST2 + ((rp * 16 + cb) * 128 + row) * 16);
      }
      STAMPT(0, 9, 0);
      u32x4 pu_[8];
      __builtin_amdgcn_s_sleep(SLC0);
#pragma nounroll
      for (unsigned s_ = 0; s_ < SPIN_CAP; ++s_) {
        unsigned mk = 0u;
#pragma unroll
        for (int j = 0; j < 8; ++j) { pu_[j] = LDW(WS_ST2 + ((rp * 16 + h * 8 + j) * 128 + row) * 16); mk = max(max(mk, pu_[j][1]), pu_[j][3]); }
        if (!__any(mk == EMPTY)) break;
        asm volatile("" ::: "memory");
        __builtin_amdgcn_s_sleep(SLC1);
      }
      STAMPT(0, 10, 0);
      f32x4 tt = {0.f, 0.f, 0.f, 0.f};
#pragma unroll
      for (int j = 0; j < 8; ++j) tt += __builtin_bit_cast(f32x4, pu_[j]);
      const float mean = xh_sum(tt[0]) * (1.0f / 1024.0f);
      const float var = fmaxf(xh_sum(tt[1]) * (1.0f / 1024.0f) - mean * mean, 0.f);
      const float mb = rbf(mean), rs = __builtin_amdgcn_rcpf(rbf(__builtin_amdgcn_sqrtf(rbf(rbf(var) + 1e-5f))));
      float sc = rbf((xh_sum(tt[2]) - mb * xh_sum(tt[3])) * rs);
      if (!(sc == sc)) sc = -INFINITY;
      unsigned b0 = h ? 0u : mk_key(sc, rp * 128 + row), b1 = 0;
      top2_wave(b0, b1);
      const int pan = rp * 4 + wave;
      if (lane == 0) st64(ws, SY_FD + cb * 512 + pan * 8, ((u64)b1 << 32) | b0);
      STAMPT(1, 11, 0);
      unsigned nq[16];
#pragma unroll
      for (int i = 0; i < 16; ++i) {
        const unsigned dq = pack2(bflo(hb[i]) - mb, bfhi(hb[i]) - mb);
        nq[i] = pack2(bflo(dq) * rs, bfhi(dq) * rs);
      }
      const int lc0 = key_idx(b0) & 31, lc1 = key_idx(b1) & 31;
      {
        const int c0 = lc0, c1 = lc1;
        char* tb = smem + L_TB + wave * 512;
        if (s32 == c0 || s32 == c1) {
          char* tp = tb + (s32 == c0 ? 0 : 256) + h * 8;
#pragma unroll
          for (int c = 0; c < 8; ++c) { *(u32x2*)(tp + c * 16) = (u32x2){nq[2 * c], nq[2 * c + 1]}; *(u32x2*)(tp + 128 + c * 16) = (u32x2){hb[2 * c], hb[2 * c + 1]}; }
        }
        __builtin_amdgcn_wave_barrier();
        const u32x4 v4 = seal(*(const u32x4*)(tb + s32 * 16));
#pragma unroll
        for (int rr_ = 0; rr_ < XSR; rr_ += 2)
          if (rr_ + h < XSR) STW(v4, WS_XS + (rr_ + h) * 524288 + (pan * 2 + (s32 >> 4)) * 4096 + ((s32 >> 3) & 1) * 2048 + cb * 128 + (s32 & 7) * 16);
      }
      u64 cc = ~0ull;
#pragma unroll
      for (int c = 0; c < 8; ++c) {
        u32x2 ob;
#pragma unroll
        for (int i = 0; i < 2; ++i) ob[i] = pack2(bflo(hb[2 * c + i]) + bflo(nq[2 * c + i]), bfhi(hb[2 * c + i]) + bfhi(nq[2 * c + i]));
        *(LAS u32x2*)((LAS char*)(size_t)(xb + ((c ^ sw) * 16))) = ob;
      }
      auto ost = [&](int rl, bool on) {
        const int rw = wave * 32 + rl;
        const u32x4 v = *(const u32x4*)(smem + L_X2 + rw * 128 + (((lane & 7) ^ ((rw >> 1) & 7)) * 16));
        if (on) *(u32x4*)(out + (size_t)(rp * 128 + rw) * D_ + cb * 64 + (lane & 7) * 8) = v;
      };
      if (wave == 0) {
        __builtin_amdgcn_s_sleep(SLD0);
#pragma nounroll
        for (unsigned s_ = 0; s_ < SPIN_CAP; ++s_) {
          cc = ld64(ws, SY_FD + cb * 512 + lane * 8);
          if (__all((unsigned)(cc >> 32) != EMPTY)) break;
          __builtin_amdgcn_s_sleep(SLD1);
        }
        STAMPT(1, 13, 0);
        unsigned c0 = (unsigned)cc, c1 = (unsigned)(cc >> 32);
        top2_wave(c0, c1);
        const unsigned w0 = key_idx(c0), w1 = key_idx(c1);
        const unsigned k0 = __builtin_amdgcn_readlane((unsigned)cc, w0 >> 5), k1 = __builtin_amdgcn_readlane((unsigned)cc, w1 >> 5);
        const unsigned sl0 = (w0 >> 5) * 2 + (k0 == c0 ? 0 : 1), sl1 = (w1 >> 5) * 2 + (k1 == c1 ? 0 : 1);
        if (lane == 0) { misc[1] = w0; misc[2] = w1; misc[3] = sl0; misc[4] = sl1; misc[5] = c0; misc[6] = c1; }
        if (has_mlp) {
          const unsigned xo = WS_XS + (idx & (XSR - 1)) * 524288u + (h ? sl1 : sl0) * 4096u + (fs & 3) * 512 + s32 * 16;
          u32x4 q;
#pragma nounroll
          for (unsigned s_ = 0; s_ < SPIN_CAP; ++s_) {
            q = LDW(xo);
            if (__all(max(q[1], q[3]) != EMPTY)) break;
            asm volatile("" ::: "memory");
            __builtin_amdgcn_s_sleep(XSL1);
          }
          *(u32x4*)(smem + L_XS + h * 528 + s32 * 16) = q;
        }
      }
      LBAR();
      STAMPT(0, 11, 0);
      const int sel0 = misc[1], sel1 = misc[2];
#pragma unroll
      for (int m = 0; m < 4; ++m) { const int rl = m * 8 + (lane >> 3), grow = rp * 128 + wave * 32 + rl; ost(rl, grow != sel0 && grow != sel1); }
      STAMPT(0, 12, 0);
      if (bid < 64 && wave == 0) {
        const int tk = bid >> 5, c0 = (bid & 31) * 32;
        const unsigned xo = WS_XS + (idx & (XSR - 1)) * 524288u + (unsigned)misc[3 + tk] * 4096u + c0 * 2 + (lane & 7) * 8;
        u64 xn, xh;
#pragma nounroll
        for (unsigned s_ = 0; s_ < SPIN_CAP; ++s_) {
          xn = ld64(ws, xo); xh = ld64(ws, xo + 2048);
          if (__all((unsigned)(xn >> 32) != EMPTY && (unsigned)(xh >> 32) != EMPTY)) break;
        }
        const float sg = rbf(__builtin_amdgcn_rcpf(1.0f + __builtin_amdgcn_exp2f(key_val((unsigned)misc[5 + tk]) * -1.44269504088896340736f)));
        u16* op = out + (size_t)(tk ? sel1 : sel0) * D_ + c0 + (lane & 7) * 4;
        const unsigned yo = opq(WS_YP + (lane >> 3) * 8192 + tk * 4096 + c0 * 4 + (lane & 7) * 16);
#pragma nounroll
        for (unsigned s_ = 0; s_ < SPIN_CAP; ++s_) { if (__builtin_amdgcn_readfirstlane(misc[0])) break; __builtin_amdgcn_s_sleep(1); }
        __builtin_amdgcn_s_sleep(YSL0);
        u32x4 pv[7];
#pragma nounroll
        for (unsigned s_ = 0; s_ < SPIN_CAP; ++s_) {
          unsigned mk = 0u;
#pragma unroll
          for (int p = 0; p < 7; ++p) pv[p] = LDWS(yo, p * 65536);
#pragma unroll
          for (int p = 0; p < 7; ++p) mk = max(max(mk, pv[p][1]), pv[p][3]);
          if (!__any(mk == EMPTY)) break;
          asm volatile("" ::: "memory");
          __builtin_amdgcn_s_sleep(YSL1);
        }
        STAMPT(0, 15, 0);
        f32x4 y = __builtin_bit_cast(f32x4, pv[0]);
#pragma unroll
        for (int p = 1; p < 7; ++p) y += __builtin_bit_cast(f32x4, pv[p]);
        float t[4];
#pragma unroll
        for (int i = 0; i < 4; ++i) {
          float v = y[i]; v += dppf<DPP_ROR8>(v); v = xh_sum(x16_sum(v));
          const unsigned nn = (unsigned)(xn >> (32 * (i >> 1))), hh = (unsigned)(xh >> (32 * (i >> 1)));
          t[i] = ((i & 1) ? bfhi(hh) : bflo(hh)) + rbf(((i & 1) ? bfhi(nn) : bflo(nn)) + rbf(rbf(v) * sg));
        }
        if (lane < 8) *(u32x2*)op = (u32x2){pack2(t[0], t[1]), pack2(t[2], t[3])};
        asm volatile(".Lfused_end:" ::: "memory");
        STAMPT(0, 14, 0);
      }
    }
  } else {
    __builtin_amdgcn_s_setprio(2);
    const int hw = wave - 4, mt = tid & 255;
    u32x4 wgr[8], wur[8], wdr[8];
    const int krow = hw * 64 + (lane >> 3);
    const unsigned gof = opq((unsigned)(((fs & 3) * 256 + krow) * F_ + (fs >> 2) * 64 + (lane & 7) * 8) * 2u);
    const unsigned dof = opq((unsigned)(((fs >> 2) * 64 + (lane >> 3)) * D_ + (fs & 3) * 256 + hw * 64 + (lane & 7) * 8) * 2u);
    const unsigned wto = opq((unsigned)((idx * 32 + (mt >> 3)) * 2048 + (cb >> 2) * 512 + ((mt >> 1) & 3) * 128));
#define WLOAD(dst, vo, base) asm volatile("global_load_dwordx4 %0, %1, %2" : "=v"(dst) : "v"(vo), "s"(uni(base)) : "memory")
    auto wload = [&](int i) {
      if (i < 8) WLOAD(wgr[i], gof, wg + i * (8 * F_));
      else if (i < 16) WLOAD(wur[i - 8], gof, wu + (i - 8) * (8 * F_));
      else WLOAD(wdr[i - 16], dof, wd + (i - 16) * (8 * D_));
    };
    auto wpin = [&](int i) {
      if (i < 8) asm volatile("" : "+v"(wgr[i])); else if (i < 16) asm volatile("" : "+v"(wur[i - 8])); else asm volatile("" : "+v"(wdr[i - 16]));
    };
    const int pbi = lane >> 4, pq = (lane >> 2) & 3, pch = lane & 3;
    const unsigned voW = opq((unsigned)((8 * hw + 4 * (pbi >> 1) + pq) * D_ + (pbi & 1) * 32 + (pch ^ ((pbi >> 1) * 2)) * 8) * 2u);
    auto stageW = [&](int j) { glds16(voW, uni(wo + cb * 64) + (size_t)(j ^ hrot) * (32 * D_ * 2), lds0 + wslot(j) + hw * 1024); };
    u32x4 e4 = {EMPTY, EMPTY, EMPTY, EMPTY};
    asm volatile("" : "+v"(e4));
    const unsigned l16 = opq(lane * 16);
    float mean[2] = {0.f, 0.f}, vr[2] = {0.f, 0.f};
    const unsigned lsa = opq(lds0 + L_SA + lane * 8);
    if (hw < 2) {
      const char* baseA = uni(x + (size_t)rb * 128 * D_);
      const char* baseB = uni(wqkv + hp * 64);
      unsigned voA[2], voB[2];
      {
        const int r0 = 64 * hw + (lane >> 3), r1 = r0 + 8;
        voA[0] = (unsigned)(r0 * D_ + ((lane & 7) ^ ((r0 >> 1) & 7)) * 8) * 2u;
        voA[1] = (unsigned)(r1 * D_ + ((lane & 7) ^ ((r1 >> 1) & 7)) * 8) * 2u;
        voB[0] = (unsigned)((32 * hw + 4 * (pbi >> 1) + pq) * (3 * D_) + (pbi & 1) * 32 + pch * 8) * 2u;
        voB[1] = (unsigned)((32 * hw + 8 + 4 * (pbi >> 1) + pq) * (3 * D_) + (pbi & 1) * 32 + (pch ^ 2) * 8) * 2u;
      }
      auto stage = [&](int t) {
        const int kt = korder(t), b = t & 3;
        const char* pa = baseA + kt * 128;
        const char* pb = baseB + (size_t)kt * (64 * 3 * D_ * 2);
#pragma unroll
        for (int j = 0; j < 8; ++j) glds16(voA[j & 1] + (j >> 1) * (16 * D_ * 2), pa, lds0 + L_A + b * 16384 + (8 * hw + j) * 1024);
#pragma unroll
        for (int j = 0; j < 4; ++j) {
          const unsigned vo = voB[j & 1] + (j >> 1) * (16 * 3 * D_ * 2);
          glds16(vo + 2048, pb, lds0 + L_KV + b * 16384 + (4 * hw + j) * 1024);
          glds16(vo + 4096, pb, lds0 + L_KV + b * 16384 + 8192 + (4 * hw + j) * 1024);
          glds16(vo, pb, lds0 + L_Q + b * 8192 + (4 * hw + j) * 1024);
        }
      };
      WAITV(10); BAR();
      stage(2); stage(3);
      WAITV(40); BAR();
      for (int t = 0; t < 12; ++t) { WAITV(20); BAR(); stage(t + 4); }
      WAITV(20); BAR();
      asm volatile("s_waitcnt vmcnt(0)" :: "v"(e0) : "memory");
      BAR();
    } else {
      const int aw = hw - 2, at = tid & 127;
      const bool master = bid == 0 && aw == 0;
      unsigned hs_cur = 0, hs_g0 = 0;
      u32x4 ms_exp = {0, 0, 0, 0}, ms_go = {0, 0, 0, 0}, pm = {0, 0, 0, 0};
      const unsigned p_off = opq(master ? SY_RE + lane * 16 : (lane == 1 ? SY_GO : SY_CH) + bid * 4);
      const char* wsu = uni(ws);
#define HS_PROBE() asm volatile("global_load_dwordx4 %0, %1, %2 sc1" : "+v"(pm) : "v"(p_off), "s"(wsu) : "memory")
#define HS_WAIT() asm volatile("s_waitcnt vmcnt(0)" : "+v"(pm) :: "memory")
      bool done = false;
      auto hs_check = [&]() __attribute__((always_inline)) {
        HS_WAIT();
        if (!master) {
          const unsigned c = __builtin_amdgcn_readlane(pm[0], 0), go = __builtin_amdgcn_readlane(pm[0], 1);
          if (go != hs_g0) done = true;
          else if (c != hs_cur) { hs_cur = c; st32(ws, SY_RE + bid * 4, c); }
        } else if (!done && __all(pm[0] == ms_exp[0] && pm[1] == ms_exp[1] && pm[2] == ms_exp[2] && pm[3] == ms_exp[3])) {
          STW(ms_go, SY_GO + lane * 16);
          done = true;
        }
      };
      const bool prober = master || aw == 1;
      const int kq = lane >> 4, r = lane & 15;
      constexpr unsigned OOB = 0x80000000u;
      const unsigned ko = WS_KVP + (hp * 16 + rb) * KVB + aw * 2176;
      const unsigned vm0 = opq(lane < 32 ? rs_o + aw * 512 + lane * 16 : lane < 56 ? cs_o + aw * 384 + (lane - 32) * 16 : ko + 2048 + (lane - 56) * 16);
      u32x4 e1;
      asm volatile("s_waitcnt vmcnt(0)\n\tv_mov_b32 %0, %4\n\tv_mov_b32 %1, %5\n\tv_mov_b32 %2, %6\n\tv_mov_b32 %3, %7"
                   : "=&v"(e1[0]), "=&v"(e1[1]), "=&v"(e1[2]), "=&v"(e1[3]) : "v"(e0[0]), "v"(e0[1]), "v"(e0[2]), "v"(e0[3]) : "memory");
      hs_g0 = __builtin_amdgcn_readfirstlane(e1[0]);
      u32x4 g0 = {0u, 0u, 0u, 0u};
      BAR();
      if (master) { ms_exp = e1 + 1u; STW(ms_exp, SY_CH + lane * 16); g0 = LDW(SY_GO + lane * 16); }
      u32x2 rst[4];
      {
        u32x4 a[4][2];
#pragma unroll
        for (int i = 0; i < 4; ++i)
#pragma unroll
          for (int k = 0; k < 2; ++k) a[i][k] = *(const u32x4*)(smem + L_A + (64 * aw + 16 * i + r) * 128 + (((4 * k + kq) ^ ((r >> 1) & 7)) * 16));
        const f32x4 Z4 = {0.f, 0.f, 0.f, 0.f};
        const int l3 = lane & 3;
#pragma unroll
        for (int i = 0; i < 4; ++i) {
          const f32x4 s1 = mfma16(ONES4, a[i][1], mfma16(ONES4, a[i][0], Z4)), s2 = mfma16(a[i][1], a[i][1], mfma16(a[i][0], a[i][0], Z4));
          const float q = l3 == 0 ? s2[0] : l3 == 1 ? s2[1] : l3 == 2 ? s2[2] : s2[3];
          rst[i] = (u32x2){min(__float_as_uint(s1[0]), 0xFFFFFFFEu), min(__float_as_uint(q), 0xFFFFFFFEu)};
          asm volatile("" : "+v"(rst[i]));
        }
      }
      const unsigned vm1 = opq(aw ? (lane < 2 ? SY_FD + cb * 512 + rp * 32 + lane * 16 : lane == 2 ? SY_FB + rb * 256 + hp * 16 : OOB)
                                  : (has_mlp ? WS_GU + fs * 1024 + lane * 16 : OOB));
      const unsigned vrs = opq((r >> 2) == kq ? rs_o + (64 * aw + r) * 8 : OOB);
      const unsigned vc0 = opq(cs_o + aw * 384 + lane * 4), vc1 = opq(lane < 32 ? cs_o + aw * 384 + 256 + lane * 4 : OOB);
      const unsigned sg_off = opq(aw ? WS_CS + hp * (16 * 768) + min(lane, 47) * 16 : WS_RS + rb * 16384 + lane * 16), sg_st = aw ? 768 : 1024;
      unsigned cb_[2][2];
#pragma unroll
      for (int c = 0; c < 2; ++c) {
        const unsigned o_ = lds0 + kq * 1024 + aw * 256 + (r >> 2) * 64 + ((c ^ (kq & 1)) * 32) + (lane & 3) * 8;
        cb_[0][c] = opq(o_ + L_Q + (tcs & 3) * 8192); cb_[1][c] = opq(o_ + L_KV + (tcs & 3) * 16384);
      }
      const int hq = (3 - (int)((size_t)wqkv >> 7) - (hp & ~3)) & 7;
      const int qln = ((((hp & 3) - hq - 1) & 3) * 8 + (idx & 7)) * 64 + (at >> 1);
      const unsigned vq = opq((unsigned)((((kph + 8) * 64 + qln / 3) & 1023) * 6144 + (qln % 3) * 2048 + ((hp & ~3) + (hq & 3)) * 128));
      const unsigned lw0 = opq(lds0 + L_CS + lane * 16), lw1 = opq(lds0 + L_WR + (lane & 7) * 16);
      const unsigned vcp = opq(min((unsigned)(idx * CODE_PER + min(at >> 3, CODE_PER - 1)) * 128u, clen));
      BAR();
      ms_go = g0 + 1u;
      STW(e4, vm0); STW(e4, vm1); STWS(e4, l16, ko); STWS(e4, l16, ko + 1024);
      BAR();
      STAMPT(2, 0, 384);
      if (aw == 1) HS_PROBE();
      HS_WAIT();
      BAR();
      STAMPT(2, 1, 384);
      if (aw == 1) { hs_cur = __builtin_amdgcn_readlane(pm[0], 0); st32(ws, SY_RE + bid * 4, hs_cur); }
#pragma unroll
      for (int i = 0; i < 4; ++i) __builtin_amdgcn_raw_buffer_store_b64(rst[i], wsr, vrs + i * 128, 0, 16);
      unsigned qt = 0;
      if (hq < 4 && (hp & 3) != hq) asm volatile("global_load_dword %0, %1, %2" : "=v"(qt) : "v"(vq), "s"(uni(wqkv)) : "memory");
      BAR();
      STAMPT(2, 2, 384);
      if (prober) HS_PROBE();
      unsigned cv0, cv1;
      {
        f32x4 cacc[6];
#pragma unroll
        for (int f = 0; f < 6; ++f) {
          cacc[f] = (f32x4){0.f, 0.f, 0.f, 0.f};
#pragma unroll
          for (int k = 0; k < 2; ++k) {
            const int o_ = (f >= 4 ? 8192 : 0) + k * 4096;
            cacc[f] = mfma16(ONES4, cat(trread(cb_[f >= 2][f & 1], o_), trread(cb_[f >= 2][f & 1], o_ + 512)), cacc[f]);
          }
        }
        cv0 = min(__float_as_uint(kq == 0 ? cacc[0][0] : kq == 1 ? cacc[1][0] : kq == 2 ? cacc[2][0] : cacc[3][0]), 0xFFFFFFFEu);
        cv1 = min(__float_as_uint(kq == 0 ? cacc[4][0] : cacc[5][0]), 0xFFFFFFFEu);
        asm volatile("" : "+v"(cv0), "+v"(cv1));
      }
      BAR();
      STAMPT(2, 3, 384);
      HS_WAIT();
      asm volatile("" :: "v"(qt));
      if (prober) hs_check();
      if (master) HS_PROBE();
      __builtin_amdgcn_raw_buffer_store_b32(cv0, wsr, vc0, 0, 16); __builtin_amdgcn_raw_buffer_store_b32(cv1, wsr, vc1, 0, 16);
      STWS(e4, l16, WS_ST2 + (rp * 16 + cb) * 2048 + aw * 1024);
      BAR();
      STAMPT(2, 4, 384);
      if (master) { hs_check(); HS_PROBE(); }
      BAR();
      STAMPT(2, 5, 384);
      if (master) { hs_check(); HS_PROBE(); }
      BAR();
      STAMPT(2, 6, 384);
      if (master) hs_check();
      if (prober) HS_PROBE();
      BAR();
      STAMPT(2, 7, 384);
      if (prober) {
        hs_check();
#pragma nounroll
        for (unsigned s_ = 0; s_ < SPIN_CAP && !done; ++s_) { HS_PROBE(); hs_check(); }
      }
      BAR();
      STAMPT(2, 8, 384);
      unsigned cpf;
      asm volatile("global_load_dword %0, %1, %2" : "=v"(cpf) : "v"(vcp), "s"(pc0) : "memory");
      u32x4 sg[16], wrv;
      asm volatile("global_load_dwordx4 %0, %1, %2" : "=v"(wrv) : "v"(l16 & 127u), "s"(uni(w_r + cb * 64)) : "memory");
#pragma unroll
      for (int i = 0; i < 16; ++i) {
        asm volatile("global_load_dwordx4 %0, %1, %2 sc1" : "=v"(sg[i]) : "v"(sg_off), "s"(wsu + i * sg_st) : "memory");
        if (i == SGS) { BAR(); STAMPT(2, 9, 384); }
      }
      auto tsum = [&](int o) __attribute__((always_inline)) {
        f32x4 t[4];
#pragma unroll
        for (int i = 0; i < 4; ++i) t[i] = add4(__builtin_bit_cast(f32x4, sg[o + i]), __builtin_bit_cast(f32x4, sg[o + 4 + i]));
        return add4(add4(t[0], t[2]), add4(t[1], t[3]));
      };
      WAITV(8);
#pragma unroll
      for (int i = 0; i < 8; ++i) asm volatile("" : "+v"(sg[i]));
      asm volatile("" : "+v"(wrv) : "v"(cpf));
      f32x4 sm = tsum(0), sn;
      asm volatile("" : "+v"(sm));
      BAR();
      STAMPT(2, 10, 384);
      WAITV(0);
#pragma unroll
      for (int i = 8; i < 16; ++i) asm volatile("" : "+v"(sg[i]));
      sn = tsum(8);
      asm volatile("" : "+v"(sn));
      BAR();
      STAMPT(2, 11, 384);
      sm = add4(sm, sn);

      const float chk = (sm[0] + sm[1]) + (sm[2] + sm[3]);
      if (__builtin_expect(__any(chk != chk), 0)) {
#pragma nounroll
        for (unsigned s_ = 0; s_ < SPIN_CAP; ++s_) {
          unsigned mx = 0;
#pragma unroll
          for (int i = 0; i < 16; ++i) { sg[i] = LDWS(sg_off, i * sg_st); mx = max(max(mx, max(sg[i][0], sg[i][1])), max(sg[i][2], sg[i][3])); }
          if (!__any(mx == EMPTY)) break;
          asm volatile("" ::: "memory");
        }
        sm = add4(tsum(0), tsum(8));
      }
      STAMPT(1, 1, 384);
      BAR();
      STAMPT(2, 12, 384);
      if (aw == 1) {
        *(LAS f32x4*)(size_t)lw0 = lane < 48 ? sm : (f32x4){0.f, 0.f, 0.f, 0.f};
        *(LAS u32x4*)(size_t)lw1 = wrv;
      } else {
#pragma unroll
        for (int e = 0; e < 2; ++e) {
          mean[e] = sm[2 * e] * (1.0f / 1024.0f);
          vr[e] = fmaxf(sm[2 * e + 1] * (1.0f / 1024.0f) - mean[e] * mean[e], 0.f);
          asm volatile("" : "+v"(mean[e]), "+v"(vr[e]));
        }
      }
      LBAR();
      STAMPT(2, 13, 384);
    }
    unsigned wt;
    asm volatile("global_load_dword %0, %1, %2" : "=v"(wt) : "v"(wto), "s"(uni(wo)) : "memory");
#pragma unroll
    for (int i = 0; i < THR - 1; ++i) wload(i);
    if (hw == 2) {
      f32x2 ra, rb_;
#pragma unroll
      for (int e = 0; e < 2; ++e) {
        ra[e] = __builtin_amdgcn_rcpf(rbf(__builtin_amdgcn_sqrtf(rbf(rbf(vr[e]) + 1e-5f))));
        rb_[e] = -rbf(mean[e]) * ra[e];
      }
      *(LAS f32x2*)(size_t)lsa = ra; *(LAS f32x2*)(size_t)(lsa + 512) = rb_;
      asm volatile("s_waitcnt lgkmcnt(0)" ::: "memory");
      misc[7] = 1;
    }
    STAMPT(1, 2, 384);
#pragma unroll
    for (int i = THR - 1; i < NW1; ++i) { WAITV(THR - 1); wload(i); }
    u32x4 wr_[20];
#define RLOAD(dst, vo, base) asm volatile("global_load_dwordx4 %0, %1, %2" : "=v"(dst) : "v"(vo), "s"(uni(base)) : "memory")
#pragma unroll
    for (int i = 0; i < NR; ++i) {
      WAITV(THR - 1);
      RLOAD(wr_[i], voW, uni(wo + cb * 64) + (size_t)((i + 12) ^ hrot) * (32 * D_ * 2));
      if (i % 6 == 0) STAMPT(3, 8 + i / 6, 256);
    }
#pragma nounroll
    for (unsigned s_ = 0; s_ < SPIN_CAP; ++s_) if (__all(misc[28 + (lane & 3)] != 0)) break;
#pragma unroll
    for (int j = 0; j < NE; ++j) { WAITV(THR - 1); stageW(j); }
    {
      u32x4 pv[16], zq;
      const unsigned ko = WS_KVP + hp * 16 * KVB + (hw >> 1) * 2176;
      unsigned kl = ko + (hw & 1) * 1024 + lane * 16, zo = ko + (lane & 15) * KVB + 2048 + (hw & 1) * 64 + (lane >> 4) * 16;
      u32x4 sel[2];
#pragma unroll
      for (int e = 0; e < 2; ++e)
#pragma unroll
        for (int i = 0; i < 4; ++i)
          sel[e][i] = ((lane & 15) >> 2) != (lane >> 4) || (i >> 1) != e || (i & 1) != ((lane & 3) >> 1) ? 0u : (lane & 1) ? 0x3F800000u : 0x3F80u;
      f32x4 s[2][2];
      const unsigned lz = opq(lds0 + L_ZL + (hw >> 1) * 64 + (lane >> 4) * 16 + (hw & 1) * 8), lk = opq(lds0 + L_KVL + hw * 1024 + lane * 16);
      auto ksum = [&]() __attribute__((always_inline)) {
        {
          float z[4];
#pragma unroll
          for (int i = 0; i < 4; ++i) { float v = __uint_as_float(zq[i]); v += dppf<DPP_X1>(v); v += dppf<DPP_X2>(v); v += dppf<DPP_HMIR>(v); z[i] = v + dppf<DPP_MIR>(v); }
          *(LAS u32x2*)(size_t)lz = (u32x2){pack2(z[0], z[1]), pack2(z[2], z[3])};
          SB0();
        }
#pragma unroll
        for (int p = 0; p < 16; ++p) {
#pragma unroll
          for (int e = 0; e < 2; ++e) s[p >> 3][e] = mfma16(sel[e], pv[p], (p & 7) ? s[p >> 3][e] : (f32x4){0.f, 0.f, 0.f, 0.f});
          SB0();
        }
        s[0][0] += s[1][0]; s[0][1] += s[1][1];
      };
      unsigned kl2 = kl + 4 * KVB;
      asm volatile("" : "+v"(sel[0]), "+v"(sel[1]), "+v"(kl), "+v"(kl2), "+v"(zo));
    WAITV(0);
#pragma unroll
    for (int i = 0; i < NR; ++i) asm volatile("" : "+v"(wr_[i]));
#pragma unroll
    for (int i = 0; i < NW1; ++i) wpin(i);
    asm volatile("" :: "v"(wt));
    STAMPT(1, 3, 256);
#pragma nounroll
      for (unsigned s_ = 0; s_ < SPIN_CAP; ++s_) { if (__builtin_amdgcn_readfirstlane(misc[16 + hw]) == 2) break; __builtin_amdgcn_s_sleep(1); }
#pragma nounroll
      for (unsigned s_ = 0; s_ < SPIN_CAP; ++s_) {
        zq = LDW(zo);
#pragma unroll
        for (int p = 0; p < 16; ++p) { pv[p] = LDWS((p & 4) ? kl2 : kl, ((p >> 3) ^ (idx & 1)) * (8 * KVB) + (p & 3) * KVB); SB0(); }
        ksum();
        const float chk = s[0][0][0] + __uint_as_float(zq[1]) + __uint_as_float(zq[3]);
        if (__builtin_expect(!__any(chk != chk), 1)) break;
        unsigned mk = max(zq[1], zq[3]);
#pragma unroll
        for (int p = 0; p < 16; ++p) mk = max(max(mk, pv[p][1]), pv[p][3]);
        if (!__any(mk == EMPTY)) break;
#pragma nounroll
        for (; s_ < SPIN_CAP; ++s_)
          if (__all(ld32(ws, ko + (lane & 15) * KVB + ((lane & 16) ? 2048 + 124 : (hw & 1) * 1024 + 1020)) != EMPTY)) break;
        asm volatile("" ::: "memory");
      }
      STAMPT(1, 4, 256);
      *(LAS u32x4*)(size_t)lk = (u32x4){pack2(s[0][0][0], s[0][0][1]), pack2(s[0][0][2], s[0][0][3]), pack2(s[0][1][0], s[0][1][1]), pack2(s[0][1][2], s[0][1][3])};
    }
    LBAR();
#pragma unroll
    for (int j = NE; j < 32; ++j) if (j < 12 || j >= NR + 12) { WAITV(THR3 - 1); stageW(j); }
#pragma nounroll
    for (unsigned s_ = 0; s_ < SPIN_CAP; ++s_) { if (__all(misc[24 + (lane & 3)] != 0)) break; __builtin_amdgcn_s_sleep(1); }
#pragma unroll
    for (int i = 0; i < NR; ++i) *(u32x4*)(smem + wslot(i + 12) + hw * 1024 + lane * 16) = wr_[i];
    asm volatile("s_waitcnt lgkmcnt(0)" ::: "memory");
    {
      const u32x4 e5 = {EMPTY, EMPTY, EMPTY, EMPTY};
      if (has_mlp && hw >= 2) STW(e5, WS_YP + (fs >> 2) * 8192 + (hw & 1) * 4096 + (fs & 3) * 1024 + lane * 16);
#pragma unroll
      for (int rr_ = 0; rr_ < XSR; rr_ += 2)
        if (rr_ + (hw >> 1) < XSR) STW(e5, WS_XS + (rr_ + (hw >> 1)) * 524288 + (rp * 16 + ((mt >> 3) & 15)) * 2048 + cb * 128 + (mt & 7) * 16);
    }
    WAITV(0);
    STAMPT(1, 6, 256);
    if (hw == 0) {
      __builtin_amdgcn_s_sleep(SLB);
#pragma nounroll
      for (unsigned s_ = 0; s_ < SPIN_CAP; ++s_) { if (__all(ld32(ws, SY_FB + rp * 256 + lane * 4) != EMPTY)) break; __builtin_amdgcn_s_sleep(SLB1); }
    }
    BAR();
#pragma nounroll
    for (unsigned s_ = 0; s_ < SPIN_CAP; ++s_) { if (__builtin_amdgcn_readfirstlane(misc[20 + hw])) break; __builtin_amdgcn_s_sleep(2); }
    unsigned cp3;
    asm volatile("global_load_dword %0, %1, %2" : "=v"(cp3) : "v"(min((unsigned)(idx * CODE_PER + min(mt >> 4, CODE_PER - 1)) * 128u, clen)), "s"(pc0) : "memory");
#pragma unroll
    for (int i = NW1; i < 24; ++i) { WAITV(THR2 - 1); wload(i); }
    WAITV(0);
    asm volatile("" :: "v"(cp3));
#pragma unroll
    for (int i = NW1; i < 24; ++i) wpin(i);
    STAMPT(1, 7, 256);
    const int c16 = lane & 15, kq = lane >> 4;
    u32x4 gf[2][8];
    u32x4 df[4][2];
    {
#pragma nounroll
      for (unsigned s_ = 0; s_ < SPIN_CAP; ++s_) { if (__all(misc[8 + (lane & 3)] != 0)) break; __builtin_amdgcn_s_sleep(1); }
      STAMPT(3, 5, 256);
#pragma unroll
      for (int i = 0; i < 8; ++i) {
        const int go = hw * 8192 + i * 1024 + ((lane ^ (((lane >> 4) & 1) * 2 + (i & 1) * 4)) * 16);
        *(u32x4*)(smem + go) = wgr[i];
        *(u32x4*)(smem + 32768 + go) = wur[i];
        *(u32x4*)(smem + L_DW + hw * 8192 + i * 1024 + ((lane ^ (((lane >> 4) & 3) * 2)) * 16)) = wdr[i];
      }
      asm volatile("s_waitcnt lgkmcnt(0)" ::: "memory");
      STAMPT(3, 6, 256);
      misc[12 + hw] = 1;
#pragma nounroll
      for (unsigned s_ = 0; s_ < SPIN_CAP; ++s_) if (__all(misc[12 + (lane & 3)] != 0)) break;
      asm volatile("" ::: "memory");
      STAMPT(3, 7, 256);
      const unsigned gb = opq(lds0 + (kq * 8 + (c16 >> 2)) * 128 + (hw ^ ((c16 >> 3) + (kq & 1) * 2)) * 32 + (lane & 3) * 8);
      const unsigned db = lds0 + L_DW + hw * 8192 + (kq * 4 + (c16 >> 2)) * 128 + (lane & 3) * 8, dsw = (c16 >> 3) + (kq & 1) * 2;
#pragma unroll
      for (int m = 0; m < 2; ++m)
#pragma unroll
        for (int k = 0; k < 8; ++k) gf[m][k] = cat(trread(gb, m * 32768 + k * 4096), trread(gb, m * 32768 + k * 4096 + 512));
#pragma unroll
      for (int t = 0; t < 4; ++t) {
        const unsigned dt = opq(db + (t ^ dsw) * 32);
#pragma unroll
        for (int e = 0; e < 2; ++e) df[t][e] = cat(trread(dt, ((t - 2 * e) & 3) * 2048), trread(dt, ((t - 2 * e - 1) & 3) * 2048));
      }
      SB0();
#pragma unroll
      for (int k = 0; k < 8; ++k) asm volatile("" : "+v"(gf[0][k]), "+v"(gf[1][k]), "+v"(df[k >> 1][k & 1]));
    }
    const unsigned gu_st = opq(c16 < 4 ? WS_GU + fs * 1024 + hw * 256 + c16 * 64 + kq * 16 : 0x80000000u);
    const unsigned gu_ld = opq(WS_GU + (fs & ~3) * 1024 + c16 * 64 + kq * 16);
    const unsigned yp_st = opq((c16 & 2) ? 0x80000000u : WS_YP + (fs >> 2) * 8192 + (c16 & 1) * 4096 + (fs & 3) * 1024 + hw * 256 + (c16 >> 2) * 64 + kq * 16);
    const unsigned xsb = opq(lds0 + L_XS + (c16 & 1) * 528 + kq * 16);
    STAMPT(1, 8, 256);
    BAR();
    if (has_mlp) {
      f32x4 ga[2] = {{0.f, 0.f, 0.f, 0.f}, {0.f, 0.f, 0.f, 0.f}};
      u32x4 xf[8];
#pragma unroll
      for (int k = 0; k < 8; ++k) xf[k] = ldsr(xsb, k * 64);
#pragma unroll
      for (int k = 0; k < 8; ++k) { ga[0] = mfma16(gf[0][k], xf[k], ga[0]); ga[1] = mfma16(gf[1][k], xf[k], ga[1]); }
      STW(seal(__builtin_bit_cast(u32x4, (c16 & 2) ? ga[1] : ga[0])), gu_st);
      STAMPT(1, 14, 256);
      __builtin_amdgcn_s_sleep(SLG0);
      u32x4 q[4];
#pragma nounroll
      for (unsigned s_ = 0; s_ < SPIN_CAP; ++s_) {
        unsigned mk = 0u;
#pragma unroll
        for (int p = 0; p < 4; ++p) q[p] = LDW(gu_ld + p * 1024);
#pragma unroll
        for (int p = 0; p < 4; ++p) mk = max(max(mk, q[p][1]), q[p][3]);
        if (!__any(mk == EMPTY)) break;
        asm volatile("" ::: "memory");
        __builtin_amdgcn_s_sleep(SLG1);
      }
      STAMPT(1, 15, 256);
      const f32x4 tot = (__builtin_bit_cast(f32x4, q[0]) + __builtin_bit_cast(f32x4, q[1])) + (__builtin_bit_cast(f32x4, q[2]) + __builtin_bit_cast(f32x4, q[3]));
      float av[4];
#pragma unroll
      for (int i = 0; i < 4; ++i) {
        const float v = rbf(tot[i]), o_ = dppf<DPP_X2>(v);
        const float gs = (c16 & 2) ? o_ : v, us = (c16 & 2) ? v : o_;
        av[i] = rbf(gs * __builtin_amdgcn_rcpf(1.0f + __builtin_amdgcn_exp2f(gs * -1.44269504088896340736f))) * us;
      }
      const unsigned a0 = pack2(av[0], av[1]), a1 = pack2(av[2], av[3]);
      const u32x4 b0 = {a0, a1, dppu<DPP_ROR4>(a0), dppu<DPP_ROR4>(a1)}, b1 = {dppu<DPP_ROR8>(a0), dppu<DPP_ROR8>(a1), dppu<DPP_ROR12>(a0), dppu<DPP_ROR12>(a1)};
      f32x4 y[4];
#pragma unroll
      for (int t = 0; t < 4; ++t) y[t] = mfma16(df[t][0], b0, (f32x4){0.f, 0.f, 0.f, 0.f});
#pragma unroll
      for (int t = 0; t < 4; ++t) y[t] = mfma16(df[t][1], b1, y[t]);
      const int T = c16 >> 2;
      STW(seal(__builtin_bit_cast(u32x4, T == 0 ? y[0] : T == 1 ? y[1] : T == 2 ? y[2] : y[3])), yp_st);
      STAMPT(0, 13, 256);
      misc[0] = 1;
    }
  }
}

extern "C" void kernel_launch(void* const* d_in, const int* in_sizes, int n_in, void* d_out, int out_size,
                              void* d_ws, size_t ws_size, hipStream_t stream) {
  (void)hipFuncSetAttribute((const void*)fused, hipFuncAttributeMaxDynamicSharedMemorySize, LDS_TOTAL);
  fused<<<256, 512, LDS_TOTAL, stream>>>((const u16*)d_in[0], (const u16*)d_in[1], (const u16*)d_in[2], (const u16*)d_in[4],
                                         (const u16*)d_in[5], (const u16*)d_in[6], (const u16*)d_in[7], (u16*)d_out, (char*)d_ws);
}
```

```cpp
#include <hip/hip_runtime.h>
#include <stdint.h>

#define LAS __attribute__((address_space(3)))
#define GAS __attribute__((address_space(1)))
#define DI __device__ __forceinline__

#define XSR 1
#define GPF 5
#define GOW 14
#define NE 4
#define HRM 3
#define OW0 4
#define SLC0 30
#define SLC1 8
#define SLD0 0
#define SLB1 0
#define HRS 4
#define SLD1 0
#define SGS 7
#define GB 1
#define NR 20
#define THR3 4
#define THR2 6
#define THR 6
#define NW1 10
#define SLB 0
constexpr int CODE_PER = 11;
constexpr int XSL1 = 2;
#define YSL0 12
#define YSL1 4
#define SLG0 14
#define SLG1 2
typedef unsigned short u16;
typedef unsigned long long u64;
typedef __attribute__((ext_vector_type(2))) __bf16 bf16x2;
typedef __attribute__((ext_vector_type(8))) __bf16 bf16x8;
typedef __attribute__((ext_vector_type(4))) short s16x4;
typedef __attribute__((ext_vector_type(16))) float f32x16;
typedef __attribute__((ext_vector_type(4))) float f32x4;
typedef __attribute__((ext_vector_type(2))) float f32x2;
typedef __attribute__((ext_vector_type(4))) unsigned u32x4;
typedef __attribute__((ext_vector_type(2))) unsigned u32x2;
typedef GAS unsigned gu32;
typedef GAS unsigned long long gu64;

constexpr int S_ = 2048, D_ = 1024, F_ = 3584;

constexpr unsigned SY_CH = 0, SY_GO = 1024, SY_RE = 2048, SY_FB = 4096, SY_FD = 8192;
constexpr unsigned WS_RS  = 16384;
constexpr unsigned WS_CS  = WS_RS + 256 * 1024;
constexpr unsigned KVB    = 4352;
constexpr unsigned WS_KVP = WS_CS + 256 * 768;
constexpr unsigned WS_ST2 = WS_KVP + 256 * KVB;
constexpr unsigned WS_GU  = WS_ST2 + 256 * 2048;
constexpr unsigned WS_XS  = WS_GU + 256 * 1024;
constexpr unsigned WS_YP  = WS_XS + XSR * 524288;
constexpr unsigned WS_O   = WS_YP + 56 * 8192;
constexpr unsigned WS_END = WS_O + S_ * D_ * 2;
constexpr size_t WS_DIAG  = 64u << 20;
#define STAMPT(kid, i, T) do {} while (0)

DI float bflo(unsigned u) { return __uint_as_float(u << 16); }
DI float bfhi(unsigned u) { return __uint_as_float(u & 0xffff0000u); }
DI float rbf(float x) { return (float)(__bf16)x; }
DI unsigned pack2(float a, float b) { bf16x2 c; c[0] = (__bf16)a; c[1] = (__bf16)b; return __builtin_bit_cast(unsigned, c); }
DI f32x4 mfma16(u32x4 a, u32x4 b, f32x4 c) {
  return __builtin_amdgcn_mfma_f32_16x16x32_bf16(__builtin_bit_cast(bf16x8, a), __builtin_bit_cast(bf16x8, b), c, 0, 0, 0);
}
DI f32x16 mfma32(u32x4 a, u32x4 b, f32x16 c) {
  return __builtin_amdgcn_mfma_f32_32x32x16_bf16(__builtin_bit_cast(bf16x8, a), __builtin_bit_cast(bf16x8, b), c, 0, 0, 0);
}
DI u32x2 trread(const char* p) {
  s16x4 v = __builtin_amdgcn_ds_read_tr16_b64_v4i16((LAS s16x4*)p);
  return __builtin_bit_cast(u32x2, v);
}
DI unsigned opq(unsigned v) { asm volatile("" : "+v"(v)); return v; }
DI u32x2 trread(unsigned base, int off) {
  s16x4 v = __builtin_amdgcn_ds_read_tr16_b64_v4i16((LAS s16x4*)((LAS char*)(size_t)base + off));
  return __builtin_bit_cast(u32x2, v);
}
DI u32x4 ldsr(unsigned base, int off) { return *(const LAS u32x4*)((const LAS char*)(size_t)base + off); }
DI u32x4 cat(u32x2 a, u32x2 b) { return (u32x4){a[0], a[1], b[0], b[1]}; }
DI f32x4 add4(f32x4 a, f32x4 b) {
  f32x4 c;
#pragma unroll
  for (int i = 0; i < 4; ++i) asm("v_add_f32 %0, %1, %2" : "=v"(c[i]) : "v"(a[i]), "v"(b[i]));
  return c;
}
DI float dot2(unsigned a, unsigned b, float c) {
  return __builtin_amdgcn_fdot2_f32_bf16(__builtin_bit_cast(bf16x2, a), __builtin_bit_cast(bf16x2, b), c, false);
}
DI const char* uni(const void* p) {
  const u64 v = (u64)p;
  return (const char*)(((u64)(unsigned)__builtin_amdgcn_readfirstlane((int)(v >> 32)) << 32) | (unsigned)__builtin_amdgcn_readfirstlane((int)v));
}
DI void glds16(unsigned voff, const void* sbase, unsigned lds) {
  asm volatile("s_mov_b32 m0, %2\n\ts_nop 0\n\tglobal_load_lds_dwordx4 %0, %1" ::"v"(voff), "s"(uni(sbase)), "s"(__builtin_amdgcn_readfirstlane(lds)) : "memory");
}
DI float xh_sum(float v) {
  auto r = __builtin_amdgcn_permlane32_swap(__float_as_uint(v), __float_as_uint(v), false, false);
  return __uint_as_float(r[0]) + __uint_as_float(r[1]);
}
template <int CTRL> DI unsigned dppu(unsigned v) { return (unsigned)__builtin_amdgcn_update_dpp(0, (int)v, CTRL, 0xf, 0xf, true); }
template <int CTRL> DI float dppf(float v) { return __uint_as_float(dppu<CTRL>(__float_as_uint(v))); }
constexpr int DPP_X1 = 0xB1, DPP_X2 = 0x4E, DPP_HMIR = 0x141, DPP_MIR = 0x140, DPP_ROR8 = 0x128, DPP_ROR4 = 0x124, DPP_ROR12 = 0x12C;
DI float x16_sum(float v) {
  auto r = __builtin_amdgcn_permlane16_swap(__float_as_uint(v), __float_as_uint(v), false, false);
  return __uint_as_float(r[0]) + __uint_as_float(r[1]);
}
DI unsigned phi_pair(f32x2 t) {
  const f32x2 u = t + 1.000001f;
  const f32x2 l = t * 1.44269504088896340736f;
  const f32x2 e = (f32x2){__builtin_amdgcn_exp2f(l[0]), __builtin_amdgcn_exp2f(l[1])} + 1e-6f;
  return pack2(__builtin_amdgcn_fmed3f(e[0], u[0], 1.000001f), __builtin_amdgcn_fmed3f(e[1], u[1], 1.000001f));
}
#define RLX_AGENT __ATOMIC_RELAXED, __HIP_MEMORY_SCOPE_AGENT
DI void st32(char* ws, unsigned off, unsigned v) { __hip_atomic_store((gu32*)(ws + off), v, RLX_AGENT); }
DI void sw32(char* ws, unsigned off, unsigned v) { (void)__hip_atomic_exchange((gu32*)(ws + off), v, RLX_AGENT); }
DI void sw64(char* ws, unsigned off, u64 v) { (void)__hip_atomic_exchange((gu64*)(ws + off), v, RLX_AGENT); }
DI unsigned ld32(char* ws, unsigned off) { return __hip_atomic_load((gu32*)(ws + off), RLX_AGENT); }
DI u64 ld64(char* ws, unsigned off) { return __hip_atomic_load((gu64*)(ws + off), RLX_AGENT); }
#define WAITV(n) asm volatile("s_waitcnt vmcnt(%0)" :: "n"(n) : "memory")
#define BAR() do { asm volatile("" ::: "memory"); __builtin_amdgcn_s_barrier(); asm volatile("" ::: "memory"); } while (0)
#define LBAR() do { asm volatile("s_waitcnt lgkmcnt(0)" ::: "memory"); __builtin_amdgcn_s_barrier(); asm volatile("" ::: "memory"); } while (0)
#define SB0() __builtin_amdgcn_sched_barrier(0)
constexpr unsigned SPIN_CAP = 1u << 22;
constexpr unsigned EMPTY = 0xFFFFFFFFu;
DI u32x4 seal(u32x4 v) { v[1] = min(v[1], 0xFFFFFFFEu); v[3] = min(v[3], 0xFFFFFFFEu); return v; }
DI unsigned mk_key(float v, unsigned idx) {
  const unsigned u = __float_as_uint(v) >> 16;
  const unsigned o = (u & 0x8000u) ? (~u & 0xffffu) : (u | 0x8000u);
  return (o << 16) | (0xFFFFu - idx);
}
DI unsigned key_idx(unsigned k) { return (0xFFFFu - (k & 0xFFFFu)) & (S_ - 1); }
DI float key_val(unsigned k) { const unsigned o = k >> 16; return __uint_as_float(((o & 0x8000u) ? (o & 0x7fffu) : (~o & 0xffffu)) << 16); }
DI void top2_mix(unsigned& b0, unsigned& b1, unsigned c0, unsigned c1) {
  const unsigned lo = min(b0, c0), mx = max(b1, c1);
  b0 = max(b0, c0); b1 = max(lo, mx);
}
DI void top2_wave(unsigned& b0, unsigned& b1) {
  top2_mix(b0, b1, dppu<DPP_X1>(b0), dppu<DPP_X1>(b1));
  top2_mix(b0, b1, dppu<DPP_X2>(b0), dppu<DPP_X2>(b1));
  top2_mix(b0, b1, dppu<DPP_HMIR>(b0), dppu<DPP_HMIR>(b1));
  top2_mix(b0, b1, dppu<DPP_MIR>(b0), dppu<DPP_MIR>(b1));
  { auto p = __builtin_amdgcn_permlane16_swap(b0, b0, false, false); auto q = __builtin_amdgcn_permlane16_swap(b1, b1, false, false);
    b0 = p[0]; b1 = q[0]; top2_mix(b0, b1, p[1], q[1]); }
  { auto p = __builtin_amdgcn_permlane32_swap(b0, b0, false, false); auto q = __builtin_amdgcn_permlane32_swap(b1, b1, false, false);
    b0 = p[0]; b1 = q[0]; top2_mix(b0, b1, p[1], q[1]); }
}

constexpr int L_A = 0, L_Q = 65536, L_KV = 98304, LDS_TOTAL = 163840;
constexpr int L_M  = L_KV;
constexpr int L_SA = L_M, L_SB = L_M + 512, L_CS = L_M + 1024;
constexpr int L_MISC = L_M + 1792;
constexpr int L_XCH = L_M + 2048;
constexpr int L_KVL = L_M + 11264;
constexpr int L_ZL = L_M + 15360;
constexpr int L_XS = L_M + 3072;
constexpr int L_TB = L_M + 9472;
constexpr int L_WR = L_M + 15872;
constexpr int L_DW = 114688;
constexpr int L_X2 = 81920;
DI constexpr int wslot(int j) { return j < 12 ? 114688 + j * 4096 : (j - 12) * 4096; }

__global__ __launch_bounds__(512, 2) void fused(const u16* __restrict__ x, const u16* __restrict__ wqkv,
                                                const u16* __restrict__ wo, const u16* __restrict__ w_r,
                                                const u16* __restrict__ wg, const u16* __restrict__ wu,
                                                const u16* __restrict__ wd, u16* __restrict__ out, char* __restrict__ ws) {
  extern __shared__ __attribute__((aligned(16))) char smem[];
  u64 pc0; unsigned clen;
  asm volatile("s_getpc_b64 %0\n.Lfused_pc:\n\ts_mov_b32 %1, .Lfused_end-.Lfused_pc-16" : "=s"(pc0), "=s"(clen));
  SB0();
  const int tid = threadIdx.x, lane = tid & 63;
  const int wave = __builtin_amdgcn_readfirstlane(tid >> 6);
  STAMPT(0, 0, 0);
  const int bid = blockIdx.x, xcd = bid & 7, idx = bid >> 3;
  const int rb = (xcd & 1) * 8 + (idx & 7);
  const int hp = (xcd >> 1) * 4 + (idx >> 3);
  const int rp = rb, cb = ((3 + (xcd >> 1) + 2 * (xcd & 1)) & 3) * 4 + (hp & 3);
  const int hrot = HRS * (cb & HRM);
  const unsigned lds0 = (unsigned)(size_t)smem;
  const __amdgpu_buffer_rsrc_t wsr = __builtin_amdgcn_make_buffer_rsrc(ws, 0, WS_END, 0x00020000);
#define LDW(off) __builtin_amdgcn_raw_buffer_load_b128(wsr, (off), 0, 16)
#define LDWS(off, so) __builtin_amdgcn_raw_buffer_load_b128(wsr, (off), (so), 16)
#define STW(v, off) __builtin_amdgcn_raw_buffer_store_b128((v), wsr, (off), 0, 16)
#define STWS(v, off, so) __builtin_amdgcn_raw_buffer_store_b128((v), wsr, (off), (so), 16)
  const int kph = 4 * (xcd >> 1) + 8 * (xcd & 1), kg_ = (hp + rb) & 3;
  auto korder = [&](int t) { return ((t + kph) & 15) ^ kg_; };
  u32x4 e0;
  {
    const int r0 = 16 * wave + (lane >> 3), r1 = r0 + 8, pb_ = lane >> 4;
    const unsigned a0 = opq((unsigned)(r0 * D_ + ((lane & 7) ^ ((r0 >> 1) & 7)) * 8) * 2u), a1 = opq((unsigned)(r1 * D_ + ((lane & 7) ^ ((r1 >> 1) & 7)) * 8) * 2u);
    const unsigned b0 = opq((unsigned)((8 * wave + 4 * (pb_ >> 1) + ((lane >> 2) & 3)) * (3 * D_) + (pb_ & 1) * 32 + ((lane & 3) ^ ((wave & 1) * 2)) * 8) * 2u);
    SB0();
    auto first = [&](int t, int dw) {
      const char* pa = uni(x + (size_t)(rb * 128 + 16 * dw) * D_) + korder(t) * 128;
      const char* pb = uni(wqkv + (size_t)(8 * dw) * (3 * D_) + hp * 64) + (size_t)korder(t) * (64 * 3 * D_ * 2);
      const int w = wave + dw;
      glds16(a0, pa, lds0 + L_A + t * 16384 + w * 2048); glds16(a1, pa, lds0 + L_A + t * 16384 + w * 2048 + 1024);
      glds16(b0 + 2048, pb, lds0 + L_KV + t * 16384 + w * 1024); glds16(b0 + 4096, pb, lds0 + L_KV + t * 16384 + 8192 + w * 1024);
      glds16(b0, pb, lds0 + L_Q + t * 8192 + w * 1024);
    };
    STAMPT(3, 0, 0);
    first(0, 0);
    STAMPT(3, 4, 0);
    SB0();
    BAR();
    STAMPT(3, 12, 0);
    if (wave < 6) first(1, 0);
    if (wave == 4 || wave == 5) first(1, 2);
    {
      const char* eb = wave == 0 ? (const char*)pc0 : wave < 6 ? uni(x) : uni(ws);
      const unsigned eo = wave == 0 ? min((unsigned)(idx * CODE_PER + min(lane, CODE_PER - 1)) * 128u, clen) : wave < 6 ? rb * 262144u : wave == 6 && bid == 0 ? SY_RE + lane * 16 : SY_GO + bid * 4;
      asm volatile("global_load_dwordx4 %0, %1, %2 sc1" : "=v"(e0) : "v"(eo), "s"(uni(eb)) : "memory");
    }
    SB0();
  }
  volatile LAS int* misc = (volatile LAS int*)(size_t)(lds0 + L_MISC);
  const bool has_mlp = bid < 224;
  const int fs = has_mlp ? (bid & 7) * 28 + (bid >> 3) : (bid & 7) * 28;
  const int tcs = (rb & 4) ? 4 : 3;
  const unsigned rs_o = WS_RS + (rb * 16 + korder(0)) * 1024, cs_o = WS_CS + (hp * 16 + korder(tcs)) * 768;
  const u32x4 ONES4 = {0x3F803F80u, 0x3F803F80u, 0x3F803F80u, 0x3F803F80u};

  if (wave < 4) {
    const int wr = wave >> 1, wc = wave & 1;
    const int r = lane & 15, kq = lane >> 4;
    f32x4 acc[4][6];
    {
      unsigned aO[2], bQ[2], bK[2], bV[2];
#pragma unroll
      for (int k = 0; k < 2; ++k) aO[k] = opq(lds0 + L_A + (64 * wr + r) * 128 + (((4 * k + kq) ^ ((r >> 1) & 7)) * 16));
#pragma unroll
      for (int c = 0; c < 2; ++c) {
        const unsigned o_ = lds0 + kq * 1024 + wc * 256 + (r >> 2) * 64 + ((c ^ (kq & 1)) * 32) + (lane & 3) * 8;
        bQ[c] = opq(o_ + L_Q); bK[c] = opq(o_ + L_KV); bV[c] = opq((o_ ^ (wr * 32)) + L_KV);
      }
#pragma unroll
      for (int i = 0; i < 4; ++i)
#pragma unroll
        for (int j = 0; j < 6; ++j) acc[i][j] = (f32x4){0.f, 0.f, 0.f, 0.f};
      struct Fr { u32x4 a[4]; u32x2 bl[6], bh[6]; };
      Fr F[2];
      auto bread = [&](int f, int b, int k, int hi) {
        return f < 2 ? trread(bQ[f & 1], k * 4096 + b * 8192 + hi * 512) : trread(f < 4 ? bK[f & 1] : bV[f & 1], k * 4096 + b * 16384 + ((f >> 1) - 1) * 8192 + hi * 512);
      };
      auto step = [&](Fr& C, Fr& N, int b, int k, int MC, int ML) {
        int ld = 0;
        constexpr int ORD[6] = {2, 3, 4, 5, 0, 1};
        auto load1 = [&]() {
          if (ML == 0) return;
          if (ld < 4) N.a[ld] = ldsr(aO[k], b * 16384 + ld * 2048);
          else {
            const int e = ld - 4, f = ML == 1 ? (e >> 1) : ORD[(e >> 1) % 6], nf = ML == 1 ? 2 : ML == 2 ? 4 : 6;
            if ((e >> 1) < nf) { if (e & 1) N.bh[f] = bread(f, b, k, 1); else N.bl[f] = bread(f, b, k, 0); }
          }
          ++ld;
        };
#pragma unroll
        for (int m = 0; m < 24; ++m) {
          const int j = ORD[m >> 2], i = m & 3;
          if (!((j < 2 ? 1 : 2) & MC)) continue;
          if (j < 2) acc[i][j] = mfma16(cat(C.bl[j], C.bh[j]), C.a[i], acc[i][j]);
          else acc[i][j] = mfma16(C.a[i], cat(C.bl[j], C.bh[j]), acc[i][j]);
          load1();
          SB0();
        }
      };
      WAITV(5); BAR();
      STAMPT(0, 1, 0);
#pragma unroll
      for (int m = 0; m < 4; ++m) F[0].a[m] = ldsr(aO[0], m * 2048);
#pragma unroll
      for (int f = 0; f < 6; ++f) { F[0].bl[f] = bread(f, 0, 0, 0); F[0].bh[f] = bread(f, 0, 0, 1); }
      for (int t4 = 0; t4 < 12; t4 += 4) {
#pragma unroll
        for (int b = 0; b < 4; ++b) {
          step(F[0], F[1], b, 1, 3, 3);
          if (b == 0 && t4 == 0) { asm volatile("s_waitcnt vmcnt(0)" :: "v"(e0) : "memory"); STAMPT(3, 13, 0); BAR(); STAMPT(3, 14, 0); }
          step(F[1], F[0], (b + 1) & 3, 0, 3, 3);
          BAR();
        }
      }
#pragma unroll
      for (int b = 0; b < 4; ++b) {
        step(F[0], F[1], b, 1, 2, 2);
        step(F[1], F[0], (b + 1) & 3, 0, 2, b < 3 ? 2 : 0);
        if (b < 2) BAR();
      }
      misc[28 + wave] = 1;
#pragma nounroll
      for (unsigned s_ = 0; s_ < SPIN_CAP; ++s_) if (__builtin_amdgcn_readfirstlane(misc[7])) break;
      asm volatile("" ::: "memory");
      STAMPT(0, 2, 0);
      {
        const float* sa = (const float*)(smem + L_SA) + 64 * wr + 4 * kq;
        const float* cs = (const float*)(smem + L_CS) + wc * 96 + r;
        const float c4[4] = {cs[32], cs[48], cs[64 + 16 * wr], cs[80 - 16 * wr]};
        unsigned kp[4][2][2], vp[4][2][2];
#pragma unroll
        for (int i = 0; i < 4; ++i) {
          const f32x4 a4 = *(const f32x4*)(sa + 16 * i), b4 = *(const f32x4*)(sa + 128 + 16 * i);
          const f32x2 aa[2] = {{a4[0], a4[1]}, {a4[2], a4[3]}}, bb[2] = {{b4[0], b4[1]}, {b4[2], b4[3]}};
#pragma unroll
          for (int j = 0; j < 2; ++j)
#pragma unroll
            for (int e = 0; e < 2; ++e) {
              const f32x2 tk = (f32x2){acc[i][2 + j][2 * e], acc[i][2 + j][2 * e + 1]} * aa[e] + bb[e] * c4[j];
              const f32x2 tv = (f32x2){acc[i][4 + j][2 * e], acc[i][4 + j][2 * e + 1]} * aa[e] + bb[e] * c4[2 + j];
              kp[i][j][e] = phi_pair(tk);
              vp[i][j][e] = pack2(tv[0], tv[1]);
            }
        }
        f32x4 kv[2][2], zv[2];
#pragma unroll
        for (int d = 0; d < 2; ++d) {
          zv[d] = (f32x4){0.f, 0.f, 0.f, 0.f}; kv[d][0] = zv[d]; kv[d][1] = zv[d];
#pragma unroll
          for (int u = 0; u < 2; ++u) {
            const u32x4 ka = {kp[2 * u][d][0], kp[2 * u][d][1], kp[2 * u + 1][d][0], kp[2 * u + 1][d][1]};
#pragma unroll
            for (int e = 0; e < 2; ++e)
              kv[d][e] = mfma16(ka, (u32x4){vp[2 * u][e][0], vp[2 * u][e][1], vp[2 * u + 1][e][0], vp[2 * u + 1][e][1]}, kv[d][e]);
            zv[d] = mfma16(ka, ONES4, zv[d]);
          }
        }
        char* xo = smem + L_XCH + wave * 2048 + lane * 16;
        *(f32x4*)xo = kv[0][1];
        *(f32x4*)(xo + 1024) = kv[1][1];
        if (r < 2) *(f32x4*)(smem + L_XCH + 8192 + wave * 128 + r * 64 + kq * 16) = r ? zv[1] : zv[0];
        STAMPT(1, 0, 0);
        asm volatile("s_waitcnt lgkmcnt(0)" ::: "memory");
        misc[16 + wave] = 1;
#pragma nounroll
        for (unsigned s_ = 0; s_ < SPIN_CAP; ++s_) if (__builtin_amdgcn_readfirstlane(misc[16 + (wave ^ 2)])) break;
        asm volatile("" ::: "memory");
        const char* xi = smem + L_XCH + (wave ^ 2) * 2048 + lane * 16;
        const f32x4 s0 = *(const f32x4*)xi + kv[0][0], s1 = *(const f32x4*)(xi + 1024) + kv[1][0];
        const unsigned ko = WS_KVP + (hp * 16 + rb) * KVB + wc * 2176;
        STW(seal((u32x4){pack2(s0[0], s0[1]), pack2(s0[2], s0[3]), pack2(s1[0], s1[1]), pack2(s1[2], s1[3])}), ko + wr * 1024 + lane * 16);
        if (wr == 0 && lane < 8) {
          const f32x4 z = *(const f32x4*)(smem + L_XCH + 8192 + wave * 128 + lane * 16) + *(const f32x4*)(smem + L_XCH + 8192 + (wave + 2) * 128 + lane * 16);
          STW(seal(__builtin_bit_cast(u32x4, z)), ko + 2048 + lane * 16);
        }
                STAMPT(0, 3, 0);
#pragma unroll
        for (int m = 0; m < 4; ++m) F[0].a[m] = ldsr(aO[0], m * 2048);
#pragma unroll
        for (int f = 0; f < 2; ++f) { F[0].bl[f] = bread(f, 0, 0, 0); F[0].bh[f] = bread(f, 0, 0, 1); }
      }
#pragma unroll
      for (int b = 0; b < 4; ++b) {
        step(F[0], F[1], b, 1, 1, 1);
        step(F[1], F[0], (b + 1) & 3, 0, 1, b < 3 ? 1 : 0);
        if (b == GB) misc[16 + wave] = 2;
      }
    }
    u32x4 qp[4];
    {
      const float* sa = (const float*)(smem + L_SA) + 64 * wr + r;
      const float* cs = (const float*)(smem + L_CS) + wc * 96 + 4 * kq;
      const f32x4 c0 = *(const f32x4*)cs, c1 = *(const f32x4*)(cs + 16);
#pragma unroll
      for (int i = 0; i < 4; ++i) {
        const float a = sa[16 * i], b = sa[128 + 16 * i];
        qp[i][0] = phi_pair((f32x2){acc[i][0][0], acc[i][0][1]} * a + (f32x2){c0[0], c0[1]} * b);
        qp[i][1] = phi_pair((f32x2){acc[i][0][2], acc[i][0][3]} * a + (f32x2){c0[2], c0[3]} * b);
        qp[i][2] = phi_pair((f32x2){acc[i][1][0], acc[i][1][1]} * a + (f32x2){c1[0], c1[1]} * b);
        qp[i][3] = phi_pair((f32x2){acc[i][1][2], acc[i][1][3]} * a + (f32x2){c1[2], c1[3]} * b);
        asm volatile("" : "+v"(qp[i]));
      }
    }
    u32x2 xr[8];
    {
      const int row = wave * 32 + (lane & 31);
#pragma unroll
      for (int c = 0; c < 8; ++c) xr[c] = *(const u32x2*)(smem + L_A + ((cb & 3) ^ kg_) * 16384 + row * 128 + ((c ^ ((row >> 1) & 7)) * 16) + (lane >> 5) * 8);
    }
    const unsigned lzf = opq(lds0 + L_ZL + wc * 64 + kq * 16), lkf = opq(lds0 + L_KVL + wc * 2048 + lane * 16);
    const unsigned oo = opq(WS_O + ((rb * 8 + wr * 4) * 32 + 2 * hp + wc) * 1024 + lane * 16);
    STAMPT(0, 4, 0);
    LBAR();
    STAMPT(0, 5, 0);
    {
      const u32x4 zf = ldsr(lzf, 0), k0 = ldsr(lkf, 0), k1 = ldsr(lkf, 1024);
      const f32x4 Z4 = {0.f, 0.f, 0.f, 0.f};
      f32x4 dn[4], x0[4], x1[4];
#pragma unroll
      for (int i = 0; i < 4; ++i) dn[i] = mfma16(zf, qp[i], Z4);
#pragma unroll
      for (int i = 0; i < 4; ++i) { x0[i] = mfma16(k0, qp[i], Z4); x1[i] = mfma16(k1, qp[i], Z4); }
      SB0();
#pragma unroll
      for (int i = 0; i < 4; ++i) {
        const float ri = __builtin_amdgcn_rcpf(dn[i][0] + 1e-6f);
        const f32x4 y0 = x0[i] * ri, y1 = x1[i] * ri;
        STW(((u32x4){pack2(y0[0], y0[1]), pack2(y0[2], y0[3]), pack2(y1[0], y1[1]), pack2(y1[2], y1[3])}), oo + i * 32768);
      }
      misc[24 + wave] = 1;
      WAITV(0);
      if (lane == 0) sw32(ws, SY_FB + rb * 256 + hp * 16 + wave * 4, 1u);
    }
    STAMPT(0, 6, 0);
    {
      const int h = lane >> 5, s32 = lane & 31;
      u32x2 wv[8];
      auto wvld = [&]() {
#pragma unroll
        for (int c = 0; c < 8; ++c) wv[c] = *(const volatile LAS u32x2*)(size_t)(lds0 + L_WR + c * 16 + h * 8);
      };
      wvld();
      u32x4 of[32][2];
      const unsigned ol = WS_O + (rp * 8 + wave * 2 + (kq & 1)) * 32768 + (h * 16 + r) * 16;
      auto oload1 = [&](int j, int e) { of[j][e] = LDWS(ol + e * 512 + (j & (HRS - 1)) * 1024, ((j & ~(HRS - 1)) ^ hrot) * 1024); };
      auto oload = [&](int j) { oload1(j, 0); oload1(j, 1); };
      f32x16 a2[4];
      unsigned wO[3];
#pragma unroll
      for (int e = 0; e < 3; ++e) wO[e] = opq(lds0 + e * 65536 + h * 512 + (r >> 2) * 64 + (((kq & 1) ^ h) * 32) + (lane & 3) * 8);
      constexpr int PF = GPF, OW = GOW;
      u32x2 wl[8], wh[8];
      auto wld = [&](int g) {
        const int j = g >> 2;
        const int o_ = (wslot(j) & 65535) + ((g >> 1) & 1) * 1024 + (g & 1) * 256;
        wl[g & 7] = trread(wO[wslot(j) >> 16], o_); wh[g & 7] = trread(wO[wslot(j) >> 16], o_ + 2048);
      };
      const int row = wave * 32 + s32;
      const unsigned xb = opq(lds0 + L_X2 + row * 128 + h * 8);
      const int sw = (row >> 1) & 7;
      float sw_ = 0.f;
#pragma unroll
      for (int c = 0; c < 16; ++c) sw_ = dot2(wv[c >> 1][c & 1], 0x3F803F80u, sw_);
      sw_ = xh_sum(sw_);
      asm volatile("" : "+v"(sw_));
      BAR();
      STAMPT(0, 7, 0);
#pragma unroll
      for (int j = 0; j < OW0; ++j) { oload(j); SB0(); }
#pragma unroll
      for (int c = 0; c < 8; ++c) {
        const u32x2 xx = xr[c];
#pragma unroll
        for (int i = 0; i < 2; ++i) {
          a2[c >> 2][(c & 3) * 4 + 2 * i] = bflo(xx[i]); a2[c >> 2][(c & 3) * 4 + 2 * i + 1] = bfhi(xx[i]);
          a2[2 + (c >> 2)][(c & 3) * 4 + 2 * i] = 0.f; a2[2 + (c >> 2)][(c & 3) * 4 + 2 * i + 1] = 0.f;
        }
      }
#pragma unroll
      for (int g = 0; g < PF; ++g) wld(g);
      SB0();
#pragma unroll
      for (int j = 0; j < 32; ++j) {
        if (j == 30) wvld();
#pragma unroll
        for (int n = 0; n < 4; ++n) {
          const int g = j * 4 + n;
          a2[n] = mfma32(cat(wl[g & 7], wh[g & 7]), of[j][n >> 1], a2[n]);
          SB0();
          if (g + PF < 128) wld(g + PF);
          {
            constexpr int RAMP = OW - OW0;
            const int q = j < RAMP ? 2 * OW0 + 4 * j + n : (n & 1) ? -1 : 2 * (j + OW) + (n >> 1);
            if (q >= 0 && q < 64) oload1(q >> 1, q & 1);
          }
          if (n == 0 && j + OW == 32) misc[20 + wave] = 1;
          SB0();
          asm volatile("" :: "v"(wl[g & 7]), "v"(wh[g & 7]), "v"(of[j][n >> 1]));
          SB0();
        }
      }
      STAMPT(0, 8, 0); STAMPT(3, 1, 64); STAMPT(3, 2, 128); STAMPT(3, 3, 192);
      a2[0] += a2[2]; a2[1] += a2[3];
      misc[8 + wave] = 1;
      unsigned hb[16];
      {
        float s[2] = {0.f, 0.f}, sq[2] = {0.f, 0.f}, pw[2] = {0.f, 0.f};
#pragma unroll
        for (int i = 0; i < 16; ++i) {
          const unsigned t = pack2(a2[i >> 3][(i & 7) * 2], a2[i >> 3][(i & 7) * 2 + 1]);
          hb[i] = t;
          s[i & 1] = dot2(t, 0x3F803F80u, s[i & 1]); sq[i & 1] = dot2(t, t, sq[i & 1]); pw[i & 1] = dot2(t, wv[i >> 1][i & 1], pw[i & 1]);
        }
        const u32x4 st = {__float_as_uint(xh_sum(s[0] + s[1])), __float_as_uint(xh_sum(sq[0] + sq[1])), __float_as_uint(xh_sum(pw[0] + pw[1])), __float_as_uint(sw_)};
        if (h == 0) STW(seal(st), WS_ST2 + ((rp * 16 + cb) * 128 + row) * 16);
      }
      STAMPT(0, 9, 0);
      u32x4 pu_[8];
      __builtin_amdgcn_s_sleep(SLC0);
#pragma nounroll
      for (unsigned s_ = 0; s_ < SPIN_CAP; ++s_) {
        unsigned mk = 0u;
#pragma unroll
        for (int j = 0; j < 8; ++j) { pu_[j] = LDW(WS_ST2 + ((rp * 16 + h * 8 + j) * 128 + row) * 16); mk = max(max(mk, pu_[j][1]), pu_[j][3]); }
        if (!__any(mk == EMPTY)) break;
        asm volatile("" ::: "memory");
        __builtin_amdgcn_s_sleep(SLC1);
      }
      STAMPT(0, 10, 0);
      f32x4 tt = {0.f, 0.f, 0.f, 0.f};
#pragma unroll
      for (int j = 0; j < 8; ++j) tt += __builtin_bit_cast(f32x4, pu_[j]);
      const float mean = xh_sum(tt[0]) * (1.0f / 1024.0f);
      const float var = fmaxf(xh_sum(tt[1]) * (1.0f / 1024.0f) - mean * mean, 0.f);
      const float mb = rbf(mean), rs = __builtin_amdgcn_rcpf(rbf(__builtin_amdgcn_sqrtf(rbf(rbf(var) + 1e-5f))));
      float sc = rbf((xh_sum(tt[2]) - mb * xh_sum(tt[3])) * rs);
      if (!(sc == sc)) sc = -INFINITY;
      unsigned b0 = h ? 0u : mk_key(sc, rp * 128 + row), b1 = 0;
      top2_wave(b0, b1);
      const int pan = rp * 4 + wave;
      if (lane == 0) sw64(ws, SY_FD + cb * 512 + pan * 8, ((u64)b1 << 32) | b0);
      STAMPT(1, 11, 0);
      unsigned nq[16];
#pragma unroll
      for (int i = 0; i < 16; ++i) {
        const unsigned dq = pack2(bflo(hb[i]) - mb, bfhi(hb[i]) - mb);
        nq[i] = pack2(bflo(dq) * rs, bfhi(dq) * rs);
      }
      const int lc0 = key_idx(b0) & 31, lc1 = key_idx(b1) & 31;
      {
        const int c0 = lc0, c1 = lc1;
        char* tb = smem + L_TB + wave * 512;
        if (s32 == c0 || s32 == c1) {
          char* tp = tb + (s32 == c0 ? 0 : 256) + h * 8;
#pragma unroll
          for (int c = 0; c < 8; ++c) { *(u32x2*)(tp + c * 16) = (u32x2){nq[2 * c], nq[2 * c + 1]}; *(u32x2*)(tp + 128 + c * 16) = (u32x2){hb[2 * c], hb[2 * c + 1]}; }
        }
        __builtin_amdgcn_wave_barrier();
        const u32x4 v4 = seal(*(const u32x4*)(tb + s32 * 16));
#pragma unroll
        for (int rr_ = 0; rr_ < XSR; rr_ += 2)
          if (rr_ + h < XSR) STW(v4, WS_XS + (rr_ + h) * 524288 + (pan * 2 + (s32 >> 4)) * 4096 + ((s32 >> 3) & 1) * 2048 + cb * 128 + (s32 & 7) * 16);
      }
      u64 cc = ~0ull;
#pragma unroll
      for (int c = 0; c < 8; ++c) {
        u32x2 ob;
#pragma unroll
        for (int i = 0; i < 2; ++i) ob[i] = pack2(bflo(hb[2 * c + i]) + bflo(nq[2 * c + i]), bfhi(hb[2 * c + i]) + bfhi(nq[2 * c + i]));
        *(LAS u32x2*)((LAS char*)(size_t)(xb + ((c ^ sw) * 16))) = ob;
      }
      auto ost = [&](int rl, bool on) {
        const int rw = wave * 32 + rl;
        const u32x4 v = *(const u32x4*)(smem + L_X2 + rw * 128 + (((lane & 7) ^ ((rw >> 1) & 7)) * 16));
        if (on) *(u32x4*)(out + (size_t)(rp * 128 + rw) * D_ + cb * 64 + (lane & 7) * 8) = v;
      };
      if (wave == 0) {
        __builtin_amdgcn_s_sleep(SLD0);
#pragma nounroll
        for (unsigned s_ = 0; s_ < SPIN_CAP; ++s_) {
          cc = ld64(ws, SY_FD + cb * 512 + lane * 8);
          if (__all((unsigned)(cc >> 32) != EMPTY)) break;
          __builtin_amdgcn_s_sleep(SLD1);
        }
        STAMPT(1, 13, 0);
        unsigned c0 = (unsigned)cc, c1 = (unsigned)(cc >> 32);
        top2_wave(c0, c1);
        const unsigned w0 = key_idx(c0), w1 = key_idx(c1);
        const unsigned k0 = __builtin_amdgcn_readlane((unsigned)cc, w0 >> 5), k1 = __builtin_amdgcn_readlane((unsigned)cc, w1 >> 5);
        const unsigned sl0 = (w0 >> 5) * 2 + (k0 == c0 ? 0 : 1), sl1 = (w1 >> 5) * 2 + (k1 == c1 ? 0 : 1);
        if (lane == 0) { misc[1] = w0; misc[2] = w1; misc[3] = sl0; misc[4] = sl1; misc[5] = c0; misc[6] = c1; }
        if (has_mlp) {
          const unsigned xo = WS_XS + (idx & (XSR - 1)) * 524288u + (h ? sl1 : sl0) * 4096u + (fs & 3) * 512 + s32 * 16;
          u32x4 q;
#pragma nounroll
          for (unsigned s_ = 0; s_ < SPIN_CAP; ++s_) {
            q = LDW(xo);
            if (__all(max(q[1], q[3]) != EMPTY)) break;
            asm volatile("" ::: "memory");
            __builtin_amdgcn_s_sleep(XSL1);
          }
          *(u32x4*)(smem + L_XS + h * 528 + s32 * 16) = q;
        }
      }
      LBAR();
      STAMPT(0, 11, 0);
      const int sel0 = misc[1], sel1 = misc[2];
#pragma unroll
      for (int m = 0; m < 4; ++m) { const int rl = m * 8 + (lane >> 3), grow = rp * 128 + wave * 32 + rl; ost(rl, grow != sel0 && grow != sel1); }
      STAMPT(0, 12, 0);
      if (bid < 64 && wave == 0) {
        const int tk = bid >> 5, c0 = (bid & 31) * 32;
        const unsigned xo = WS_XS + (idx & (XSR - 1)) * 524288u + (unsigned)misc[3 + tk] * 4096u + c0 * 2 + (lane & 7) * 8;
        u64 xn, xh;
#pragma nounroll
        for (unsigned s_ = 0; s_ < SPIN_CAP; ++s_) {
          xn = ld64(ws, xo); xh = ld64(ws, xo + 2048);
          if (__all((unsigned)(xn >> 32) != EMPTY && (unsigned)(xh >> 32) != EMPTY)) break;
        }
        const float sg = rbf(__builtin_amdgcn_rcpf(1.0f + __builtin_amdgcn_exp2f(key_val((unsigned)misc[5 + tk]) * -1.44269504088896340736f)));
        u16* op = out + (size_t)(tk ? sel1 : sel0) * D_ + c0 + (lane & 7) * 4;
        const unsigned yo = opq(WS_YP + (lane >> 3) * 8192 + tk * 4096 + c0 * 4 + (lane & 7) * 16);
#pragma nounroll
        for (unsigned s_ = 0; s_ < SPIN_CAP; ++s_) { if (__builtin_amdgcn_readfirstlane(misc[0])) break; __builtin_amdgcn_s_sleep(1); }
        __builtin_amdgcn_s_sleep(YSL0);
        u32x4 pv[7];
#pragma nounroll
        for (unsigned s_ = 0; s_ < SPIN_CAP; ++s_) {
          unsigned mk = 0u;
#pragma unroll
          for (int p = 0; p < 7; ++p) pv[p] = LDWS(yo, p * 65536);
#pragma unroll
          for (int p = 0; p < 7; ++p) mk = max(max(mk, pv[p][1]), pv[p][3]);
          if (!__any(mk == EMPTY)) break;
          asm volatile("" ::: "memory");
          __builtin_amdgcn_s_sleep(YSL1);
        }
        STAMPT(0, 15, 0);
        f32x4 y = __builtin_bit_cast(f32x4, pv[0]);
#pragma unroll
        for (int p = 1; p < 7; ++p) y += __builtin_bit_cast(f32x4, pv[p]);
        float t[4];
#pragma unroll
        for (int i = 0; i < 4; ++i) {
          float v = y[i]; v += dppf<DPP_ROR8>(v); v = xh_sum(x16_sum(v));
          const unsigned nn = (unsigned)(xn >> (32 * (i >> 1))), hh = (unsigned)(xh >> (32 * (i >> 1)));
          t[i] = ((i & 1) ? bfhi(hh) : bflo(hh)) + rbf(((i & 1) ? bfhi(nn) : bflo(nn)) + rbf(rbf(v) * sg));
        }
        if (lane < 8) *(u32x2*)op = (u32x2){pack2(t[0], t[1]), pack2(t[2], t[3])};
        asm volatile(".Lfused_end:" ::: "memory");
        STAMPT(0, 14, 0);
      }
    }
  } else {
    __builtin_amdgcn_s_setprio(2);
    const int hw = wave - 4, mt = tid & 255;
    u32x4 wgr[8], wur[8], wdr[8];
    const int krow = hw * 64 + (lane >> 3);
    const unsigned gof = opq((unsigned)(((fs & 3) * 256 + krow) * F_ + (fs >> 2) * 64 + (lane & 7) * 8) * 2u);
    const unsigned dof = opq((unsigned)(((fs >> 2) * 64 + (lane >> 3)) * D_ + (fs & 3) * 256 + hw * 64 + (lane & 7) * 8) * 2u);
    const unsigned wto = opq((unsigned)((idx * 32 + (mt >> 3)) * 2048 + (cb >> 2) * 512 + ((mt >> 1) & 3) * 128));
#define WLOAD(dst, vo, base) asm volatile("global_load_dwordx4 %0, %1, %2" : "=v"(dst) : "v"(vo), "s"(uni(base)) : "memory")
    auto wload = [&](int i) {
      if (i < 8) WLOAD(wgr[i], gof, wg + i * (8 * F_));
      else if (i < 16) WLOAD(wur[i - 8], gof, wu + (i - 8) * (8 * F_));
      else WLOAD(wdr[i - 16], dof, wd + (i - 16) * (8 * D_));
    };
    auto wpin = [&](int i) {
      if (i < 8) asm volatile("" : "+v"(wgr[i])); else if (i < 16) asm volatile("" : "+v"(wur[i - 8])); else asm volatile("" : "+v"(wdr[i - 16]));
    };
    const int pbi = lane >> 4, pq = (lane >> 2) & 3, pch = lane & 3;
    const unsigned voW = opq((unsigned)((8 * hw + 4 * (pbi >> 1) + pq) * D_ + (pbi & 1) * 32 + (pch ^ ((pbi >> 1) * 2)) * 8) * 2u);
    auto stageW = [&](int j) { glds16(voW, uni(wo + cb * 64) + (size_t)(j ^ hrot) * (32 * D_ * 2), lds0 + wslot(j) + hw * 1024); };
    u32x4 e4 = {EMPTY, EMPTY, EMPTY, EMPTY};
    asm volatile("" : "+v"(e4));
    const unsigned l16 = opq(lane * 16);
    float mean[2] = {0.f, 0.f}, vr[2] = {0.f, 0.f};
    const unsigned lsa = opq(lds0 + L_SA + lane * 8);
    if (hw < 2) {
      const char* baseA = uni(x + (size_t)rb * 128 * D_);
      const char* baseB = uni(wqkv + hp * 64);
      unsigned voA[2], voB[2];
      {
        const int r0 = 64 * hw + (lane >> 3), r1 = r0 + 8;
        voA[0] = (unsigned)(r0 * D_ + ((lane & 7) ^ ((r0 >> 1) & 7)) * 8) * 2u;
        voA[1] = (unsigned)(r1 * D_ + ((lane & 7) ^ ((r1 >> 1) & 7)) * 8) * 2u;
        voB[0] = (unsigned)((32 * hw + 4 * (pbi >> 1) + pq) * (3 * D_) + (pbi & 1) * 32 + pch * 8) * 2u;
        voB[1] = (unsigned)((32 * hw + 8 + 4 * (pbi >> 1) + pq) * (3 * D_) + (pbi & 1) * 32 + (pch ^ 2) * 8) * 2u;
      }
      auto stage = [&](int t) {
        const int kt = korder(t), b = t & 3;
        const char* pa = baseA + kt * 128;
        const char* pb = baseB + (size_t)kt * (64 * 3 * D_ * 2);
#pragma unroll
        for (int j = 0; j < 8; ++j) glds16(voA[j & 1] + (j >> 1) * (16 * D_ * 2), pa, lds0 + L_A + b * 16384 + (8 * hw + j) * 1024);
#pragma unroll
        for (int j = 0; j < 4; ++j) {
          const unsigned vo = voB[j & 1] + (j >> 1) * (16 * 3 * D_ * 2);
          glds16(vo + 2048, pb, lds0 + L_KV + b * 16384 + (4 * hw + j) * 1024);
          glds16(vo + 4096, pb, lds0 + L_KV + b * 16384 + 8192 + (4 * hw + j) * 1024);
          glds16(vo, pb, lds0 + L_Q + b * 8192 + (4 * hw + j) * 1024);
        }
      };
      WAITV(10); BAR();
      stage(2); stage(3);
      WAITV(40); BAR();
      for (int t = 0; t < 12; ++t) { WAITV(20); BAR(); stage(t + 4); }
      WAITV(20); BAR();
      asm volatile("s_waitcnt vmcnt(0)" :: "v"(e0) : "memory");
      BAR();
    } else {
      const int aw = hw - 2, at = tid & 127;
      const bool master = bid == 0 && aw == 0;
      unsigned hs_cur = 0, hs_g0 = 0;
      u32x4 ms_exp = {0, 0, 0, 0}, ms_go = {0, 0, 0, 0}, pm = {0, 0, 0, 0};
      const unsigned p_off = opq(master ? SY_RE + lane * 16 : (lane == 1 ? SY_GO : SY_CH) + bid * 4);
      const char* wsu = uni(ws);
#define HS_PROBE() asm volatile("global_load_dwordx4 %0, %1, %2 sc1" : "+v"(pm) : "v"(p_off), "s"(wsu) : "memory")
#define HS_WAIT() asm volatile("s_waitcnt vmcnt(0)" : "+v"(pm) :: "memory")
      bool done = false;
      auto hs_check = [&]() __attribute__((always_inline)) {
        HS_WAIT();
        if (!master) {
          const unsigned c = __builtin_amdgcn_readlane(pm[0], 0), go = __builtin_amdgcn_readlane(pm[0], 1);
          if (go != hs_g0) done = true;
          else if (c != hs_cur) { hs_cur = c; st32(ws, SY_RE + bid * 4, c); }
        } else if (!done && __all(pm[0] == ms_exp[0] && pm[1] == ms_exp[1] && pm[2] == ms_exp[2] && pm[3] == ms_exp[3])) {
          STW(ms_go, SY_GO + lane * 16);
          done = true;
        }
      };
      const bool prober = master || aw == 1;
      const int kq = lane >> 4, r = lane & 15;
      constexpr unsigned OOB = 0x80000000u;
      const unsigned ko = WS_KVP + (hp * 16 + rb) * KVB + aw * 2176;
      const unsigned vm0 = opq(lane < 32 ? rs_o + aw * 512 + lane * 16 : lane < 56 ? cs_o + aw * 384 + (lane - 32) * 16 : ko + 2048 + (lane - 56) * 16);
      u32x4 e1;
      asm volatile("s_waitcnt vmcnt(0)\n\tv_mov_b32 %0, %4\n\tv_mov_b32 %1, %5\n\tv_mov_b32 %2, %6\n\tv_mov_b32 %3, %7"
                   : "=&v"(e1[0]), "=&v"(e1[1]), "=&v"(e1[2]), "=&v"(e1[3]) : "v"(e0[0]), "v"(e0[1]), "v"(e0[2]), "v"(e0[3]) : "memory");
      hs_g0 = __builtin_amdgcn_readfirstlane(e1[0]);
      u32x4 g0 = {0u, 0u, 0u, 0u};
      BAR();
      if (master) { ms_exp = e1 + 1u; STW(ms_exp, SY_CH + lane * 16); g0 = LDW(SY_GO + lane * 16); }
      u32x2 rst[4];
      {
        u32x4 a[4][2];
#pragma unroll
        for (int i = 0; i < 4; ++i)
#pragma unroll
          for (int k = 0; k < 2; ++k) a[i][k] = *(const u32x4*)(smem + L_A + (64 * aw + 16 * i + r) * 128 + (((4 * k + kq) ^ ((r >> 1) & 7)) * 16));
        const f32x4 Z4 = {0.f, 0.f, 0.f, 0.f};
        const int l3 = lane & 3;
#pragma unroll
        for (int i = 0; i < 4; ++i) {
          const f32x4 s1 = mfma16(ONES4, a[i][1], mfma16(ONES4, a[i][0], Z4)), s2 = mfma16(a[i][1], a[i][1], mfma16(a[i][0], a[i][0], Z4));
          const float q = l3 == 0 ? s2[0] : l3 == 1 ? s2[1] : l3 == 2 ? s2[2] : s2[3];
          rst[i] = (u32x2){min(__float_as_uint(s1[0]), 0xFFFFFFFEu), min(__float_as_uint(q), 0xFFFFFFFEu)};
          asm volatile("" : "+v"(rst[i]));
        }
      }
      const unsigned vm1 = opq(aw ? (lane < 2 ? SY_FD + cb * 512 + rp * 32 + lane * 16 : lane == 2 ? SY_FB + rb * 256 + hp * 16 : OOB)
                                  : (has_mlp ? WS_GU + fs * 1024 + lane * 16 : OOB));
      const unsigned vrs = opq((r >> 2) == kq ? rs_o + (64 * aw + r) * 8 : OOB);
      const unsigned vc0 = opq(cs_o + aw * 384 + lane * 4), vc1 = opq(lane < 32 ? cs_o + aw * 384 + 256 + lane * 4 : OOB);
      const unsigned sg_off = opq(aw ? WS_CS + hp * (16 * 768) + min(lane, 47) * 16 : WS_RS + rb * 16384 + lane * 16), sg_st = aw ? 768 : 1024;
      unsigned cb_[2][2];
#pragma unroll
      for (int c = 0; c < 2; ++c) {
        const unsigned o_ = lds0 + kq * 1024 + aw * 256 + (r >> 2) * 64 + ((c ^ (kq & 1)) * 32) + (lane & 3) * 8;
        cb_[0][c] = opq(o_ + L_Q + (tcs & 3) * 8192); cb_[1][c] = opq(o_ + L_KV + (tcs & 3) * 16384);
      }
      const int hq = (3 - (int)((size_t)wqkv >> 7) - (hp & ~3)) & 7;
      const int qln = ((((hp & 3) - hq - 1) & 3) * 8 + (idx & 7)) * 64 + (at >> 1);
      const unsigned vq = opq((unsigned)((((kph + 8) * 64 + qln / 3) & 1023) * 6144 + (qln % 3) * 2048 + ((hp & ~3) + (hq & 3)) * 128));
      const unsigned lw0 = opq(lds0 + L_CS + lane * 16), lw1 = opq(lds0 + L_WR + (lane & 7) * 16);
      const unsigned vcp = opq(min((unsigned)(idx * CODE_PER + min(at >> 3, CODE_PER - 1)) * 128u, clen));
      BAR();
      ms_go = g0 + 1u;
      STW(e4, vm0); STW(e4, vm1); STWS(e4, l16, ko); STWS(e4, l16, ko + 1024);
      BAR();
      STAMPT(2, 0, 384);
      if (aw == 1) HS_PROBE();
      HS_WAIT();
      BAR();
      STAMPT(2, 1, 384);
      if (aw == 1) { hs_cur = __builtin_amdgcn_readlane(pm[0], 0); st32(ws, SY_RE + bid * 4, hs_cur); }
#pragma unroll
      for (int i = 0; i < 4; ++i) __builtin_amdgcn_raw_buffer_store_b64(rst[i], wsr, vrs + i * 128, 0, 16);
      unsigned qt = 0;
      if (hq < 4 && (hp & 3) != hq) asm volatile("global_load_dword %0, %1, %2" : "=v"(qt) : "v"(vq), "s"(uni(wqkv)) : "memory");
      BAR();
      STAMPT(2, 2, 384);
      if (prober) HS_PROBE();
      unsigned cv0, cv1;
      {
        f32x4 cacc[6];
#pragma unroll
        for (int f = 0; f < 6; ++f) {
          cacc[f] = (f32x4){0.f, 0.f, 0.f, 0.f};
#pragma unroll
          for (int k = 0; k < 2; ++k) {
            const int o_ = (f >= 4 ? 8192 : 0) + k * 4096;
            cacc[f] = mfma16(ONES4, cat(trread(cb_[f >= 2][f & 1], o_), trread(cb_[f >= 2][f & 1], o_ + 512)), cacc[f]);
          }
        }
        cv0 = min(__float_as_uint(kq == 0 ? cacc[0][0] : kq == 1 ? cacc[1][0] : kq == 2 ? cacc[2][0] : cacc[3][0]), 0xFFFFFFFEu);
        cv1 = min(__float_as_uint(kq == 0 ? cacc[4][0] : cacc[5][0]), 0xFFFFFFFEu);
        asm volatile("" : "+v"(cv0), "+v"(cv1));
      }
      BAR();
      STAMPT(2, 3, 384);
      HS_WAIT();
      asm volatile("" :: "v"(qt));
      if (prober) hs_check();
      if (master) HS_PROBE();
      __builtin_amdgcn_raw_buffer_store_b32(cv0, wsr, vc0, 0, 16); __builtin_amdgcn_raw_buffer_store_b32(cv1, wsr, vc1, 0, 16);
      STWS(e4, l16, WS_ST2 + (rp * 16 + cb) * 2048 + aw * 1024);
      BAR();
      STAMPT(2, 4, 384);
      if (master) { hs_check(); HS_PROBE(); }
      BAR();
      STAMPT(2, 5, 384);
      if (master) { hs_check(); HS_PROBE(); }
      BAR();
      STAMPT(2, 6, 384);
      if (master) hs_check();
      if (prober) HS_PROBE();
      BAR();
      STAMPT(2, 7, 384);
      if (prober) {
        hs_check();
#pragma nounroll
        for (unsigned s_ = 0; s_ < SPIN_CAP && !done; ++s_) { HS_PROBE(); hs_check(); }
      }
      BAR();
      STAMPT(2, 8, 384);
      unsigned cpf;
      asm volatile("global_load_dword %0, %1, %2" : "=v"(cpf) : "v"(vcp), "s"(pc0) : "memory");
      u32x4 sg[16], wrv;
      asm volatile("global_load_dwordx4 %0, %1, %2" : "=v"(wrv) : "v"(l16 & 127u), "s"(uni(w_r + cb * 64)) : "memory");
#pragma unroll
      for (int i = 0; i < 16; ++i) {
        asm volatile("global_load_dwordx4 %0, %1, %2 sc1" : "=v"(sg[i]) : "v"(sg_off), "s"(wsu + i * sg_st) : "memory");
        if (i == SGS) { BAR(); STAMPT(2, 9, 384); }
      }
      auto tsum = [&](int o) __attribute__((always_inline)) {
        f32x4 t[4];
#pragma unroll
        for (int i = 0; i < 4; ++i) t[i] = add4(__builtin_bit_cast(f32x4, sg[o + i]), __builtin_bit_cast(f32x4, sg[o + 4 + i]));
        return add4(add4(t[0], t[2]), add4(t[1], t[3]));
      };
      WAITV(8);
#pragma unroll
      for (int i = 0; i < 8; ++i) asm volatile("" : "+v"(sg[i]));
      asm volatile("" : "+v"(wrv) : "v"(cpf));
      f32x4 sm = tsum(0), sn;
      asm volatile("" : "+v"(sm));
      BAR();
      STAMPT(2, 10, 384);
      WAITV(0);
#pragma unroll
      for (int i = 8; i < 16; ++i) asm volatile("" : "+v"(sg[i]));
      sn = tsum(8);
      asm volatile("" : "+v"(sn));
      BAR();
      STAMPT(2, 11, 384);
      sm = add4(sm, sn);

      const float chk = (sm[0] + sm[1]) + (sm[2] + sm[3]);
      if (__builtin_expect(__any(chk != chk), 0)) {
#pragma nounroll
        for (unsigned s_ = 0; s_ < SPIN_CAP; ++s_) {
          unsigned mx = 0;
#pragma unroll
          for (int i = 0; i < 16; ++i) { sg[i] = LDWS(sg_off, i * sg_st); mx = max(max(mx, max(sg[i][0], sg[i][1])), max(sg[i][2], sg[i][3])); }
          if (!__any(mx == EMPTY)) break;
          asm volatile("" ::: "memory");
        }
        sm = add4(tsum(0), tsum(8));
      }
      STAMPT(1, 1, 384);
      BAR();
      STAMPT(2, 12, 384);
      if (aw == 1) {
        *(LAS f32x4*)(size_t)lw0 = lane < 48 ? sm : (f32x4){0.f, 0.f, 0.f, 0.f};
        *(LAS u32x4*)(size_t)lw1 = wrv;
      } else {
#pragma unroll
        for (int e = 0; e < 2; ++e) {
          mean[e] = sm[2 * e] * (1.0f / 1024.0f);
          vr[e] = fmaxf(sm[2 * e + 1] * (1.0f / 1024.0f) - mean[e] * mean[e], 0.f);
          asm volatile("" : "+v"(mean[e]), "+v"(vr[e]));
        }
      }
      LBAR();
      STAMPT(2, 13, 384);
    }
    unsigned wt;
    asm volatile("global_load_dword %0, %1, %2" : "=v"(wt) : "v"(wto), "s"(uni(wo)) : "memory");
#pragma unroll
    for (int i = 0; i < THR - 1; ++i) wload(i);
    if (hw == 2) {
      f32x2 ra, rb_;
#pragma unroll
      for (int e = 0; e < 2; ++e) {
        ra[e] = __builtin_amdgcn_rcpf(rbf(__builtin_amdgcn_sqrtf(rbf(rbf(vr[e]) + 1e-5f))));
        rb_[e] = -rbf(mean[e]) * ra[e];
      }
      *(LAS f32x2*)(size_t)lsa = ra; *(LAS f32x2*)(size_t)(lsa + 512) = rb_;
      asm volatile("s_waitcnt lgkmcnt(0)" ::: "memory");
      misc[7] = 1;
    }
    STAMPT(1, 2, 384);
#pragma unroll
    for (int i = THR - 1; i < NW1; ++i) { WAITV(THR - 1); wload(i); }
    u32x4 wr_[20];
#define RLOAD(dst, vo, base) asm volatile("global_load_dwordx4 %0, %1, %2" : "=v"(dst) : "v"(vo), "s"(uni(base)) : "memory")
#pragma unroll
    for (int i = 0; i < NR; ++i) {
      WAITV(THR - 1);
      RLOAD(wr_[i], voW, uni(wo + cb * 64) + (size_t)((i + 12) ^ hrot) * (32 * D_ * 2));
      if (i % 6 == 0) STAMPT(3, 8 + i / 6, 256);
    }
#pragma nounroll
    for (unsigned s_ = 0; s_ < SPIN_CAP; ++s_) if (__all(misc[28 + (lane & 3)] != 0)) break;
#pragma unroll
    for (int j = 0; j < NE; ++j) { WAITV(THR - 1); stageW(j); }
    {
      u32x4 pv[16], zq;
      const unsigned ko = WS_KVP + hp * 16 * KVB + (hw >> 1) * 2176;
      unsigned kl = ko + (hw & 1) * 1024 + lane * 16, zo = ko + (lane & 15) * KVB + 2048 + (hw & 1) * 64 + (lane >> 4) * 16;
      u32x4 sel[2];
#pragma unroll
      for (int e = 0; e < 2; ++e)
#pragma unroll
        for (int i = 0; i < 4; ++i)
          sel[e][i] = ((lane & 15) >> 2) != (lane >> 4) || (i >> 1) != e || (i & 1) != ((lane & 3) >> 1) ? 0u : (lane & 1) ? 0x3F800000u : 0x3F80u;
      f32x4 s[2][2];
      const unsigned lz = opq(lds0 + L_ZL + (hw >> 1) * 64 + (lane >> 4) * 16 + (hw & 1) * 8), lk = opq(lds0 + L_KVL + hw * 1024 + lane * 16);
      auto ksum = [&]() __attribute__((always_inline)) {
        {
          float z[4];
#pragma unroll
          for (int i = 0; i < 4; ++i) { float v = __uint_as_float(zq[i]); v += dppf<DPP_X1>(v); v += dppf<DPP_X2>(v); v += dppf<DPP_HMIR>(v); z[i] = v + dppf<DPP_MIR>(v); }
          *(LAS u32x2*)(size_t)lz = (u32x2){pack2(z[0], z[1]), pack2(z[2], z[3])};
          SB0();
        }
#pragma unroll
        for (int p = 0; p < 16; ++p) {
#pragma unroll
          for (int e = 0; e < 2; ++e) s[p >> 3][e] = mfma16(sel[e], pv[p], (p & 7) ? s[p >> 3][e] : (f32x4){0.f, 0.f, 0.f, 0.f});
          SB0();
        }
        s[0][0] += s[1][0]; s[0][1] += s[1][1];
      };
      unsigned kl2 = kl + 4 * KVB;
      asm volatile("" : "+v"(sel[0]), "+v"(sel[1]), "+v"(kl), "+v"(kl2), "+v"(zo));
    WAITV(0);
#pragma unroll
    for (int i = 0; i < NR; ++i) asm volatile("" : "+v"(wr_[i]));
#pragma unroll
    for (int i = 0; i < NW1; ++i) wpin(i);
    asm volatile("" :: "v"(wt));
    STAMPT(1, 3, 256);
#pragma nounroll
      for (unsigned s_ = 0; s_ < SPIN_CAP; ++s_) { if (__builtin_amdgcn_readfirstlane(misc[16 + hw]) == 2) break; __builtin_amdgcn_s_sleep(1); }
#pragma nounroll
      for (unsigned s_ = 0; s_ < SPIN_CAP; ++s_) {
        zq = LDW(zo);
#pragma unroll
        for (int p = 0; p < 16; ++p) { pv[p] = LDWS((p & 4) ? kl2 : kl, ((p >> 3) ^ (idx & 1)) * (8 * KVB) + (p & 3) * KVB); SB0(); }
        ksum();
        const float chk = s[0][0][0] + __uint_as_float(zq[1]) + __uint_as_float(zq[3]);
        if (__builtin_expect(!__any(chk != chk), 1)) break;
        unsigned mk = max(zq[1], zq[3]);
#pragma unroll
        for (int p = 0; p < 16; ++p) mk = max(max(mk, pv[p][1]), pv[p][3]);
        if (!__any(mk == EMPTY)) break;
#pragma nounroll
        for (; s_ < SPIN_CAP; ++s_)
          if (__all(ld32(ws, ko + (lane & 15) * KVB + ((lane & 16) ? 2048 + 124 : (hw & 1) * 1024 + 1020)) != EMPTY)) break;
        asm volatile("" ::: "memory");
      }
      STAMPT(1, 4, 256);
      *(LAS u32x4*)(size_t)lk = (u32x4){pack2(s[0][0][0], s[0][0][1]), pack2(s[0][0][2], s[0][0][3]), pack2(s[0][1][0], s[0][1][1]), pack2(s[0][1][2], s[0][1][3])};
    }
    LBAR();
#pragma unroll
    for (int j = NE; j < 32; ++j) if (j < 12 || j >= NR + 12) { WAITV(THR3 - 1); stageW(j); }
#pragma nounroll
    for (unsigned s_ = 0; s_ < SPIN_CAP; ++s_) { if (__all(misc[24 + (lane & 3)] != 0)) break; __builtin_amdgcn_s_sleep(1); }
#pragma unroll
    for (int i = 0; i < NR; ++i) *(u32x4*)(smem + wslot(i + 12) + hw * 1024 + lane * 16) = wr_[i];
    asm volatile("s_waitcnt lgkmcnt(0)" ::: "memory");
    {
      const u32x4 e5 = {EMPTY, EMPTY, EMPTY, EMPTY};
      if (has_mlp && hw >= 2) STW(e5, WS_YP + (fs >> 2) * 8192 + (hw & 1) * 4096 + (fs & 3) * 1024 + lane * 16);
#pragma unroll
      for (int rr_ = 0; rr_ < XSR; rr_ += 2)
        if (rr_ + (hw >> 1) < XSR) STW(e5, WS_XS + (rr_ + (hw >> 1)) * 524288 + (rp * 16 + ((mt >> 3) & 15)) * 2048 + cb * 128 + (mt & 7) * 16);
    }
    WAITV(0);
    STAMPT(1, 6, 256);
    if (hw == 0) {
      __builtin_amdgcn_s_sleep(SLB);
#pragma nounroll
      for (unsigned s_ = 0; s_ < SPIN_CAP; ++s_) { if (__all(ld32(ws, SY_FB + rp * 256 + lane * 4) != EMPTY)) break; __builtin_amdgcn_s_sleep(SLB1); }
    }
    BAR();
#pragma nounroll
    for (unsigned s_ = 0; s_ < SPIN_CAP; ++s_) { if (__builtin_amdgcn_readfirstlane(misc[20 + hw])) break; __builtin_amdgcn_s_sleep(2); }
    unsigned cp3;
    asm volatile("global_load_dword %0, %1, %2" : "=v"(cp3) : "v"(min((unsigned)(idx * CODE_PER + min(mt >> 4, CODE_PER - 1)) * 128u, clen)), "s"(pc0) : "memory");
#pragma unroll
    for (int i = NW1; i < 24; ++i) { WAITV(THR2 - 1); wload(i); }
    WAITV(0);
    asm volatile("" :: "v"(cp3));
#pragma unroll
    for (int i = NW1; i < 24; ++i) wpin(i);
    STAMPT(1, 7, 256);
    const int c16 = lane & 15, kq = lane >> 4;
    u32x4 gf[2][8];
    u32x4 df[4][2];
    {
#pragma nounroll
      for (unsigned s_ = 0; s_ < SPIN_CAP; ++s_) { if (__all(misc[8 + (lane & 3)] != 0)) break; __builtin_amdgcn_s_sleep(1); }
      STAMPT(3, 5, 256);
#pragma unroll
      for (int i = 0; i < 8; ++i) {
        const int go = hw * 8192 + i * 1024 + ((lane ^ (((lane >> 4) & 1) * 2 + (i & 1) * 4)) * 16);
        *(u32x4*)(smem + go) = wgr[i];
        *(u32x4*)(smem + 32768 + go) = wur[i];
        *(u32x4*)(smem + L_DW + hw * 8192 + i * 1024 + ((lane ^ (((lane >> 4) & 3) * 2)) * 16)) = wdr[i];
      }
      asm volatile("s_waitcnt lgkmcnt(0)" ::: "memory");
      STAMPT(3, 6, 256);
      misc[12 + hw] = 1;
#pragma nounroll
      for (unsigned s_ = 0; s_ < SPIN_CAP; ++s_) if (__all(misc[12 + (lane & 3)] != 0)) break;
      asm volatile("" ::: "memory");
      STAMPT(3, 7, 256);
      const unsigned gb = opq(lds0 + (kq * 8 + (c16 >> 2)) * 128 + (hw ^ ((c16 >> 3) + (kq & 1) * 2)) * 32 + (lane & 3) * 8);
      const unsigned db = lds0 + L_DW + hw * 8192 + (kq * 4 + (c16 >> 2)) * 128 + (lane & 3) * 8, dsw = (c16 >> 3) + (kq & 1) * 2;
#pragma unroll
      for (int m = 0; m < 2; ++m)
#pragma unroll
        for (int k = 0; k < 8; ++k) gf[m][k] = cat(trread(gb, m * 32768 + k * 4096), trread(gb, m * 32768 + k * 4096 + 512));
#pragma unroll
      for (int t = 0; t < 4; ++t) {
        const unsigned dt = opq(db + (t ^ dsw) * 32);
#pragma unroll
        for (int e = 0; e < 2; ++e) df[t][e] = cat(trread(dt, ((t - 2 * e) & 3) * 2048), trread(dt, ((t - 2 * e - 1) & 3) * 2048));
      }
      SB0();
#pragma unroll
      for (int k = 0; k < 8; ++k) asm volatile("" : "+v"(gf[0][k]), "+v"(gf[1][k]), "+v"(df[k >> 1][k & 1]));
    }
    const unsigned gu_st = opq(c16 < 4 ? WS_GU + fs * 1024 + hw * 256 + c16 * 64 + kq * 16 : 0x80000000u);
    const unsigned gu_ld = opq(WS_GU + (fs & ~3) * 1024 + c16 * 64 + kq * 16);
    const unsigned yp_st = opq((c16 & 2) ? 0x80000000u : WS_YP + (fs >> 2) * 8192 + (c16 & 1) * 4096 + (fs & 3) * 1024 + hw * 256 + (c16 >> 2) * 64 + kq * 16);
    const unsigned xsb = opq(lds0 + L_XS + (c16 & 1) * 528 + kq * 16);
    STAMPT(1, 8, 256);
    BAR();
    if (has_mlp) {
      f32x4 ga[2] = {{0.f, 0.f, 0.f, 0.f}, {0.f, 0.f, 0.f, 0.f}};
      u32x4 xf[8];
#pragma unroll
      for (int k = 0; k < 8; ++k) xf[k] = ldsr(xsb, k * 64);
#pragma unroll
      for (int k = 0; k < 8; ++k) { ga[0] = mfma16(gf[0][k], xf[k], ga[0]); ga[1] = mfma16(gf[1][k], xf[k], ga[1]); }
      STW(seal(__builtin_bit_cast(u32x4, (c16 & 2) ? ga[1] : ga[0])), gu_st);
      STAMPT(1, 14, 256);
      __builtin_amdgcn_s_sleep(SLG0);
      u32x4 q[4];
#pragma nounroll
      for (unsigned s_ = 0; s_ < SPIN_CAP; ++s_) {
        unsigned mk = 0u;
#pragma unroll
        for (int p = 0; p < 4; ++p) q[p] = LDW(gu_ld + p * 1024);
#pragma unroll
        for (int p = 0; p < 4; ++p) mk = max(max(mk, q[p][1]), q[p][3]);
        if (!__any(mk == EMPTY)) break;
        asm volatile("" ::: "memory");
        __builtin_amdgcn_s_sleep(SLG1);
      }
      STAMPT(1, 15, 256);
      const f32x4 tot = (__builtin_bit_cast(f32x4, q[0]) + __builtin_bit_cast(f32x4, q[1])) + (__builtin_bit_cast(f32x4, q[2]) + __builtin_bit_cast(f32x4, q[3]));
      float av[4];
#pragma unroll
      for (int i = 0; i < 4; ++i) {
        const float v = rbf(tot[i]), o_ = dppf<DPP_X2>(v);
        const float gs = (c16 & 2) ? o_ : v, us = (c16 & 2) ? v : o_;
        av[i] = rbf(gs * __builtin_amdgcn_rcpf(1.0f + __builtin_amdgcn_exp2f(gs * -1.44269504088896340736f))) * us;
      }
      const unsigned a0 = pack2(av[0], av[1]), a1 = pack2(av[2], av[3]);
      const u32x4 b0 = {a0, a1, dppu<DPP_ROR4>(a0), dppu<DPP_ROR4>(a1)}, b1 = {dppu<DPP_ROR8>(a0), dppu<DPP_ROR8>(a1), dppu<DPP_ROR12>(a0), dppu<DPP_ROR12>(a1)};
      f32x4 y[4];
#pragma unroll
      for (int t = 0; t < 4; ++t) y[t] = mfma16(df[t][0], b0, (f32x4){0.f, 0.f, 0.f, 0.f});
#pragma unroll
      for (int t = 0; t < 4; ++t) y[t] = mfma16(df[t][1], b1, y[t]);
      const int T = c16 >> 2;
      STW(seal(__builtin_bit_cast(u32x4, T == 0 ? y[0] : T == 1 ? y[1] : T == 2 ? y[2] : y[3])), yp_st);
      STAMPT(0, 13, 256);
      misc[0] = 1;
    }
  }
}

extern "C" void kernel_launch(void* const* d_in, const int* in_sizes, int n_in, void* d_out, int out_size,
                              void* d_ws, size_t ws_size, hipStream_t stream) {
  (void)hipFuncSetAttribute((const void*)fused, hipFuncAttributeMaxDynamicSharedMemorySize, LDS_TOTAL);
  fused<<<256, 512, LDS_TOTAL, stream>>>((const u16*)d_in[0], (const u16*)d_in[1], (const u16*)d_in[2], (const u16*)d_in[4],
                                         (const u16*)d_in[5], (const u16*)d_in[6], (const u16*)d_in[7], (u16*)d_out, (char*)d_ws);
}
```
